```python
import torch

def _softmax(x, axis=-1):
    return torch.softmax(x, dim=axis)
def _gelu(x):
    return torch.nn.functional.gelu(x, approximate="tanh")
def _silu(x):
    return torch.nn.functional.silu(x)
def _sigmoid(x):
    return torch.sigmoid(x)
def _layernorm(x, axis=-1, eps=1e-5):
    m = x.mean(dim=axis, keepdim=True)
    v = x.var(dim=axis, keepdim=True, unbiased=False)
    return (x - m) / torch.sqrt(v + eps)
def _scatter_add(tgt, idx, src, axis=0):
    return tgt.clone().index_add_(axis, idx, src)
def _segment_sum(src, seg, n):
    out = torch.zeros((n,) + src.shape[1:], dtype=src.dtype)
    return out.index_add_(0, seg, src)
def _pad(x, pw):
    flat = tuple(v for pair in reversed(pw) for v in pair)
    return torch.nn.functional.pad(x, flat)

kernel_name = 'parallel_sum__sliding_local_dilated__glu_gated_glu__prompt'

def setup_inputs():

    _w = (lambda a: (lambda t: t.to(torch.bfloat16) if t.is_floating_point() else t)(torch.as_tensor(a)))
    def _A_setup():
        B, S, D = 1, 2048, 1024
        return {
            "x": _w((lambda *s: torch.randn(*s, dtype=torch.bfloat16) * 0.05)(B, S, D)),
            "wqkv": _w((lambda *s: torch.randn(*s, dtype=torch.bfloat16) * 0.05)(D, 3 * D)),
            "wo": _w((lambda *s: torch.randn(*s, dtype=torch.bfloat16) * 0.05)(D, D)),
        }
    _A_in = _A_setup()

    def _B_setup():
        B, S, D, F = 1, 2048, 1024, 3584
        return {
            "x": _w((lambda *s: torch.randn(*s, dtype=torch.bfloat16) * 0.05)(B, S, D)),
            "w_gate": _w((lambda *s: torch.randn(*s, dtype=torch.bfloat16) * 0.05)(D, F)),
            "w_up": _w((lambda *s: torch.randn(*s, dtype=torch.bfloat16) * 0.05)(D, F)),
            "w_down": _w((lambda *s: torch.randn(*s, dtype=torch.bfloat16) * 0.05)(F, D)),
        }
    _B_in = _B_setup()

    return {
        "A_x": _A_in["x"],
        "A_wqkv": _A_in["wqkv"],
        "A_wo": _A_in["wo"],
        "B_x": _B_in["x"],
        "B_w_gate": _B_in["w_gate"],
        "B_w_up": _B_in["w_up"],
        "B_w_down": _B_in["w_down"],
    }

def reference(A_x, A_wqkv, A_wo, B_x, B_w_gate, B_w_up, B_w_down):
    def _A_ref(x, wqkv, wo):
        B, S, D = x.shape
        H = 32; Dh = D // H
        W, dil = 8, 2
        qkv = (lambda x, *d: x.permute(*d))((x @ wqkv).reshape(B, S, 3, H, Dh), 2, 0, 3, 1, 4)
        q, k, v = qkv[0], qkv[1], qkv[2]
        scale = Dh ** -0.5
        pos = torch.arange(S)
        off = torch.arange(W) * dil
        idx = pos[:, None] - off[None, :]
        mask = idx >= 0
        idx_c = torch.where(mask, idx, 0).reshape(-1)
        kg = k[:, :, idx_c, :].reshape(B, H, S, W, Dh)
        vg = v[:, :, idx_c, :].reshape(B, H, S, W, Dh)
        logits = torch.einsum("bhsd,bhswd->bhsw", q, kg) * scale
        logits = torch.where(mask[None, None, :, :], logits, -1e9)
        attn = _softmax(logits, axis=-1)
        o = torch.einsum("bhsw,bhswd->bhsd", attn, vg)
        return (lambda x, *d: x.permute(*d))(o, 0, 2, 1, 3).reshape(B, S, D) @ wo

    def _B_ref(x, w_gate, w_up, w_down):
        g = torch.einsum("bsd,df->bsf", x, w_gate)
        u = torch.einsum("bsd,df->bsf", x, w_up)
        h = _sigmoid(g) * u
        return torch.einsum("bsf,fd->bsd", h, w_down)

    x = A_x
    xn = _layernorm(x, axis=-1)
    return x + _A_ref(xn, A_wqkv, A_wo) + _B_ref(xn, B_w_gate, B_w_up, B_w_down)
```

```cpp
#include <hip/hip_runtime.h>
#include <hip/hip_bf16.h>
#include <stdint.h>

typedef __bf16 bf16x8 __attribute__((ext_vector_type(8)));
typedef __bf16 bf16x4 __attribute__((ext_vector_type(4)));
typedef __bf16 bf16x2 __attribute__((ext_vector_type(2)));
typedef float f32x4 __attribute__((ext_vector_type(4)));
typedef float f32x2 __attribute__((ext_vector_type(2)));
typedef unsigned u32x4 __attribute__((ext_vector_type(4)));
typedef unsigned u32x2 __attribute__((ext_vector_type(2)));
typedef unsigned short u16;
#define LAS __attribute__((address_space(3)))
#define GAS __attribute__((address_space(1)))
#define DEVI __device__ __forceinline__

constexpr int S_ = 2048, D_ = 1024, F_ = 3584;
constexpr int MB_ = S_ / 16;
constexpr size_t SLAB_B = (size_t)MB_ * 1024;
constexpr size_t WS_XN = 0;
constexpr size_t WS_HO = 32 * SLAB_B;

DEVI unsigned pk_bf16(float lo, float hi) {
    bf16x2 v = {(__bf16)lo, (__bf16)hi};
    return __builtin_bit_cast(unsigned, v);
}
DEVI float bf_lo(unsigned u) { return __uint_as_float(u << 16); }
DEVI float bf_hi(unsigned u) { return __uint_as_float(u & 0xffff0000u); }
DEVI float rbf(float f) { return (float)(__bf16)f; }
DEVI bf16x8 as_frag(u32x4 v) { return __builtin_bit_cast(bf16x8, v); }
#define MFMA16(a, b, c) __builtin_amdgcn_mfma_f32_16x16x32_bf16((a), (b), (c), 0, 0, 0)
#define SBAR() __builtin_amdgcn_sched_barrier(0)
#define BAR() __builtin_amdgcn_s_barrier()

DEVI bf16x8 tr_frag(LAS const char* p, int off0, int off1) {
    bf16x4 lo = __builtin_amdgcn_ds_read_tr16_b64_v4bf16((LAS bf16x4*)(p + off0));
    bf16x4 hi = __builtin_amdgcn_ds_read_tr16_b64_v4bf16((LAS bf16x4*)(p + off1));
    return __builtin_shufflevector(lo, hi, 0, 1, 2, 3, 4, 5, 6, 7);
}

DEVI void glds16(const char* sbase, unsigned voff, unsigned ldsdst) {
    unsigned keep;
    asm volatile("s_mov_b32 %0, m0\n\ts_mov_b32 m0, %3\n\ts_nop 0\n\tglobal_load_lds_dwordx4 %1, %2\n\ts_mov_b32 m0, %0"
                 : "=&s"(keep) : "v"(voff), "s"(sbase), "s"(ldsdst) : "memory");
}

__global__ __launch_bounds__(512) void k_ln(const u16* __restrict__ x, char* __restrict__ ws) {
    __shared__ float red[2][8][8];
    const int tid = threadIdx.x, w = tid >> 6, l = tid & 63, r = l & 7, c8 = l >> 3;
    const int row = blockIdx.x * 8 + r;
    u32x4 v[2];
#pragma unroll
    for (int i = 0; i < 2; ++i) v[i] = *(const u32x4*)(x + (size_t)row * D_ + (16 * w + 8 * i + c8) * 8);
    float f[16];
#pragma unroll
    for (int i = 0; i < 2; ++i)
#pragma unroll
        for (int j = 0; j < 4; ++j) { f[i * 8 + 2 * j] = bf_lo(v[i][j]); f[i * 8 + 2 * j + 1] = bf_hi(v[i][j]); }
    float s = 0.f;
#pragma unroll
    for (int j = 0; j < 16; ++j) s += f[j];
    s += __shfl_xor(s, 8); s += __shfl_xor(s, 16); s += __shfl_xor(s, 32);
    if (c8 == 0) red[0][w][r] = s;
    __syncthreads();
    float tot = 0.f;
#pragma unroll
    for (int k = 0; k < 8; ++k) tot += red[0][k][r];
    const float mean = tot * (1.0f / D_);
    float q = 0.f;
#pragma unroll
    for (int j = 0; j < 16; ++j) { const float d = f[j] - mean; q += d * d; }
    q += __shfl_xor(q, 8); q += __shfl_xor(q, 16); q += __shfl_xor(q, 32);
    if (c8 == 0) red[1][w][r] = q;
    __syncthreads();
    float qt = 0.f;
#pragma unroll
    for (int k = 0; k < 8; ++k) qt += red[1][k][r];
    const float mb = rbf(mean);
    const float vb = rbf(qt * (1.0f / D_));
    const float den = rbf(sqrtf(rbf(vb + 1e-5f)));
    const float rc = 1.0f / den;
    const int mblk = row >> 4, rr = row & 15;
#pragma unroll
    for (int i = 0; i < 2; ++i) {
        u32x4 o;
#pragma unroll
        for (int j = 0; j < 4; ++j) {
            float y[2];
#pragma unroll
            for (int e = 0; e < 2; ++e) {
                const float d = rbf(f[i * 8 + 2 * j + e] - mb);
                float qq = d * rc;
                qq = fmaf(fmaf(-qq, den, d), rc, qq);
                y[e] = qq;
            }
            o[j] = pk_bf16(y[0], y[1]);
        }
        const int ch = 16 * w + 8 * i + c8, slab = ch >> 2, g = ch & 3;
        *(u32x4*)(ws + WS_XN + slab * SLAB_B + (size_t)mblk * 1024 + (g * 16 + rr) * 16) = o;
    }
}

constexpr int G1_AST = 17 * 1024;
constexpr int G1_BST = 21 * 1024;
constexpr int G1_REG = 7168;
constexpr int G1_BOFF = 4 * G1_AST;
constexpr int G1_XOFF = G1_BOFF + 4 * G1_BST;
constexpr int G1_LDS = G1_XOFF + 8192;

struct G1Ld {
    const char* base[5];
    unsigned step[5];
    unsigned voff[5];
    unsigned lds0[5], ldss[5];
    unsigned cnt;
};

template <int SLOT, bool WAIT>
DEVI void g1_issue(G1Ld& L, unsigned cnt) {
    unsigned keep;
    asm volatile(
        "s_mov_b32 %0, m0\n\t"
        "s_cmp_eq_u32 %16, 0\n\t"
        "s_cbranch_scc1 .Lg1_none_%=\n\t"
        "s_mov_b32 m0, %11\n\ts_nop 0\n\tglobal_load_lds_dwordx4 %1, %6\n\t"
        "s_mov_b32 m0, %12\n\ts_nop 0\n\tglobal_load_lds_dwordx4 %2, %7\n\t"
        "s_mov_b32 m0, %13\n\ts_nop 0\n\tglobal_load_lds_dwordx4 %3, %8\n\t"
        "s_mov_b32 m0, %14\n\ts_cmp_eq_u32 %16, 4\n\tglobal_load_lds_dwordx4 %4, %9\n\t"
        "s_cbranch_scc1 .Lg1_four_%=\n\t"
        "s_mov_b32 m0, %15\n\ts_nop 0\n\tglobal_load_lds_dwordx4 %5, %10\n"
        ".Lg1_four_%=:\n\t"
        ".if %17\n\ts_waitcnt vmcnt(8)\n\t.endif\n\t"
        "s_branch .Lg1_done_%=\n"
        ".Lg1_none_%=:\n\t"
        "s_waitcnt vmcnt(0)\n"
        ".Lg1_done_%=:\n\t"
        "s_mov_b32 m0, %0"
        : "=&s"(keep)
        : "v"(L.voff[0]), "v"(L.voff[1]), "v"(L.voff[2]), "v"(L.voff[3]), "v"(L.voff[4]),
          "s"(L.base[0]), "s"(L.base[1]), "s"(L.base[2]), "s"(L.base[3]), "s"(L.base[4]),
          "s"(L.lds0[0] + SLOT * L.ldss[0]), "s"(L.lds0[1] + SLOT * L.ldss[1]), "s"(L.lds0[2] + SLOT * L.ldss[2]),
          "s"(L.lds0[3] + SLOT * L.ldss[3]), "s"(L.lds0[4] + SLOT * L.ldss[4]), "s"(cnt), "i"(WAIT ? 1 : 0)
        : "memory", "scc");
#pragma unroll
    for (int i = 0; i < 5; ++i) L.base[i] += L.step[i];
}

#define WAIT_VM_LGKM0(n) __builtin_amdgcn_s_waitcnt(((n) & 15) | 0x70 | (((n) >> 4) << 14))
#define WAIT_VM(n) __builtin_amdgcn_s_waitcnt(((n) & 15) | 0x70 | 0xF00 | (((n) >> 4) << 14))
#define WAIT_LGKM0() __builtin_amdgcn_s_waitcnt(0xC07F)
#define MEMFENCE() asm volatile("" ::: "memory")

template <int GRP, int ST>
DEVI void g1_stage(f32x4 (&acc)[4][10], f32x4& acch, G1Ld& L, unsigned cnt, LAS const char* pA, LAS const char* pH,
                   LAS const char* pB01, LAS const char* pB23, LAS const char* pBh01, LAS const char* pBh23) {
    bf16x8 A[4], B[10], Ah, Bh;
    LAS const char* pB = (ST < 2) ? pB01 : pB23;
    LAS const char* pBh = (ST < 2) ? pBh01 : pBh23;
    constexpr int bo = (ST & 1) * G1_BST;
#pragma unroll
    for (int m = 0; m < 4; ++m) A[m] = *(LAS const bf16x8*)(pA + ST * G1_AST + (1 + m) * 1024);
    if (GRP == 0) {
#pragma unroll
        for (int n = 0; n < 6; ++n) B[n] = tr_frag(pB, bo + n * 32, bo + n * 32 + 8 * 224);
#pragma unroll
        for (int n = 0; n < 2; ++n) {
            B[6 + n] = tr_frag(pB, bo + G1_REG + n * 32, bo + G1_REG + n * 32 + 8 * 224);
            B[8 + n] = tr_frag(pB, bo + 2 * G1_REG + n * 32, bo + 2 * G1_REG + n * 32 + 8 * 224);
        }
        Ah = *(LAS const bf16x8*)(pH + ST * G1_AST);
        Bh = tr_frag(pBh, bo, bo + 8 * 224);
    } else {
#pragma unroll
        for (int n = 0; n < 5; ++n) {
            B[n] = tr_frag(pB, bo + G1_REG + (2 + n) * 32, bo + G1_REG + (2 + n) * 32 + 8 * 224);
            B[5 + n] = tr_frag(pB, bo + 2 * G1_REG + (2 + n) * 32, bo + 2 * G1_REG + (2 + n) * 32 + 8 * 224);
        }
    }
    g1_issue<(ST + 3) & 3, true>(L, cnt);
    WAIT_LGKM0();
    MEMFENCE();
    SBAR();
    BAR();
    SBAR();
    __builtin_amdgcn_s_setprio(1);
#pragma unroll
    for (int n = 0; n < 10; ++n)
#pragma unroll
        for (int m = 0; m < 4; ++m) {
            if (GRP == 0 && (n == 4 || n == 5)) acc[m][n] = MFMA16(A[m], B[n], acc[m][n]);
            else acc[m][n] = MFMA16(B[n], A[m], acc[m][n]);
        }
    if (GRP == 0) acch = MFMA16(Bh, Ah, acch);
    __builtin_amdgcn_s_setprio(0);
    SBAR();
    BAR();
    MEMFENCE();
    SBAR();
}

DEVI float glu1(float g, float u) {
    const float e = __builtin_amdgcn_exp2f(g * -1.4426950408889634f);
    return u * __builtin_amdgcn_rcpf(1.0f + e);
}

DEVI void glu_store(const f32x4& g0, const f32x4& u0, const f32x4& g1, const f32x4& u1, char* dst) {
    unsigned a0 = pk_bf16(glu1(g0[0], u0[0]), glu1(g0[1], u0[1])), a1 = pk_bf16(glu1(g0[2], u0[2]), glu1(g0[3], u0[3]));
    unsigned b0 = pk_bf16(glu1(g1[0], u1[0]), glu1(g1[1], u1[1])), b1 = pk_bf16(glu1(g1[2], u1[2]), glu1(g1[3], u1[3]));
    auto r0 = __builtin_amdgcn_permlane16_swap(a0, b0, false, false);
    auto r1 = __builtin_amdgcn_permlane16_swap(a1, b1, false, false);
    u32x4 o = {r0[0], r1[0], r0[1], r1[1]};
    *(u32x4*)dst = o;
}

template <int GRP>
DEVI void g1_body(G1Ld& L, LAS char* lds, char* ws, int rt, int ct, int wm, int lane) {
    const int g = lane >> 4, i16 = lane & 15, q = i16 >> 2, p = lane & 3;
    LAS const char* pA = lds + wm * 4096 + lane * 16;
    LAS const char* pH = lds + lane * 16;
    const int brow = (16 * (g >> 1) + 4 * (g & 1) + q) * 224 + p * 8;
    LAS const char* pB01 = lds + G1_BOFF + brow;
    LAS const char* pB23 = pB01 + 2 * G1_BST;
    LAS const char* pBh01 = pB01 + (2 + wm) * 32;
    LAS const char* pBh23 = pB23 + (2 + wm) * 32;
    f32x4 acc[4][10], acch = {0.f, 0.f, 0.f, 0.f};
#pragma unroll
    for (int m = 0; m < 4; ++m)
#pragma unroll
        for (int n = 0; n < 10; ++n) acc[m][n] = (f32x4){0.f, 0.f, 0.f, 0.f};

    g1_issue<0, false>(L, L.cnt); g1_issue<1, false>(L, L.cnt); g1_issue<2, true>(L, L.cnt);
    SBAR(); BAR(); SBAR();
    if (GRP == 1) { BAR(); SBAR(); }
    MEMFENCE();
#pragma unroll 1
    for (int it = 0; it < 8; ++it) {
        const unsigned cnt = it < 7 ? L.cnt : 0u;
        g1_stage<GRP, 0>(acc, acch, L, L.cnt, pA, pH, pB01, pB23, pBh01, pBh23);
        g1_stage<GRP, 1>(acc, acch, L, cnt, pA, pH, pB01, pB23, pBh01, pBh23);
        g1_stage<GRP, 2>(acc, acch, L, cnt, pA, pH, pB01, pB23, pBh01, pBh23);
        g1_stage<GRP, 3>(acc, acch, L, cnt, pA, pH, pB01, pB23, pBh01, pBh23);
    }

    char* ho = ws + WS_HO;
    const int mb0 = 16 * rt + 4 * wm;
    const unsigned hl = (unsigned)((g & 1) * 1024 + (g >> 1) * 256 + i16 * 16);
    if (GRP == 1) {
#pragma unroll
        for (int n = 0; n < 5; ++n) {
            const int cb = 7 * ct + 2 + n;
            char* dst = ho + (size_t)(32 + (cb >> 1)) * SLAB_B + (cb & 1) * 512 + (size_t)mb0 * 1024 + hl;
#pragma unroll
            for (int m = 0; m < 4; m += 2) glu_store(acc[m][n], acc[m][5 + n], acc[m + 1][n], acc[m + 1][5 + n], dst + m * 1024);
        }
    } else {
        u32x4 Qf[4], Kf[4]; u32x2 Vh[4][2];
#pragma unroll
        for (int m = 0; m < 4; ++m) {
            Qf[m] = (u32x4){pk_bf16(acc[m][0][0], acc[m][0][1]), pk_bf16(acc[m][0][2], acc[m][0][3]), pk_bf16(acc[m][1][0], acc[m][1][1]), pk_bf16(acc[m][1][2], acc[m][1][3])};
            Kf[m] = (u32x4){pk_bf16(acc[m][2][0], acc[m][2][1]), pk_bf16(acc[m][2][2], acc[m][2][3]), pk_bf16(acc[m][3][0], acc[m][3][1]), pk_bf16(acc[m][3][2], acc[m][3][3])};
#pragma unroll
            for (int b = 0; b < 2; ++b) Vh[m][b] = (u32x2){pk_bf16(acc[m][4 + b][0], acc[m][4 + b][1]), pk_bf16(acc[m][4 + b][2], acc[m][4 + b][3])};
        }
        LAS char* xs = lds + G1_XOFF;
        if (wm < 3) {
            *(LAS u32x4*)(xs + (wm + 1) * 2048 + lane * 16) = Kf[3];
            *(LAS u32x4*)(xs + (wm + 1) * 2048 + 1024 + lane * 16) = (u32x4){Vh[3][0][0], Vh[3][0][1], Vh[3][1][0], Vh[3][1][1]};
        }
        {
            const unsigned h0 = pk_bf16(acch[0], acch[1]), h1 = pk_bf16(acch[2], acch[3]);
            if (wm < 2) {
                *(LAS u32x2*)(xs + lane * 16 + wm * 8) = (u32x2){h0, h1};
            } else {
                LAS char* t = xs + 1024 + ((i16 >> 2) * 16 + 4 * g) * 16 + (wm - 2) * 8 + (i16 & 3) * 2;
                *(LAS u16*)(t) = (u16)h0; *(LAS u16*)(t + 16) = (u16)(h0 >> 16); *(LAS u16*)(t + 32) = (u16)h1; *(LAS u16*)(t + 48) = (u16)(h1 >> 16);
            }
        }
#pragma unroll
        for (int n = 0; n < 2; ++n) {
            const int cb = 7 * ct + n;
            char* dst = ho + (size_t)(32 + (cb >> 1)) * SLAB_B + (cb & 1) * 512 + (size_t)mb0 * 1024 + hl;
#pragma unroll
            for (int m = 0; m < 4; m += 2) glu_store(acc[m][6 + n], acc[m][8 + n], acc[m + 1][6 + n], acc[m + 1][8 + n], dst + m * 1024);
        }
        asm volatile("s_waitcnt lgkmcnt(0)" ::: "memory");
        SBAR(); BAR(); SBAR();
        u32x4 Kp = *(LAS const u32x4*)(xs + wm * 2048 + lane * 16);
        u32x4 Vp = *(LAS const u32x4*)(xs + wm * 2048 + 1024 + lane * 16);
        const bool odd = (i16 & 1) != 0;
        const int par = i16 & 1;
        const int dp0 = 16 + i16 - 4 * g - par, dc0 = i16 - 4 * g - par;
        const bool vp0 = dp0 <= 14, vp1 = dp0 - 2 <= 14, vc0 = dc0 >= 0, vc1 = dc0 - 2 >= 0;
        const float NEG = -__builtin_inff();
        const float sc = 0.17677669529663687f * 1.4426950408889634f;
        const bool first = (rt == 0) && (wm == 0);
        char* od = ho + (size_t)ct * SLAB_B + (size_t)mb0 * 1024 + (((g & 1) << 1) | (g >> 1)) * 256 + i16 * 16;
#pragma unroll
        for (int m = 0; m < 4; ++m) {
            const f32x4 z = {0.f, 0.f, 0.f, 0.f};
            f32x4 s0 = MFMA16(as_frag(Kp), as_frag(Qf[m]), z);
            f32x4 s1 = MFMA16(as_frag(Kf[m]), as_frag(Qf[m]), z);
            float a0 = (odd ? s0[1] : s0[0]) * sc, a1 = (odd ? s0[3] : s0[2]) * sc;
            float b0 = (odd ? s1[1] : s1[0]) * sc, b1 = (odd ? s1[3] : s1[2]) * sc;
            if (m == 0 && first) { a0 = -1e9f * 1.4426950408889634f; a1 = a0; }
            a0 = vp0 ? a0 : NEG; a1 = vp1 ? a1 : NEG; b0 = vc0 ? b0 : NEG; b1 = vc1 ? b1 : NEG;
            float mx = fmaxf(fmaxf(a0, a1), fmaxf(b0, b1));
            { auto t = __builtin_amdgcn_permlane16_swap(__float_as_uint(mx), __float_as_uint(mx), false, false); mx = fmaxf(__uint_as_float(t[0]), __uint_as_float(t[1])); }
            { auto t = __builtin_amdgcn_permlane32_swap(__float_as_uint(mx), __float_as_uint(mx), false, false); mx = fmaxf(__uint_as_float(t[0]), __uint_as_float(t[1])); }
            a0 = __builtin_amdgcn_exp2f(a0 - mx); a1 = __builtin_amdgcn_exp2f(a1 - mx); b0 = __builtin_amdgcn_exp2f(b0 - mx); b1 = __builtin_amdgcn_exp2f(b1 - mx);
            float sm = (a0 + a1) + (b0 + b1);
            { auto t = __builtin_amdgcn_permlane16_swap(__float_as_uint(sm), __float_as_uint(sm), false, false); sm = __uint_as_float(t[0]) + __uint_as_float(t[1]); }
            { auto t = __builtin_amdgcn_permlane32_swap(__float_as_uint(sm), __float_as_uint(sm), false, false); sm = __uint_as_float(t[0]) + __uint_as_float(t[1]); }
            const float rs = __builtin_amdgcn_rcpf(sm);
            a0 *= rs; a1 *= rs; b0 *= rs; b1 *= rs;
            const u32x4 Pf = {odd ? pk_bf16(0.f, a0) : pk_bf16(a0, 0.f), odd ? pk_bf16(0.f, a1) : pk_bf16(a1, 0.f),
                              odd ? pk_bf16(0.f, b0) : pk_bf16(b0, 0.f), odd ? pk_bf16(0.f, b1) : pk_bf16(b1, 0.f)};
            const u32x4 V0 = {Vp[0], Vp[1], Vh[m][0][0], Vh[m][0][1]}, V1 = {Vp[2], Vp[3], Vh[m][1][0], Vh[m][1][1]};
            const f32x4 o0 = MFMA16(as_frag(V0), as_frag(Pf), z);
            const f32x4 o1 = MFMA16(as_frag(V1), as_frag(Pf), z);
            unsigned x0 = pk_bf16(o0[0], o0[1]), x1 = pk_bf16(o0[2], o0[3]), y0 = pk_bf16(o1[0], o1[1]), y1 = pk_bf16(o1[2], o1[3]);
            auto r0 = __builtin_amdgcn_permlane16_swap(x0, y0, false, false);
            auto r1 = __builtin_amdgcn_permlane16_swap(x1, y1, false, false);
            *(u32x4*)(od + m * 1024) = (u32x4){r0[0], r1[0], r0[1], r1[1]};
            Kp = Kf[m];
            Vp = (u32x4){Vh[m][0][0], Vh[m][0][1], Vh[m][1][0], Vh[m][1][1]};
        }
    }
}

__global__ __launch_bounds__(512, 2) void k_gemm1(const char* __restrict__ wqkv, const char* __restrict__ wg, const char* __restrict__ wu, char* __restrict__ ws) {
    extern __shared__ __attribute__((aligned(16))) char smem[];
    LAS char* lds = (LAS char*)smem;
    const int tid = threadIdx.x, lane = tid & 63;
    const int wid = __builtin_amdgcn_readfirstlane(tid >> 6);
    const int bid = blockIdx.x, xcd = bid & 7, idx = bid >> 3;
    const int ct = 4 * xcd + (idx & 3), rt = idx >> 2;
    const unsigned ldsb = (unsigned)(size_t)lds;
    G1Ld L;
#pragma unroll
    for (int i = 0; i < 5; ++i) {
        int P = wid + 8 * i;
        if (P > 37) P = 37;
        if (P < 17) {
            const bool top = (rt == 0) && (P == 0);
            L.base[i] = ws + WS_XN + (size_t)(top ? 0 : 16 * rt - 1 + P) * 1024;
            L.step[i] = (unsigned)SLAB_B;
            L.voff[i] = top ? (unsigned)((lane & 48) * 16) : (unsigned)(lane * 16);
            L.lds0[i] = ldsb + P * 1024; L.ldss[i] = G1_AST;
        } else {
            const int pb = P - 17, reg = pb / 7, pc = pb - reg * 7;
            const int c = pc * 64 + lane, rho = c / 14, cc = c - rho * 14;
            const int k = (rho & ~12) | ((rho & 4) << 1) | ((rho & 8) >> 1);
            if (reg == 0) {
                const int c2 = cc < 12 ? cc : 11;
                L.base[i] = wqkv + ct * 64;
                L.step[i] = 32u * 3072u * 2u;
                L.voff[i] = (unsigned)(k * 6144 + (c2 >> 2) * 2048 + (c2 & 3) * 16);
            } else {
                L.base[i] = (reg == 1 ? wg : wu) + ct * 224;
                L.step[i] = 32u * 3584u * 2u;
                L.voff[i] = (unsigned)(k * 7168 + cc * 16);
            }
            L.lds0[i] = ldsb + G1_BOFF + pb * 1024; L.ldss[i] = G1_BST;
        }
    }
    L.cnt = wid < 6 ? 5u : 4u;
    if (wid < 4) g1_body<0>(L, lds, ws, rt, ct, wid & 3, lane);
    else g1_body<1>(L, lds, ws, rt, ct, wid & 3, lane);
}

constexpr int G2_ST = 16384;
constexpr int G2_XOFF = 4 * G2_ST;
constexpr int G2_LDS = G2_XOFF + 32768;

struct G2St {
    const char* pa;
    const char* pw;
    unsigned va0, va1, vw;
    unsigned ldsw;
};

template <int SLOT>
DEVI void g2_issue(G2St& T, u32x4 (&A)[4], const char* wdown, int tnext) {
    glds16(T.pw, T.vw, T.ldsw + SLOT * G2_ST);
    glds16(T.pw + 64 * 2048, T.vw, T.ldsw + SLOT * G2_ST + 8192);
    A[0] = *(const GAS u32x4*)(T.pa + T.va0);
    A[1] = *(const GAS u32x4*)(T.pa + T.va0 + 1024);
    A[2] = *(const GAS u32x4*)(T.pa + T.va1);
    A[3] = *(const GAS u32x4*)(T.pa + T.va1 + 1024);
    T.pa += 4 * SLAB_B;
    T.pw = (tnext == 7) ? wdown : T.pw + 128 * 2048;
}

template <int SLOT, int MODE>
DEVI void g2_stage(f32x4 (&acc)[2][4], G2St& T, u32x4 (&Ac)[4], u32x4 (&An)[4], LAS const char* (&pB)[4], const char* wdown, int t) {
    if (MODE <= 1) asm volatile("s_waitcnt vmcnt(12)" ::: "memory");
    else if (MODE == 2) asm volatile("s_waitcnt vmcnt(6)" ::: "memory");
    else asm volatile("s_waitcnt vmcnt(0)" ::: "memory");
    SBAR(); BAR(); SBAR();
    bf16x8 B[2][4];
#pragma unroll
    for (int s = 0; s < 2; ++s)
#pragma unroll
        for (int n = 0; n < 4; ++n) B[s][n] = tr_frag(pB[n], SLOT * G2_ST + s * 4096, SLOT * G2_ST + s * 4096 + 512);
    if (MODE == 0) g2_issue<(SLOT + 3) & 3>(T, An, wdown, t + 3);
#pragma unroll
    for (int s = 0; s < 2; ++s)
#pragma unroll
        for (int n = 0; n < 4; ++n)
#pragma unroll
            for (int m = 0; m < 2; ++m) acc[m][n] = MFMA16(B[s][n], as_frag(Ac[s * 2 + m]), acc[m][n]);
}

__global__ __launch_bounds__(512, 2) void k_gemm2(const u16* __restrict__ x, const char* __restrict__ wo, const char* __restrict__ wd, const char* __restrict__ ws, u16* __restrict__ out) {
    extern __shared__ __attribute__((aligned(16))) char smem[];
    LAS char* lds = (LAS char*)smem;
    const int tid = threadIdx.x, lane = tid & 63;
    const int wid = __builtin_amdgcn_readfirstlane(tid >> 6);
    const int kg = wid >> 2, wm = wid & 3;
    const int bid = blockIdx.x, xcd = bid & 7, idx = bid >> 3;
    const int rt = 4 * (xcd >> 1) + (idx >> 3), ct = 8 * (xcd & 1) + (idx & 7);
    const int g = lane >> 4, i16 = lane & 15, q = i16 >> 2, p = lane & 3;

    G2St T;
    T.pa = ws + WS_HO + (size_t)(2 * kg) * SLAB_B + (size_t)(8 * rt + 2 * wm) * 1024;
    T.va0 = lane * 16; T.va1 = lane * 16 + (unsigned)SLAB_B;
    T.pw = wo + ct * 128;
    const char* wdown = wd + ct * 128;
    {
        const int r = lane >> 3, cp = lane & 7, c = cp ^ ((r & 2) | ((wid & 1) << 2));
        T.vw = (unsigned)((8 * wid + r) * 2048 + c * 16);
    }
    T.ldsw = (unsigned)(size_t)lds + wid * 1024;
    LAS const char* pB[4];
#pragma unroll
    for (int n = 0; n < 4; ++n) pB[n] = lds + kg * 8192 + (8 * g + q) * 128 + (((2 * n + (p >> 1)) ^ ((q & 2) | ((g & 1) << 2))) * 16) + (p & 1) * 8;

    const int orow = 128 * rt + 32 * wm + 16 * kg + i16;
    u32x2 xr[4];
#pragma unroll
    for (int n = 0; n < 4; ++n) xr[n] = *(const u32x2*)(x + (size_t)orow * D_ + 64 * ct + 16 * n + 4 * g);

    f32x4 acc[2][4];
#pragma unroll
    for (int m = 0; m < 2; ++m)
#pragma unroll
        for (int n = 0; n < 4; ++n) acc[m][n] = (f32x4){0.f, 0.f, 0.f, 0.f};
    u32x4 A0[4], A1[4], A2[4], A3[4];
    g2_issue<0>(T, A0, wdown, 0); g2_issue<1>(T, A1, wdown, 1); g2_issue<2>(T, A2, wdown, 2);
    for (int t = 0; t < 32; t += 4) {
        g2_stage<0, 0>(acc, T, A0, A3, pB, wdown, t);
        g2_stage<1, 0>(acc, T, A1, A0, pB, wdown, t + 1);
        g2_stage<2, 0>(acc, T, A2, A1, pB, wdown, t + 2);
        g2_stage<3, 0>(acc, T, A3, A2, pB, wdown, t + 3);
    }
    g2_stage<0, 0>(acc, T, A0, A3, pB, wdown, 32);
    g2_stage<1, 1>(acc, T, A1, A0, pB, wdown, 33);
    g2_stage<2, 2>(acc, T, A2, A1, pB, wdown, 34);
    g2_stage<3, 3>(acc, T, A3, A2, pB, wdown, 35);

    LAS char* xs = lds + G2_XOFF;
#pragma unroll
    for (int n = 0; n < 4; ++n) *(LAS f32x4*)(xs + wid * 4096 + n * 1024 + lane * 16) = kg ? acc[0][n] : acc[1][n];
    asm volatile("s_waitcnt lgkmcnt(0)" ::: "memory");
    SBAR(); BAR(); SBAR();
    unsigned pk[4][2];
#pragma unroll
    for (int n = 0; n < 4; ++n) {
        const f32x4 o = *(LAS const f32x4*)(xs + (wid ^ 4) * 4096 + n * 1024 + lane * 16);
        const f32x4 a = kg ? acc[1][n] : acc[0][n];
        pk[n][0] = pk_bf16(a[0] + o[0] + bf_lo(xr[n][0]), a[1] + o[1] + bf_hi(xr[n][0]));
        pk[n][1] = pk_bf16(a[2] + o[2] + bf_lo(xr[n][1]), a[3] + o[3] + bf_hi(xr[n][1]));
    }
    u16* orp = out + (size_t)orow * D_ + 64 * ct + (g & 1) * 16 + (g >> 1) * 8;
#pragma unroll
    for (int n = 0; n < 4; n += 2) {
        auto r0 = __builtin_amdgcn_permlane16_swap(pk[n][0], pk[n + 1][0], false, false);
        auto r1 = __builtin_amdgcn_permlane16_swap(pk[n][1], pk[n + 1][1], false, false);
        *(u32x4*)(orp + 16 * n) = (u32x4){r0[0], r1[0], r0[1], r1[1]};
    }
}

extern "C" void kernel_launch(void* const* d_in, const int* in_sizes, int n_in,
                              void* d_out, int out_size, void* d_ws, size_t ws_size,
                              hipStream_t stream) {
    const u16* x = (const u16*)d_in[0];
    const char* wqkv = (const char*)d_in[1];
    const char* wo = (const char*)d_in[2];
    const char* wg = (const char*)d_in[4];
    const char* wu = (const char*)d_in[5];
    const char* wd = (const char*)d_in[6];
    char* ws = (char*)d_ws;
    (void)hipFuncSetAttribute((const void*)k_gemm1, hipFuncAttributeMaxDynamicSharedMemorySize, G1_LDS);
    (void)hipFuncSetAttribute((const void*)k_gemm2, hipFuncAttributeMaxDynamicSharedMemorySize, G2_LDS);
    k_ln<<<256, 512, 0, stream>>>(x, ws);
    k_gemm1<<<256, 512, G1_LDS, stream>>>(wqkv, wg, wu, ws);
    k_gemm2<<<256, 512, G2_LDS, stream>>>(x, wo, wd, ws, (u16*)d_out);
}
```

```cpp
#include <hip/hip_runtime.h>
#include <hip/hip_bf16.h>
#include <stdint.h>

typedef __bf16 bf16x8 __attribute__((ext_vector_type(8)));
typedef __bf16 bf16x4 __attribute__((ext_vector_type(4)));
typedef __bf16 bf16x2 __attribute__((ext_vector_type(2)));
typedef float f32x4 __attribute__((ext_vector_type(4)));
typedef float f32x2 __attribute__((ext_vector_type(2)));
typedef unsigned u32x4 __attribute__((ext_vector_type(4)));
typedef unsigned u32x2 __attribute__((ext_vector_type(2)));
typedef unsigned short u16;
#define LAS __attribute__((address_space(3)))
#define GAS __attribute__((address_space(1)))
#define DEVI __device__ __forceinline__

constexpr int S_ = 2048, D_ = 1024, F_ = 3584;
constexpr int MB_ = S_ / 16;
constexpr size_t SLAB_B = (size_t)MB_ * 1024;
constexpr size_t WS_XN = 0;
constexpr size_t WS_HO = 32 * SLAB_B;
constexpr size_t WS_XB = WS_HO + 144 * SLAB_B;

DEVI unsigned pk_bf16(float lo, float hi) {
    bf16x2 v = {(__bf16)lo, (__bf16)hi};
    return __builtin_bit_cast(unsigned, v);
}
DEVI float bf_lo(unsigned u) { return __uint_as_float(u << 16); }
DEVI float bf_hi(unsigned u) { return __uint_as_float(u & 0xffff0000u); }
DEVI float rbf(float f) { return (float)(__bf16)f; }
DEVI bf16x8 as_frag(u32x4 v) { return __builtin_bit_cast(bf16x8, v); }
#define MFMA16(a, b, c) __builtin_amdgcn_mfma_f32_16x16x32_bf16((a), (b), (c), 0, 0, 0)
#define SBAR() __builtin_amdgcn_sched_barrier(0)
#define BAR() __builtin_amdgcn_s_barrier()

#define TL_ENTRY()
#define TL_EXIT(ws, kern)
#define STAMP_DECL
#define STAMP_ARGS
#define STAMP_PASS
#define STAMP_SEG(i)
#define STAMP_START()
#define STAMP_SEGI(i)
#define STAMP_OUT(ws, kern, wid, lane)

DEVI bf16x8 tr_frag(LAS const char* p, int off0, int off1) {
    bf16x4 lo = __builtin_amdgcn_ds_read_tr16_b64_v4bf16((LAS bf16x4*)(p + off0));
    bf16x4 hi = __builtin_amdgcn_ds_read_tr16_b64_v4bf16((LAS bf16x4*)(p + off1));
    return __builtin_shufflevector(lo, hi, 0, 1, 2, 3, 4, 5, 6, 7);
}

DEVI void glds16(const char* sbase, unsigned voff, unsigned ldsdst) {
    unsigned keep;
    asm volatile("s_mov_b32 %0, m0\n\ts_mov_b32 m0, %3\n\ts_nop 0\n\tglobal_load_lds_dwordx4 %1, %2\n\ts_mov_b32 m0, %0"
                 : "=&s"(keep) : "v"(voff), "s"(sbase), "s"(ldsdst) : "memory");
}

__global__ __launch_bounds__(512) void k_ln(const u16* __restrict__ x, char* __restrict__ ws) {
    __shared__ float red[2][8][8];
    TL_ENTRY();
    const int tid = threadIdx.x, w = tid >> 6, l = tid & 63, r = l & 7, c8 = l >> 3;
    const int row = blockIdx.x * 8 + r;
    u32x4 v[2];
#pragma unroll
    for (int i = 0; i < 2; ++i) v[i] = *(const u32x4*)(x + (size_t)row * D_ + (16 * w + 8 * i + c8) * 8);
    float f[16];
#pragma unroll
    for (int i = 0; i < 2; ++i)
#pragma unroll
        for (int j = 0; j < 4; ++j) { f[i * 8 + 2 * j] = bf_lo(v[i][j]); f[i * 8 + 2 * j + 1] = bf_hi(v[i][j]); }
    float s = 0.f;
#pragma unroll
    for (int j = 0; j < 16; ++j) s += f[j];
    s += __shfl_xor(s, 8); s += __shfl_xor(s, 16); s += __shfl_xor(s, 32);
    if (c8 == 0) red[0][w][r] = s;
    __syncthreads();
    float tot = 0.f;
#pragma unroll
    for (int k = 0; k < 8; ++k) tot += red[0][k][r];
    const float mean = tot * (1.0f / D_);
    float q = 0.f;
#pragma unroll
    for (int j = 0; j < 16; ++j) { const float d = f[j] - mean; q += d * d; }
    q += __shfl_xor(q, 8); q += __shfl_xor(q, 16); q += __shfl_xor(q, 32);
    if (c8 == 0) red[1][w][r] = q;
    __syncthreads();
    float qt = 0.f;
#pragma unroll
    for (int k = 0; k < 8; ++k) qt += red[1][k][r];
    const float mb = rbf(mean);
    const float vb = rbf(qt * (1.0f / D_));
    const float den = rbf(sqrtf(rbf(vb + 1e-5f)));
    const float rc = 1.0f / den;
    const int mblk = row >> 4, rr = row & 15;
#pragma unroll
    for (int i = 0; i < 2; ++i) {
        u32x4 o;
#pragma unroll
        for (int j = 0; j < 4; ++j) {
            float y[2];
#pragma unroll
            for (int e = 0; e < 2; ++e) {
                const float d = rbf(f[i * 8 + 2 * j + e] - mb);
                float qq = d * rc;
                qq = fmaf(fmaf(-qq, den, d), rc, qq);
                y[e] = qq;
            }
            o[j] = pk_bf16(y[0], y[1]);
        }
        const int ch = 16 * w + 8 * i + c8, slab = ch >> 2, g = ch & 3;
        asm volatile("global_store_dwordx4 %0, %1, off sc1\n\ts_nop 1" :: "v"(ws + WS_XN + slab * SLAB_B + (size_t)mblk * 1024 + (g * 16 + rr) * 16), "v"(o) : "memory");
    }
    TL_EXIT(ws, 0);
}

constexpr int G1_AST = 17 * 1024;
constexpr int G1_BST = 21 * 1024;
constexpr int G1_REG = 7168;
constexpr int G1_BOFF = 4 * G1_AST;
constexpr int G1_XOFF = G1_BOFF + 4 * G1_BST;
constexpr int G1_LDS = G1_XOFF + 8192;

typedef unsigned long long u64;
struct G1Ld {
    const char *pa, *p2, *pg, *pu;
    unsigned stepa, step2, stepg;
    unsigned va, v2, vg;
    unsigned la, l2, s2, lg, lu;
    const char* warm; unsigned warmstep;
    char* pf;
    u64 ma, m2, mg;
};

#define WAIT_LGKM0() __builtin_amdgcn_s_waitcnt(0xC07F)
#define MEMFENCE() asm volatile("" ::: "memory")
#define OPAQUE_S(x) asm volatile("" : "+s"(x))

template <int LOFF, bool TWO>
DEVI void g1_piece(const char* base, unsigned voff, unsigned lds, u64 msk) {
    asm volatile("s_add_u32 m0, %2, %4\n\ts_mov_b64 exec, %3\n\tglobal_load_lds_dwordx4 %0, %1\n\t"
                 ".if %5\n\tglobal_load_lds_dwordx4 %0, %1 offset:1024\n\t.endif\n\ts_mov_b64 exec, -1"
                 :: "v"(voff), "s"(base), "s"(lds), "s"(msk), "i"(LOFF), "i"(TWO ? 1 : 0) : "memory", "scc");
}
template <int SLOT> DEVI void g1_pa(G1Ld& L) { g1_piece<SLOT * G1_AST, true>(L.pa, L.va, L.la, L.ma); L.pa += L.stepa; OPAQUE_S(L.pa); }
template <int SLOT> DEVI void g1_p2(G1Ld& L) { g1_piece<0, false>(L.p2, L.v2, L.l2 + SLOT * L.s2, L.m2); L.p2 += L.step2; OPAQUE_S(L.p2); }
template <int SLOT> DEVI void g1_pg(G1Ld& L) { g1_piece<SLOT * G1_BST, false>(L.pg, L.vg, L.lg, L.mg); L.pg += L.stepg; OPAQUE_S(L.pg); }
template <int SLOT> DEVI void g1_pu(G1Ld& L) { g1_piece<SLOT * G1_BST, false>(L.pu, L.vg, L.lu, L.mg); L.pu += L.stepg; OPAQUE_S(L.pu); }
template <int SLOT> DEVI void g1_issue(G1Ld& L) { g1_pa<SLOT>(L); g1_p2<SLOT>(L); g1_pg<SLOT>(L); g1_pu<SLOT>(L); }
DEVI void g1_tail(G1Ld& L, bool tail, const char* warm, unsigned warmstep) {
    L.p2 = tail ? L.p2 - L.step2 : L.p2; L.pg = tail ? L.pg - L.stepg : L.pg; L.pu = tail ? L.pu - L.stepg : L.pu;
    L.step2 = tail ? 0u : L.step2; L.stepg = tail ? 0u : L.stepg;
    L.m2 = tail ? 1ull : L.m2; L.mg = tail ? 1ull : L.mg;
    L.pa = tail ? warm : L.pa; L.stepa = tail ? warmstep : L.stepa;
}

struct G1Ptr { LAS const char *pA, *pH, *pB01, *pB23, *pBh01, *pBh23; };

template <int GRP, int SL> DEVI bf16x8 g1_ldb(const G1Ptr& P, int n) {
    LAS const char* pB = (SL < 2) ? P.pB01 : P.pB23;
    const int off = (SL & 1) * G1_BST + (GRP == 0 ? (n < 6 ? n * 32 : (n < 8 ? G1_REG + (n - 6) * 32 : 2 * G1_REG + (n - 8) * 32))
                                                  : (n < 5 ? G1_REG + (2 + n) * 32 : 2 * G1_REG + (n - 3) * 32));
    return tr_frag(pB, off, off + 8 * 224);
}
template <int SL> DEVI bf16x8 g1_lda(const G1Ptr& P, int m) { return *(LAS const bf16x8*)(P.pA + SL * G1_AST + (1 + m) * 1024); }
template <int SL> DEVI void g1_ldh(const G1Ptr& P, bf16x8& Ah, bf16x8& Bh) {
    Ah = *(LAS const bf16x8*)(P.pH + SL * G1_AST);
    Bh = tr_frag((SL < 2) ? P.pBh01 : P.pBh23, (SL & 1) * G1_BST, (SL & 1) * G1_BST + 8 * 224);
}

template <int GRP, int ST>
DEVI void g1_stage(f32x4 (&acc)[4][10], f32x4& acch, bf16x8 (&A)[4], bf16x8 (&B)[10], bf16x8& Ah, bf16x8& Bh, G1Ld& L, bool tail, const G1Ptr& P STAMP_ARGS) {
    constexpr int NX = (ST + 1) & 3, IS = (ST + 3) & 3;
#define MM(m, n) do { if (GRP == 0 && ((n) == 4 || (n) == 5)) acc[m][n] = MFMA16(A[m], B[n], acc[m][n]); else acc[m][n] = MFMA16(B[n], A[m], acc[m][n]); SBAR(); } while (0)
#define RB(n) do { B[n] = g1_ldb<GRP, NX>(P, n); SBAR(); } while (0)
#define RA(m) do { A[m] = g1_lda<NX>(P, m); SBAR(); } while (0)
#define G0(x) do { if (GRP == 0) { x; SBAR(); } } while (0)
    MM(0, 0); MM(1, 0); G0(g1_pa<IS>(L));
    MM(1, 1); MM(0, 1); G0(g1_p2<IS>(L));
    MM(0, 2); MM(1, 2); G0(g1_pg<IS>(L));
    MM(1, 3); MM(0, 3); G0(g1_pu<IS>(L));
    MM(0, 4); MM(1, 4);
    G0(acch = MFMA16(Bh, Ah, acch));
    asm volatile("s_waitcnt vmcnt(10)" ::: "memory");
    WAIT_LGKM0();
    MEMFENCE(); SBAR();
    BAR();
    MEMFENCE(); SBAR();
    if (GRP == 1) {
        if (ST == 0) g1_tail(L, tail, L.warm, L.warmstep);
        g1_issue<ST>(L);
        if (ST == 2) {
            const u32x4 ones = {0xffffffffu, 0xffffffffu, 0xffffffffu, 0xffffffffu};
            asm volatile("global_store_dwordx4 %0, %1, %2 sc1\n\ts_nop 1" :: "v"(L.va), "v"(ones), "s"(L.pf) : "memory");
            L.pf += 1024; OPAQUE_S(L.pf);
        }
        SBAR();
    }
    MM(2, 0); MM(3, 0); RB(0);
    MM(3, 1); MM(2, 1); RB(1);
    MM(2, 2); MM(3, 2); RB(2);
    MM(3, 3); MM(2, 3); RB(3);
    MM(2, 4); MM(3, 4); RB(4);
    MM(0, 5); MM(1, 5); G0(g1_ldh<NX>(P, Ah, Bh));
    MM(1, 6); MM(0, 6);
    MM(0, 7); MM(1, 7);
    MM(1, 8); MM(0, 8);
    MM(0, 9); RA(0); MM(1, 9); RA(1);
    MM(2, 5); MM(3, 5); RB(5);
    MM(3, 6); MM(2, 6); RB(6);
    MM(2, 7); MM(3, 7); RB(7);
    MM(3, 8); MM(2, 8); RB(8);
    MM(2, 9); RA(2); MM(3, 9); RA(3); RB(9);
#undef MM
#undef RB
#undef RA
#undef G0
}

DEVI void st16(char* dst, u32x4 v) {
    asm volatile("global_store_dwordx4 %0, %1, off sc1\n\ts_nop 1" :: "v"(dst), "v"(v) : "memory");
}

DEVI unsigned glu2(float g0, float g1, float u0, float u1) {
    const f32x2 t = (f32x2){g0, g1} * -1.4426950408889634f;
    const f32x2 d = (f32x2){__builtin_amdgcn_exp2f(t[0]), __builtin_amdgcn_exp2f(t[1])} + 1.0f;
    const f32x2 h = (f32x2){u0, u1} * (f32x2){__builtin_amdgcn_rcpf(d[0]), __builtin_amdgcn_rcpf(d[1])};
    return pk_bf16(h[0], h[1]);
}

DEVI void glu_store(const f32x4& g0, const f32x4& u0, const f32x4& g1, const f32x4& u1, char* dst) {
    unsigned a0 = glu2(g0[0], g0[1], u0[0], u0[1]), a1 = glu2(g0[2], g0[3], u0[2], u0[3]);
    unsigned b0 = glu2(g1[0], g1[1], u1[0], u1[1]), b1 = glu2(g1[2], g1[3], u1[2], u1[3]);
    auto r0 = __builtin_amdgcn_permlane16_swap(a0, b0, false, false);
    auto r1 = __builtin_amdgcn_permlane16_swap(a1, b1, false, false);
    st16(dst, (u32x4){r0[0], r1[0], r0[1], r1[1]});
}

template <int GRP>
DEVI void g1_body(G1Ld& L, LAS char* lds, char* ws, int rt, int ct, int wm, int lane) {
    const int g = lane >> 4, i16 = lane & 15, q = i16 >> 2, p = lane & 3;
    G1Ptr P;
    P.pA = lds + wm * 4096 + lane * 16;
    P.pH = lds + lane * 16;
    const int brow = (16 * (g >> 1) + 4 * (g & 1) + q) * 224 + p * 8;
    P.pB01 = lds + G1_BOFF + brow;
    P.pB23 = P.pB01 + 2 * G1_BST;
    P.pBh01 = P.pB01 + (2 + wm) * 32;
    P.pBh23 = P.pB23 + (2 + wm) * 32;
    f32x4 acc[4][10], acch = {0.f, 0.f, 0.f, 0.f};
#pragma unroll
    for (int m = 0; m < 4; ++m)
#pragma unroll
        for (int n = 0; n < 10; ++n) acc[m][n] = (f32x4){0.f, 0.f, 0.f, 0.f};

    STAMP_DECL STAMP_START();
    g1_issue<0>(L); g1_issue<1>(L);
    asm volatile("s_waitcnt vmcnt(5)" ::: "memory");
    SBAR(); BAR(); MEMFENCE(); SBAR();
    bf16x8 A[4], B[10], Ah, Bh;
#pragma unroll
    for (int n = 0; n < 10; ++n) { if (n < 4) A[n] = g1_lda<0>(P, n); B[n] = g1_ldb<GRP, 0>(P, n); }
    if (GRP == 0) g1_ldh<0>(P, Ah, Bh);
    SBAR();
    g1_issue<2>(L);
    if (GRP == 1) g1_issue<3>(L);
    SBAR();
    STAMP_SEG(0);
#pragma unroll 1
    for (int it = 0; it < 8; ++it) {
        g1_stage<GRP, 0>(acc, acch, A, B, Ah, Bh, L, it == 7, P STAMP_PASS);
        if (GRP == 0) g1_tail(L, it == 7, L.warm, L.warmstep);
        g1_stage<GRP, 1>(acc, acch, A, B, Ah, Bh, L, it == 7, P STAMP_PASS);
        g1_stage<GRP, 2>(acc, acch, A, B, Ah, Bh, L, it == 7, P STAMP_PASS);
        g1_stage<GRP, 3>(acc, acch, A, B, Ah, Bh, L, it == 7, P STAMP_PASS);
    }
    STAMP_SEG(1);

    char* ho = ws + WS_HO;
    const int mb0 = 16 * rt + 4 * wm;
    const unsigned hl = (unsigned)((g & 1) * 1024 + (g >> 1) * 256 + i16 * 16);
    if (GRP == 1) {
        SBAR(); BAR(); SBAR();
#pragma unroll
        for (int n = 0; n < 5; ++n) {
            const int cb = 7 * ct + 2 + n;
            char* dst = ho + (size_t)(32 + (cb >> 1)) * SLAB_B + (cb & 1) * 512 + (size_t)mb0 * 1024 + hl;
#pragma unroll
            for (int m = 0; m < 4; m += 2) glu_store(acc[m][n], acc[m][5 + n], acc[m + 1][n], acc[m + 1][5 + n], dst + m * 1024);
        }
    } else {
        u32x4 Qf[4], Kf[4]; u32x2 Vh[4][2];
#pragma unroll
        for (int m = 0; m < 4; ++m) {
            Qf[m] = (u32x4){pk_bf16(acc[m][0][0], acc[m][0][1]), pk_bf16(acc[m][0][2], acc[m][0][3]), pk_bf16(acc[m][1][0], acc[m][1][1]), pk_bf16(acc[m][1][2], acc[m][1][3])};
            Kf[m] = (u32x4){pk_bf16(acc[m][2][0], acc[m][2][1]), pk_bf16(acc[m][2][2], acc[m][2][3]), pk_bf16(acc[m][3][0], acc[m][3][1]), pk_bf16(acc[m][3][2], acc[m][3][3])};
#pragma unroll
            for (int b = 0; b < 2; ++b) Vh[m][b] = (u32x2){pk_bf16(acc[m][4 + b][0], acc[m][4 + b][1]), pk_bf16(acc[m][4 + b][2], acc[m][4 + b][3])};
        }
        LAS char* xs = lds + G1_XOFF;
        if (wm < 3) {
            *(LAS u32x4*)(xs + (wm + 1) * 2048 + lane * 16) = Kf[3];
            *(LAS u32x4*)(xs + (wm + 1) * 2048 + 1024 + lane * 16) = (u32x4){Vh[3][0][0], Vh[3][0][1], Vh[3][1][0], Vh[3][1][1]};
        }
        {
            const unsigned h0 = pk_bf16(acch[0], acch[1]), h1 = pk_bf16(acch[2], acch[3]);
            if (wm < 2) {
                *(LAS u32x2*)(xs + lane * 16 + wm * 8) = (u32x2){h0, h1};
            } else {
                LAS char* t = xs + 1024 + ((i16 >> 2) * 16 + 4 * g) * 16 + (wm - 2) * 8 + (i16 & 3) * 2;
                *(LAS u16*)(t) = (u16)h0; *(LAS u16*)(t + 16) = (u16)(h0 >> 16); *(LAS u16*)(t + 32) = (u16)h1; *(LAS u16*)(t + 48) = (u16)(h1 >> 16);
            }
        }
        asm volatile("s_waitcnt lgkmcnt(0)" ::: "memory");
        SBAR(); BAR(); SBAR();
        u32x4 Kp = *(LAS const u32x4*)(xs + wm * 2048 + lane * 16);
        u32x4 Vp = *(LAS const u32x4*)(xs + wm * 2048 + 1024 + lane * 16);
#pragma unroll
        for (int n = 0; n < 2; ++n) {
            const int cb = 7 * ct + n;
            char* dst = ho + (size_t)(32 + (cb >> 1)) * SLAB_B + (cb & 1) * 512 + (size_t)mb0 * 1024 + hl;
#pragma unroll
            for (int m = 0; m < 4; m += 2) glu_store(acc[m][6 + n], acc[m][8 + n], acc[m + 1][6 + n], acc[m + 1][8 + n], dst + m * 1024);
        }
        const bool odd = (i16 & 1) != 0;
        const int par = i16 & 1;
        const int dp0 = 16 + i16 - 4 * g - par, dc0 = i16 - 4 * g - par;
        const bool vp0 = dp0 <= 14, vp1 = dp0 - 2 <= 14, vc0 = dc0 >= 0, vc1 = dc0 - 2 >= 0;
        const float NEG = -__builtin_inff();
        const float sc = 0.17677669529663687f * 1.4426950408889634f;
        const bool first = (rt == 0) && (wm == 0);
        char* od = ho + (size_t)ct * SLAB_B + (size_t)mb0 * 1024 + (((g & 1) << 1) | (g >> 1)) * 256 + i16 * 16;
#pragma unroll
        for (int m = 0; m < 4; ++m) {
            const f32x4 z = {0.f, 0.f, 0.f, 0.f};
            f32x4 s0 = MFMA16(as_frag(Kp), as_frag(Qf[m]), z);
            f32x4 s1 = MFMA16(as_frag(Kf[m]), as_frag(Qf[m]), z);
            float a0 = (odd ? s0[1] : s0[0]) * sc, a1 = (odd ? s0[3] : s0[2]) * sc;
            float b0 = (odd ? s1[1] : s1[0]) * sc, b1 = (odd ? s1[3] : s1[2]) * sc;
            if (m == 0 && first) { a0 = -1e9f * 1.4426950408889634f; a1 = a0; }
            a0 = vp0 ? a0 : NEG; a1 = vp1 ? a1 : NEG; b0 = vc0 ? b0 : NEG; b1 = vc1 ? b1 : NEG;
            float mx = fmaxf(fmaxf(a0, a1), fmaxf(b0, b1));
            { auto t = __builtin_amdgcn_permlane16_swap(__float_as_uint(mx), __float_as_uint(mx), false, false); mx = fmaxf(__uint_as_float(t[0]), __uint_as_float(t[1])); }
            { auto t = __builtin_amdgcn_permlane32_swap(__float_as_uint(mx), __float_as_uint(mx), false, false); mx = fmaxf(__uint_as_float(t[0]), __uint_as_float(t[1])); }
            a0 = __builtin_amdgcn_exp2f(a0 - mx); a1 = __builtin_amdgcn_exp2f(a1 - mx); b0 = __builtin_amdgcn_exp2f(b0 - mx); b1 = __builtin_amdgcn_exp2f(b1 - mx);
            float sm = (a0 + a1) + (b0 + b1);
            { auto t = __builtin_amdgcn_permlane16_swap(__float_as_uint(sm), __float_as_uint(sm), false, false); sm = __uint_as_float(t[0]) + __uint_as_float(t[1]); }
            { auto t = __builtin_amdgcn_permlane32_swap(__float_as_uint(sm), __float_as_uint(sm), false, false); sm = __uint_as_float(t[0]) + __uint_as_float(t[1]); }
            const float rs = __builtin_amdgcn_rcpf(sm);
            a0 *= rs; a1 *= rs; b0 *= rs; b1 *= rs;
            const u32x4 Pf = {odd ? pk_bf16(0.f, a0) : pk_bf16(a0, 0.f), odd ? pk_bf16(0.f, a1) : pk_bf16(a1, 0.f),
                              odd ? pk_bf16(0.f, b0) : pk_bf16(b0, 0.f), odd ? pk_bf16(0.f, b1) : pk_bf16(b1, 0.f)};
            const u32x4 V0 = {Vp[0], Vp[1], Vh[m][0][0], Vh[m][0][1]}, V1 = {Vp[2], Vp[3], Vh[m][1][0], Vh[m][1][1]};
            const f32x4 o0 = MFMA16(as_frag(V0), as_frag(Pf), z);
            const f32x4 o1 = MFMA16(as_frag(V1), as_frag(Pf), z);
            unsigned x0 = pk_bf16(o0[0], o0[1]), x1 = pk_bf16(o0[2], o0[3]), y0 = pk_bf16(o1[0], o1[1]), y1 = pk_bf16(o1[2], o1[3]);
            auto r0 = __builtin_amdgcn_permlane16_swap(x0, y0, false, false);
            auto r1 = __builtin_amdgcn_permlane16_swap(x1, y1, false, false);
            st16(od + m * 1024, (u32x4){r0[0], r1[0], r0[1], r1[1]});
            Kp = Kf[m];
            Vp = (u32x4){Vh[m][0][0], Vh[m][0][1], Vh[m][1][0], Vh[m][1][1]};
        }
    }
    STAMP_SEG(5);
    STAMP_SEG(6);
    STAMP_OUT(ws, 0, GRP * 4 + wm, lane);
}

__global__ __launch_bounds__(512, 2) void k_gemm1(const char* __restrict__ wqkv, const char* __restrict__ wg, const char* __restrict__ wu, const char* __restrict__ wo, const char* __restrict__ wd, char* __restrict__ ws) {
    extern __shared__ __attribute__((aligned(16))) char smem[];
    TL_ENTRY();
    LAS char* lds = (LAS char*)smem;
    const int tid = threadIdx.x, lane = tid & 63;
    const int wid = __builtin_amdgcn_readfirstlane(tid >> 6);
    const int bid = blockIdx.x, xcd = bid & 7, idx = bid >> 3;
    const int ct = 4 * xcd + (idx & 3), rt = idx >> 2;
    const unsigned ldsb = (unsigned)(size_t)lds;
    G1Ld L;
    {
        const int c = wid * 64 + lane, rho = c / 14, cc = c - rho * 14;
        const int k = (rho & ~12) | ((rho & 4) << 1) | ((rho & 8) >> 1);
        const int c2 = cc < 12 ? cc : 11;
        const bool last = wid == 7;
        L.pa = ws + WS_XN + (size_t)(16 * rt + 2 * wid) * 1024;
        L.stepa = (unsigned)SLAB_B;
        L.va = lane * 16;
        L.la = ldsb + (1 + 2 * wid) * 1024;
        L.p2 = last ? ws + WS_XN + (size_t)(rt == 0 ? 0 : 16 * rt - 1) * 1024 : wqkv + ct * 64;
        L.step2 = last ? (unsigned)SLAB_B : 32u * 3072u * 2u;
        L.v2 = last ? (unsigned)((rt == 0 ? (lane & 48) : lane) * 16) : (unsigned)(k * 6144 + (c2 >> 2) * 2048 + (c2 & 3) * 16);
        L.l2 = last ? ldsb : ldsb + G1_BOFF + wid * 1024;
        L.s2 = last ? G1_AST : G1_BST;
        L.pg = wg + ct * 224;
        L.pu = wu + ct * 224;
        L.stepg = 32u * 3584u * 2u;
        L.vg = last ? 0u : (unsigned)(k * 7168 + cc * 16);
        L.lg = last ? ldsb + G1_BOFF + 192 : ldsb + G1_BOFF + G1_REG + wid * 1024;
        L.lu = last ? ldsb + G1_BOFF + 192 : ldsb + G1_BOFF + 2 * G1_REG + wid * 1024;
        L.ma = ~0ull; L.m2 = ~0ull; L.mg = last ? 1ull : ~0ull;
    }
    const int wdo = bid * 28672 < 3584 * 2048 - 32768 ? bid * 28672 : 3584 * 2048 - 32768;
    L.warm = wid < 4 ? wo + bid * 8192 + wid * 2048 : wd + wdo + (wid & 3) * 2048;
    L.warmstep = wid < 4 ? 0u : 8192u;
    L.pf = ws + WS_XB + (size_t)bid * 32768 + (wid & 3) * 8192;
    if (wid < 4) g1_body<0>(L, lds, ws, rt, ct, wid & 3, lane);
    else g1_body<1>(L, lds, ws, rt, ct, wid & 3, lane);
    TL_EXIT(ws, 1);
}

#define POLL_SLEEP 2
constexpr int G2_ST = 16384;
constexpr int G2_NS = 6;
constexpr int G2_LDS = G2_NS * G2_ST;

struct G2St {
    const char* pa;
    const char* pw;
    const char* wdown;
    unsigned va, vw0, vw1;
    unsigned ldsw;
    int wst;
};
template <int LOFF> DEVI void g2_glds(const char* base, unsigned voff, unsigned lds) {
    asm volatile("s_add_u32 m0, %2, %3\n\ts_nop 0\n\tglobal_load_lds_dwordx4 %0, %1" :: "v"(voff), "s"(base), "s"(lds), "i"(LOFF) : "memory", "scc");
}
template <int OFF> DEVI void g2_lda(u32x4& dst, const char* base, unsigned voff) {
    asm volatile("global_load_dwordx4 %0, %1, %2 offset:%3" : "=v"(dst) : "v"(voff), "s"(base), "i"(OFF) : "memory");
}
template <int SLOT, int I> DEVI void g2_w(G2St& T) {
    if (I == 0) g2_glds<SLOT * G2_ST>(T.pw, T.vw0, T.ldsw);
    else { g2_glds<SLOT * G2_ST + 8192>(T.pw, T.vw1, T.ldsw); T.pw = (T.wst == 15) ? T.wdown : T.pw + 64 * 2048; T.wst += 1; OPAQUE_S(T.pw); }
}
template <int I> DEVI void g2_a(G2St& T, u32x4 (&A)[2]) {
    if (I == 0) g2_lda<0>(A[0], T.pa, T.va);
    else { g2_lda<1024>(A[1], T.pa, T.va); T.pa += 2 * SLAB_B; OPAQUE_S(T.pa); }
}
template <int SLOT> DEVI void g2_group(G2St& T, u32x4 (&A)[2]) { g2_w<SLOT, 0>(T); g2_w<SLOT, 1>(T); g2_a<0>(T, A); g2_a<1>(T, A); }

template <int SLOT> DEVI bf16x8 g2_ldb(LAS const char* (&pB)[2][4], int n) {
    return tr_frag(pB[SLOT / 3][n & 3], (SLOT % 3) * G2_ST + (n >> 2) * 8192, (SLOT % 3) * G2_ST + (n >> 2) * 8192 + 512);
}

template <int U, int WAIT, bool ISW, bool ISA, bool RL>
DEVI void g2_stage(f32x4 (&acc)[2][8], G2St& T, u32x4 (&Ac)[2], u32x4 (&An)[2], bf16x8 (&B)[8], LAS const char* (&pB)[2][4] STAMP_ARGS) {
    constexpr int NX = (U + 1) % G2_NS, IS = (U + 5) % G2_NS;
    asm volatile("s_waitcnt vmcnt(%2)" : "+v"(Ac[0]), "+v"(Ac[1]) : "i"(WAIT) : "memory");
    STAMP_SEGI(1);
    SBAR(); BAR(); MEMFENCE(); SBAR();
    STAMP_SEGI(2);
#define P2(n) do { acc[(n) & 1][n] = MFMA16(B[n], as_frag(Ac[(n) & 1]), acc[(n) & 1][n]); SBAR(); acc[~(n) & 1][n] = MFMA16(B[n], as_frag(Ac[~(n) & 1]), acc[~(n) & 1][n]); SBAR(); \
                   if (RL) { B[n] = g2_ldb<NX>(pB, n); SBAR(); } } while (0)
    P2(0); if (ISW) { g2_w<IS, 0>(T); SBAR(); }
    P2(1);
    P2(2); if (ISW) { g2_w<IS, 1>(T); SBAR(); }
    P2(3);
    P2(4); if (ISA) { g2_a<0>(T, An); SBAR(); }
    P2(5);
    P2(6); if (ISA) { g2_a<1>(T, An); SBAR(); }
    P2(7);
#undef P2
    STAMP_SEGI(3);
}

__global__ __launch_bounds__(512, 2) void k_gemm2(const u16* __restrict__ x, const char* __restrict__ wo, const char* __restrict__ wd, char* __restrict__ ws, u16* __restrict__ out) {
    extern __shared__ __attribute__((aligned(16))) char smem[];
    TL_ENTRY();
    LAS char* lds = (LAS char*)smem;
    const int tid = threadIdx.x, lane = tid & 63;
    const int wid = __builtin_amdgcn_readfirstlane(tid >> 6);
    const int kg = wid >> 2, wm = wid & 3;
    const int bid = blockIdx.x, kh = bid & 1, quad = (bid >> 1) & 3, idx = bid >> 3;
    const int rt = 8 * (quad >> 1) + (idx >> 2), ct = 4 * (quad & 1) + (idx & 3);
    const int g = lane >> 4, i16 = lane & 15, q = i16 >> 2, p = lane & 3;

    G2St T;
    T.pa = ws + WS_HO + (size_t)(72 * kh + kg) * SLAB_B + (size_t)(8 * rt + 2 * wm) * 1024;
    T.va = lane * 16;
    T.wdown = wd + ct * 256;
    T.pw = kh ? T.wdown + (size_t)(2304 - 1024) * 2048 : wo + ct * 256;
    T.wst = 36 * kh;
    {
        const int r = lane >> 3, cp = lane & 7, c = cp ^ ((r & 2) | ((wid & 1) << 2));
        const int keep = kh ^ kg ^ 1;
        T.vw0 = (unsigned)((8 * wid + r) * 2048 + c * 16 + keep * 128);
        T.vw1 = T.vw0 ^ 128;
    }
    T.ldsw = (unsigned)(size_t)lds + wid * 1024;

    STAMP_DECL STAMP_START();
    u32x4 A0[2], A1[2], A2[2], A3[2], A4[2], A5[2];
    g2_w<0, 0>(T); g2_w<0, 1>(T);
    g2_group<1>(T, A0);

    LAS const char* pB[2][4];
#pragma unroll
    for (int n = 0; n < 4; ++n) {
        pB[0][n] = lds + kg * 4096 + (8 * g + q) * 128 + (((2 * n + (p >> 1)) ^ ((q & 2) | ((g & 1) << 2))) * 16) + (p & 1) * 8;
        pB[1][n] = pB[0][n] + 3 * G2_ST;
    }
    f32x4 acc[2][8];
#pragma unroll
    for (int m = 0; m < 2; ++m)
#pragma unroll
        for (int n = 0; n < 8; ++n) acc[m][n] = (f32x4){0.f, 0.f, 0.f, 0.f};
    g2_group<2>(T, A1);
    asm volatile("s_waitcnt vmcnt(8)" ::: "memory");
    SBAR(); BAR(); MEMFENCE(); SBAR();
    bf16x8 B[8];
#pragma unroll
    for (int n = 0; n < 8; ++n) B[n] = g2_ldb<0>(pB, n);
    SBAR();
    g2_group<3>(T, A2); g2_group<4>(T, A3);
    SBAR();
    STAMP_SEG(0);
#pragma unroll 1
    for (int it = 0; it < 5; ++it) {
        g2_stage<0, 12, true, true, true>(acc, T, A0, A4, B, pB STAMP_PASS);
        g2_stage<1, 12, true, true, true>(acc, T, A1, A5, B, pB STAMP_PASS);
        g2_stage<2, 12, true, true, true>(acc, T, A2, A0, B, pB STAMP_PASS);
        g2_stage<3, 12, true, true, true>(acc, T, A3, A1, B, pB STAMP_PASS);
        g2_stage<4, 12, true, true, true>(acc, T, A4, A2, B, pB STAMP_PASS);
        g2_stage<5, 12, true, true, true>(acc, T, A5, A3, B, pB STAMP_PASS);
    }
    g2_stage<0, 12, true, true, true>(acc, T, A0, A4, B, pB STAMP_PASS);
    g2_stage<1, 12, false, true, true>(acc, T, A1, A5, B, pB STAMP_PASS);
    const int orow = 128 * rt + 32 * wm + i16, ocol = 128 * ct + 64 * kh;
    u32x4 xr[4];
    {
        const u16* xp = x + (size_t)orow * D_ + ocol + (g & 1) * 16 + (g >> 1) * 8;
        const u16* xq = xp + 16 * D_;
        asm volatile("global_load_dwordx4 %0, %4, off\n\tglobal_load_dwordx4 %1, %4, off offset:64\n\tglobal_load_dwordx4 %2, %5, off\n\tglobal_load_dwordx4 %3, %5, off offset:64"
                     : "=&v"(xr[0]), "=&v"(xr[1]), "=&v"(xr[2]), "=&v"(xr[3]) : "v"(xp), "v"(xq) : "memory");
    }
    g2_stage<2, 14, false, false, true>(acc, T, A2, A0, B, pB STAMP_PASS);
    g2_stage<3, 10, false, false, true>(acc, T, A3, A1, B, pB STAMP_PASS);
    g2_stage<4, 6, false, false, true>(acc, T, A4, A2, B, pB STAMP_PASS);
    g2_stage<5, 4, false, false, false>(acc, T, A5, A3, B, pB STAMP_PASS);
    STAMP_SEG(3);

    {
        LAS char* xs = lds + wid * 8192 + lane * 16;
#pragma unroll
        for (int m = 0; m < 2; ++m)
#pragma unroll
            for (int j = 0; j < 4; ++j) *(LAS f32x4*)(xs + (m * 4 + j) * 1024) = acc[m][4 + j];
    }
    asm volatile("s_waitcnt vmcnt(0)" : "+v"(xr[0]), "+v"(xr[1]), "+v"(xr[2]), "+v"(xr[3]) :: "memory");
    WAIT_LGKM0();
    MEMFENCE(); SBAR(); BAR(); MEMFENCE(); SBAR();
    LAS const char* xl = lds + (wid ^ 4) * 8192 + lane * 16;
    char* xb = ws + WS_XB + wm * 8192 + lane * 16;
    if (kg == 0) {
        char* dst = xb + (size_t)bid * 32768;
#pragma unroll
        for (int m = 0; m < 2; ++m)
#pragma unroll
            for (int j = 0; j < 4; ++j) {
                u32x4 w = __builtin_bit_cast(u32x4, acc[m][j] + *(LAS const f32x4*)(xl + (m * 4 + j) * 1024));
#pragma unroll
                for (int e = 0; e < 4; ++e) w[e] = w[e] < 0xfffffffeu ? w[e] : 0xfffffffeu;
                asm volatile("global_store_dwordx4 %0, %1, off offset:%2 sc1\n\ts_nop 1" :: "v"(dst + m * 4096), "v"(w), "i"(j * 1024) : "memory");
            }
        STAMP_SEG(4);
    } else {
#pragma unroll
        for (int m = 0; m < 2; ++m)
#pragma unroll
            for (int j = 0; j < 4; ++j) acc[m][j] += *(LAS const f32x4*)(xl + (m * 4 + j) * 1024);
        STAMP_SEG(4);
        u32x4 rc[8];
        const char* src = xb + (size_t)(bid ^ 1) * 32768;
        unsigned spin = 0;
        {
            const char* last = src - lane * 16 + 8188;
            for (;; ++spin) {
                unsigned w;
                asm volatile("global_load_dword %0, %1, off sc1\n\ts_waitcnt vmcnt(0)" : "=&v"(w) : "v"(last) : "memory");
                if (__builtin_amdgcn_readfirstlane(w) != 0xffffffffu || spin > (1u << 22)) break;
                __builtin_amdgcn_s_sleep(POLL_SLEEP);
            }
        }
        for (;; ++spin) {
            asm volatile("global_load_dwordx4 %0, %8, off sc1\n\tglobal_load_dwordx4 %1, %8, off offset:1024 sc1\n\tglobal_load_dwordx4 %2, %8, off offset:2048 sc1\n\tglobal_load_dwordx4 %3, %8, off offset:3072 sc1\n\t"
                         "global_load_dwordx4 %4, %9, off sc1\n\tglobal_load_dwordx4 %5, %9, off offset:1024 sc1\n\tglobal_load_dwordx4 %6, %9, off offset:2048 sc1\n\tglobal_load_dwordx4 %7, %9, off offset:3072 sc1\n\ts_waitcnt vmcnt(0)"
                         : "=&v"(rc[0]), "=&v"(rc[1]), "=&v"(rc[2]), "=&v"(rc[3]), "=&v"(rc[4]), "=&v"(rc[5]), "=&v"(rc[6]), "=&v"(rc[7]) : "v"(src), "v"(src + 4096) : "memory");
            unsigned mx = 0;
#pragma unroll
            for (int j = 0; j < 8; ++j) { const unsigned a = rc[j][0] > rc[j][1] ? rc[j][0] : rc[j][1], b = rc[j][2] > rc[j][3] ? rc[j][2] : rc[j][3]; mx = mx > a ? mx : a; mx = mx > b ? mx : b; }
            if (!__any(mx == 0xffffffffu) || spin > (1u << 22)) break;
            __builtin_amdgcn_s_sleep(4);
        }
        u16* orp = out + (size_t)orow * D_ + ocol + (g & 1) * 16 + (g >> 1) * 8;
#pragma unroll
        for (int m = 0; m < 2; ++m)
#pragma unroll
            for (int j = 0; j < 4; j += 2) {
                const f32x4 s0 = acc[m][j] + __builtin_bit_cast(f32x4, rc[m * 4 + j]), s1 = acc[m][j + 1] + __builtin_bit_cast(f32x4, rc[m * 4 + j + 1]);
                float v[8];
#pragma unroll
                for (int e = 0; e < 4; ++e) { auto r = __builtin_amdgcn_permlane16_swap(__float_as_uint(s0[e]), __float_as_uint(s1[e]), false, false); v[e] = __uint_as_float(r[0]); v[4 + e] = __uint_as_float(r[1]); }
                const u32x4 xv = xr[m * 2 + (j >> 1)];
                u32x4 o;
#pragma unroll
                for (int e = 0; e < 4; ++e) o[e] = pk_bf16(v[2 * e] + bf_lo(xv[e]), v[2 * e + 1] + bf_hi(xv[e]));
                *(u32x4*)(orp + m * 16 * D_ + 16 * j) = o;
            }
    }
    STAMP_SEG(5);
    STAMP_SEG(6);
    STAMP_OUT(ws, 1, wid, lane);
    TL_EXIT(ws, 2);
}

extern "C" void kernel_launch(void* const* d_in, const int* in_sizes, int n_in,
                              void* d_out, int out_size, void* d_ws, size_t ws_size,
                              hipStream_t stream) {
    const u16* x = (const u16*)d_in[0];
    const char* wqkv = (const char*)d_in[1];
    const char* wo = (const char*)d_in[2];
    const char* wg = (const char*)d_in[4];
    const char* wu = (const char*)d_in[5];
    const char* wd = (const char*)d_in[6];
    char* ws = (char*)d_ws;
    (void)hipFuncSetAttribute((const void*)k_gemm1, hipFuncAttributeMaxDynamicSharedMemorySize, G1_LDS);
    (void)hipFuncSetAttribute((const void*)k_gemm2, hipFuncAttributeMaxDynamicSharedMemorySize, G2_LDS);
    k_ln<<<256, 512, 0, stream>>>(x, ws);
    k_gemm1<<<256, 512, G1_LDS, stream>>>(wqkv, wg, wu, wo, wd, ws);
    k_gemm2<<<256, 512, G2_LDS, stream>>>(x, wo, wd, ws, (u16*)d_out);
}
```

```cpp
#define STAMPS
#define AB_NOINNER
#define PROBE_I -1
#define PROBE_K 1
#define PROBE_KERN 0
#define PROBE_SEG 0
#define PROBE_DIV 10
#include <hip/hip_runtime.h>
#include <hip/hip_bf16.h>
#include <stdint.h>

typedef __bf16 bf16x8 __attribute__((ext_vector_type(8)));
typedef __bf16 bf16x4 __attribute__((ext_vector_type(4)));
typedef __bf16 bf16x2 __attribute__((ext_vector_type(2)));
typedef float f32x4 __attribute__((ext_vector_type(4)));
typedef float f32x2 __attribute__((ext_vector_type(2)));
typedef unsigned u32x4 __attribute__((ext_vector_type(4)));
typedef unsigned u32x2 __attribute__((ext_vector_type(2)));
typedef unsigned short u16;
#define LAS __attribute__((address_space(3)))
#define GAS __attribute__((address_space(1)))
#define DEVI __device__ __forceinline__

constexpr int S_ = 2048, D_ = 1024, F_ = 3584;
constexpr int MB_ = S_ / 16;
constexpr size_t SLAB_B = (size_t)MB_ * 1024;
constexpr size_t WS_XN = 0;
constexpr size_t WS_HO = 32 * SLAB_B;
constexpr size_t WS_XB = WS_HO + 144 * SLAB_B;

DEVI unsigned pk_bf16(float lo, float hi) {
    bf16x2 v = {(__bf16)lo, (__bf16)hi};
    return __builtin_bit_cast(unsigned, v);
}
DEVI float bf_lo(unsigned u) { return __uint_as_float(u << 16); }
DEVI float bf_hi(unsigned u) { return __uint_as_float(u & 0xffff0000u); }
DEVI float rbf(float f) { return (float)(__bf16)f; }
DEVI bf16x8 as_frag(u32x4 v) { return __builtin_bit_cast(bf16x8, v); }
#define MFMA16(a, b, c) __builtin_amdgcn_mfma_f32_16x16x32_bf16((a), (b), (c), 0, 0, 0)
#define SBAR() __builtin_amdgcn_sched_barrier(0)
#define BAR() __builtin_amdgcn_s_barrier()

#define STAMP(var) do { SBAR(); asm volatile("s_memtime %0\n\ts_waitcnt lgkmcnt(0)" : "=s"(var) :: "memory"); SBAR(); } while (0)
#define STAMP_DECL unsigned long long st_a, st_b, st_r0, st_r1; unsigned st_sum[8] = {0, 0, 0, 0, 0, 0, 0, 0}; asm volatile("s_memrealtime %0\n\ts_waitcnt lgkmcnt(0)" : "=s"(st_r0) :: "memory");
#define STAMP_ARGS , unsigned long long& st_a, unsigned long long& st_b, unsigned (&st_sum)[8]
#define STAMP_PASS , st_a, st_b, st_sum
#define STAMP_SEG(i) do { STAMP(st_b); st_sum[i] += (unsigned)(st_b - st_a); st_a = st_b; } while (0)
#define STAMP_START() STAMP(st_a)
#define STAMP_SEGI(i)
#define STAMP_OUT(ws, kern, wid, lane) do { asm volatile("s_memrealtime %0\n\ts_waitcnt lgkmcnt(0)" : "=s"(st_r1) :: "memory"); st_sum[7] = (unsigned)(st_r1 - st_r0); if (lane == 0) { unsigned* o_ = (unsigned*)((char*)(ws) + (128u << 20) + (kern) * (1u << 20)) + (blockIdx.x * 8 + (wid)) * 8; for (int i_ = 0; i_ < 8; ++i_) o_[i_] = st_sum[i_]; } } while (0)
#define TL_ENTRY() unsigned long long tl_0; asm volatile("s_memrealtime %0\n\ts_waitcnt lgkmcnt(0)" : "=s"(tl_0) :: "memory");
#define TL_EXIT(ws, kern) do { unsigned long long tl_1; asm volatile("s_waitcnt vmcnt(0)\n\ts_memrealtime %0\n\ts_waitcnt lgkmcnt(0)" : "=s"(tl_1) :: "memory"); \
    if ((threadIdx.x & 63) == 0) { unsigned long long* o_ = (unsigned long long*)((char*)(ws) + (140u << 20) + (kern) * (1u << 20)) + (blockIdx.x * 8 + (threadIdx.x >> 6)) * 2; o_[0] = tl_0; o_[1] = tl_1; } } while (0)

DEVI bf16x8 tr_frag(LAS const char* p, int off0, int off1) {
    bf16x4 lo = __builtin_amdgcn_ds_read_tr16_b64_v4bf16((LAS bf16x4*)(p + off0));
    bf16x4 hi = __builtin_amdgcn_ds_read_tr16_b64_v4bf16((LAS bf16x4*)(p + off1));
    return __builtin_shufflevector(lo, hi, 0, 1, 2, 3, 4, 5, 6, 7);
}

DEVI void glds16(const char* sbase, unsigned voff, unsigned ldsdst) {
    unsigned keep;
    asm volatile("s_mov_b32 %0, m0\n\ts_mov_b32 m0, %3\n\ts_nop 0\n\tglobal_load_lds_dwordx4 %1, %2\n\ts_mov_b32 m0, %0"
                 : "=&s"(keep) : "v"(voff), "s"(sbase), "s"(ldsdst) : "memory");
}

__global__ __launch_bounds__(512) void k_ln(const u16* __restrict__ x, char* __restrict__ ws) {
    __shared__ float red[2][8][8];
    TL_ENTRY();
    const int tid = threadIdx.x, w = tid >> 6, l = tid & 63, r = l & 7, c8 = l >> 3;
    const int row = blockIdx.x * 8 + r;
    u32x4 v[2];
#pragma unroll
    for (int i = 0; i < 2; ++i) v[i] = *(const u32x4*)(x + (size_t)row * D_ + (16 * w + 8 * i + c8) * 8);
    float f[16];
#pragma unroll
    for (int i = 0; i < 2; ++i)
#pragma unroll
        for (int j = 0; j < 4; ++j) { f[i * 8 + 2 * j] = bf_lo(v[i][j]); f[i * 8 + 2 * j + 1] = bf_hi(v[i][j]); }
    float s = 0.f;
#pragma unroll
    for (int j = 0; j < 16; ++j) s += f[j];
    s += __shfl_xor(s, 8); s += __shfl_xor(s, 16); s += __shfl_xor(s, 32);
    if (c8 == 0) red[0][w][r] = s;
    __syncthreads();
    float tot = 0.f;
#pragma unroll
    for (int k = 0; k < 8; ++k) tot += red[0][k][r];
    const float mean = tot * (1.0f / D_);
    float q = 0.f;
#pragma unroll
    for (int j = 0; j < 16; ++j) { const float d = f[j] - mean; q += d * d; }
    q += __shfl_xor(q, 8); q += __shfl_xor(q, 16); q += __shfl_xor(q, 32);
    if (c8 == 0) red[1][w][r] = q;
    __syncthreads();
    float qt = 0.f;
#pragma unroll
    for (int k = 0; k < 8; ++k) qt += red[1][k][r];
    const float mb = rbf(mean);
    const float vb = rbf(qt * (1.0f / D_));
    const float den = rbf(sqrtf(rbf(vb + 1e-5f)));
    const float rc = 1.0f / den;
    const int mblk = row >> 4, rr = row & 15;
#pragma unroll
    for (int i = 0; i < 2; ++i) {
        u32x4 o;
#pragma unroll
        for (int j = 0; j < 4; ++j) {
            float y[2];
#pragma unroll
            for (int e = 0; e < 2; ++e) {
                const float d = rbf(f[i * 8 + 2 * j + e] - mb);
                float qq = d * rc;
                qq = fmaf(fmaf(-qq, den, d), rc, qq);
                y[e] = qq;
            }
            o[j] = pk_bf16(y[0], y[1]);
        }
        const int ch = 16 * w + 8 * i + c8, slab = ch >> 2, g = ch & 3;
        asm volatile("global_store_dwordx4 %0, %1, off sc1\n\ts_nop 1" :: "v"(ws + WS_XN + slab * SLAB_B + (size_t)mblk * 1024 + (g * 16 + rr) * 16), "v"(o) : "memory");
    }
    TL_EXIT(ws, 0);
}

constexpr int G1_AST = 17 * 1024;
constexpr int G1_BST = 21 * 1024;
constexpr int G1_REG = 7168;
constexpr int G1_BOFF = 4 * G1_AST;
constexpr int G1_XOFF = G1_BOFF + 4 * G1_BST;
constexpr int G1_LDS = G1_XOFF + 8192;

typedef unsigned long long u64;
struct G1Ld {
    const char *pa, *p2, *pg, *pu;
    unsigned stepa, step2, stepg;
    unsigned va, v2, vg;
    unsigned la, l2, s2, lg, lu;
    const char* warm; unsigned warmstep;
    char* pf;
    u64 ma, m2, mg;
};

#define WAIT_LGKM0() __builtin_amdgcn_s_waitcnt(0xC07F)
#define MEMFENCE() asm volatile("" ::: "memory")
#define OPAQUE_S(x) asm volatile("" : "+s"(x))

template <int LOFF, bool TWO>
DEVI void g1_piece(const char* base, unsigned voff, unsigned lds, u64 msk) {
    asm volatile("s_add_u32 m0, %2, %4\n\ts_mov_b64 exec, %3\n\tglobal_load_lds_dwordx4 %0, %1\n\t"
                 ".if %5\n\tglobal_load_lds_dwordx4 %0, %1 offset:1024\n\t.endif\n\ts_mov_b64 exec, -1"
                 :: "v"(voff), "s"(base), "s"(lds), "s"(msk), "i"(LOFF), "i"(TWO ? 1 : 0) : "memory", "scc");
}
template <int SLOT> DEVI void g1_pa(G1Ld& L) { g1_piece<SLOT * G1_AST, true>(L.pa, L.va, L.la, L.ma); L.pa += L.stepa; OPAQUE_S(L.pa); }
template <int SLOT> DEVI void g1_p2(G1Ld& L) { g1_piece<0, false>(L.p2, L.v2, L.l2 + SLOT * L.s2, L.m2); L.p2 += L.step2; OPAQUE_S(L.p2); }
template <int SLOT> DEVI void g1_pg(G1Ld& L) { g1_piece<SLOT * G1_BST, false>(L.pg, L.vg, L.lg, L.mg); L.pg += L.stepg; OPAQUE_S(L.pg); }
template <int SLOT> DEVI void g1_pu(G1Ld& L) { g1_piece<SLOT * G1_BST, false>(L.pu, L.vg, L.lu, L.mg); L.pu += L.stepg; OPAQUE_S(L.pu); }
template <int SLOT> DEVI void g1_issue(G1Ld& L) { g1_pa<SLOT>(L); g1_p2<SLOT>(L); g1_pg<SLOT>(L); g1_pu<SLOT>(L); }
DEVI void g1_tail(G1Ld& L, bool tail, const char* warm, unsigned warmstep) {
    L.p2 = tail ? L.p2 - L.step2 : L.p2; L.pg = tail ? L.pg - L.stepg : L.pg; L.pu = tail ? L.pu - L.stepg : L.pu;
    L.step2 = tail ? 0u : L.step2; L.stepg = tail ? 0u : L.stepg;
    L.m2 = tail ? 1ull : L.m2; L.mg = tail ? 1ull : L.mg;
    L.pa = tail ? warm : L.pa; L.stepa = tail ? warmstep : L.stepa;
}

struct G1Ptr { LAS const char *pA, *pH, *pB01, *pB23, *pBh01, *pBh23; };

template <int GRP, int SL> DEVI bf16x8 g1_ldb(const G1Ptr& P, int n) {
    LAS const char* pB = (SL < 2) ? P.pB01 : P.pB23;
    const int off = (SL & 1) * G1_BST + (GRP == 0 ? (n < 6 ? n * 32 : (n < 8 ? G1_REG + (n - 6) * 32 : 2 * G1_REG + (n - 8) * 32))
                                                  : (n < 5 ? G1_REG + (2 + n) * 32 : 2 * G1_REG + (n - 3) * 32));
    return tr_frag(pB, off, off + 8 * 224);
}
template <int SL> DEVI bf16x8 g1_lda(const G1Ptr& P, int m) { return *(LAS const bf16x8*)(P.pA + SL * G1_AST + (1 + m) * 1024); }
template <int SL> DEVI void g1_ldh(const G1Ptr& P, bf16x8& Ah, bf16x8& Bh) {
    Ah = *(LAS const bf16x8*)(P.pH + SL * G1_AST);
    Bh = tr_frag((SL < 2) ? P.pBh01 : P.pBh23, (SL & 1) * G1_BST, (SL & 1) * G1_BST + 8 * 224);
}

template <int GRP, int ST>
DEVI void g1_stage(f32x4 (&acc)[4][10], f32x4& acch, bf16x8 (&A)[4], bf16x8 (&B)[10], bf16x8& Ah, bf16x8& Bh, G1Ld& L, bool tail, const G1Ptr& P STAMP_ARGS) {
    constexpr int NX = (ST + 1) & 3, IS = (ST + 3) & 3;
#define MM(m, n) do { if (GRP == 0 && ((n) == 4 || (n) == 5)) acc[m][n] = MFMA16(A[m], B[n], acc[m][n]); else acc[m][n] = MFMA16(B[n], A[m], acc[m][n]); SBAR(); } while (0)
#define RB(n) do { B[n] = g1_ldb<GRP, NX>(P, n); SBAR(); } while (0)
#define RA(m) do { A[m] = g1_lda<NX>(P, m); SBAR(); } while (0)
#define G0(x) do { if (GRP == 0) { x; SBAR(); } } while (0)
    MM(0, 0); MM(1, 0); G0(g1_pa<IS>(L));
    MM(1, 1); MM(0, 1); G0(g1_p2<IS>(L));
    MM(0, 2); MM(1, 2); G0(g1_pg<IS>(L));
    MM(1, 3); MM(0, 3); G0(g1_pu<IS>(L));
    MM(0, 4); MM(1, 4);
    G0(acch = MFMA16(Bh, Ah, acch));
    asm volatile("s_waitcnt vmcnt(10)" ::: "memory");
    WAIT_LGKM0();
    MEMFENCE(); SBAR();
    BAR();
    MEMFENCE(); SBAR();
    if (GRP == 1) {
        if (ST == 0) g1_tail(L, tail, L.warm, L.warmstep);
        g1_issue<ST>(L);
        if (ST == 2) {
            const u32x4 ones = {0xffffffffu, 0xffffffffu, 0xffffffffu, 0xffffffffu};
            asm volatile("global_store_dwordx4 %0, %1, %2 sc1\n\ts_nop 1" :: "v"(L.va), "v"(ones), "s"(L.pf) : "memory");
            L.pf += 1024; OPAQUE_S(L.pf);
        }
        SBAR();
    }
    MM(2, 0); MM(3, 0); RB(0);
    MM(3, 1); MM(2, 1); RB(1);
    MM(2, 2); MM(3, 2); RB(2);
    MM(3, 3); MM(2, 3); RB(3);
    MM(2, 4); MM(3, 4); RB(4);
    MM(0, 5); MM(1, 5); G0(g1_ldh<NX>(P, Ah, Bh));
    MM(1, 6); MM(0, 6);
    MM(0, 7); MM(1, 7);
    MM(1, 8); MM(0, 8);
    MM(0, 9); RA(0); MM(1, 9); RA(1);
    MM(2, 5); MM(3, 5); RB(5);
    MM(3, 6); MM(2, 6); RB(6);
    MM(2, 7); MM(3, 7); RB(7);
    MM(3, 8); MM(2, 8); RB(8);
    MM(2, 9); RA(2); MM(3, 9); RA(3); RB(9);
#undef MM
#undef RB
#undef RA
#undef G0
}

DEVI void st16(char* dst, u32x4 v) {
    asm volatile("global_store_dwordx4 %0, %1, off sc1\n\ts_nop 1" :: "v"(dst), "v"(v) : "memory");
}

DEVI unsigned glu2(float g0, float g1, float u0, float u1) {
    const f32x2 t = (f32x2){g0, g1} * -1.4426950408889634f;
    const f32x2 d = (f32x2){__builtin_amdgcn_exp2f(t[0]), __builtin_amdgcn_exp2f(t[1])} + 1.0f;
    const f32x2 h = (f32x2){u0, u1} * (f32x2){__builtin_amdgcn_rcpf(d[0]), __builtin_amdgcn_rcpf(d[1])};
    return pk_bf16(h[0], h[1]);
}

DEVI void glu_store(const f32x4& g0, const f32x4& u0, const f32x4& g1, const f32x4& u1, char* dst) {
    unsigned a0 = glu2(g0[0], g0[1], u0[0], u0[1]), a1 = glu2(g0[2], g0[3], u0[2], u0[3]);
    unsigned b0 = glu2(g1[0], g1[1], u1[0], u1[1]), b1 = glu2(g1[2], g1[3], u1[2], u1[3]);
    auto r0 = __builtin_amdgcn_permlane16_swap(a0, b0, false, false);
    auto r1 = __builtin_amdgcn_permlane16_swap(a1, b1, false, false);
    st16(dst, (u32x4){r0[0], r1[0], r0[1], r1[1]});
}

template <int GRP>
DEVI void g1_body(G1Ld& L, LAS char* lds, char* ws, int rt, int ct, int wm, int lane) {
    const int g = lane >> 4, i16 = lane & 15, q = i16 >> 2, p = lane & 3;
    G1Ptr P;
    P.pA = lds + wm * 4096 + lane * 16;
    P.pH = lds + lane * 16;
    const int brow = (16 * (g >> 1) + 4 * (g & 1) + q) * 224 + p * 8;
    P.pB01 = lds + G1_BOFF + brow;
    P.pB23 = P.pB01 + 2 * G1_BST;
    P.pBh01 = P.pB01 + (2 + wm) * 32;
    P.pBh23 = P.pB23 + (2 + wm) * 32;
    f32x4 acc[4][10], acch = {0.f, 0.f, 0.f, 0.f};
#pragma unroll
    for (int m = 0; m < 4; ++m)
#pragma unroll
        for (int n = 0; n < 10; ++n) acc[m][n] = (f32x4){0.f, 0.f, 0.f, 0.f};

    STAMP_DECL STAMP_START();
    g1_issue<0>(L); g1_issue<1>(L);
    asm volatile("s_waitcnt vmcnt(5)" ::: "memory");
    SBAR(); BAR(); MEMFENCE(); SBAR();
    bf16x8 A[4], B[10], Ah, Bh;
#pragma unroll
    for (int n = 0; n < 10; ++n) { if (n < 4) A[n] = g1_lda<0>(P, n); B[n] = g1_ldb<GRP, 0>(P, n); }
    if (GRP == 0) g1_ldh<0>(P, Ah, Bh);
    SBAR();
    g1_issue<2>(L);
    if (GRP == 1) g1_issue<3>(L);
    SBAR();
    STAMP_SEG(0);
#pragma unroll 1
    for (int it = 0; it < 8; ++it) {
        g1_stage<GRP, 0>(acc, acch, A, B, Ah, Bh, L, it == 7, P STAMP_PASS);
        if (GRP == 0) g1_tail(L, it == 7, L.warm, L.warmstep);
        g1_stage<GRP, 1>(acc, acch, A, B, Ah, Bh, L, it == 7, P STAMP_PASS);
        g1_stage<GRP, 2>(acc, acch, A, B, Ah, Bh, L, it == 7, P STAMP_PASS);
        g1_stage<GRP, 3>(acc, acch, A, B, Ah, Bh, L, it == 7, P STAMP_PASS);
    }
    STAMP_SEG(1);

    char* ho = ws + WS_HO;
    const int mb0 = 16 * rt + 4 * wm;
    const unsigned hl = (unsigned)((g & 1) * 1024 + (g >> 1) * 256 + i16 * 16);
    if (GRP == 1) {
        SBAR(); BAR(); SBAR();
#pragma unroll
        for (int n = 0; n < 5; ++n) {
            const int cb = 7 * ct + 2 + n;
            char* dst = ho + (size_t)(32 + (cb >> 1)) * SLAB_B + (cb & 1) * 512 + (size_t)mb0 * 1024 + hl;
#pragma unroll
            for (int m = 0; m < 4; m += 2) glu_store(acc[m][n], acc[m][5 + n], acc[m + 1][n], acc[m + 1][5 + n], dst + m * 1024);
        }
    } else {
        u32x4 Qf[4], Kf[4]; u32x2 Vh[4][2];
#pragma unroll
        for (int m = 0; m < 4; ++m) {
            Qf[m] = (u32x4){pk_bf16(acc[m][0][0], acc[m][0][1]), pk_bf16(acc[m][0][2], acc[m][0][3]), pk_bf16(acc[m][1][0], acc[m][1][1]), pk_bf16(acc[m][1][2], acc[m][1][3])};
            Kf[m] = (u32x4){pk_bf16(acc[m][2][0], acc[m][2][1]), pk_bf16(acc[m][2][2], acc[m][2][3]), pk_bf16(acc[m][3][0], acc[m][3][1]), pk_bf16(acc[m][3][2], acc[m][3][3])};
#pragma unroll
            for (int b = 0; b < 2; ++b) Vh[m][b] = (u32x2){pk_bf16(acc[m][4 + b][0], acc[m][4 + b][1]), pk_bf16(acc[m][4 + b][2], acc[m][4 + b][3])};
        }
        LAS char* xs = lds + G1_XOFF;
        if (wm < 3) {
            *(LAS u32x4*)(xs + (wm + 1) * 2048 + lane * 16) = Kf[3];
            *(LAS u32x4*)(xs + (wm + 1) * 2048 + 1024 + lane * 16) = (u32x4){Vh[3][0][0], Vh[3][0][1], Vh[3][1][0], Vh[3][1][1]};
        }
        {
            const unsigned h0 = pk_bf16(acch[0], acch[1]), h1 = pk_bf16(acch[2], acch[3]);
            if (wm < 2) {
                *(LAS u32x2*)(xs + lane * 16 + wm * 8) = (u32x2){h0, h1};
            } else {
                LAS char* t = xs + 1024 + ((i16 >> 2) * 16 + 4 * g) * 16 + (wm - 2) * 8 + (i16 & 3) * 2;
                *(LAS u16*)(t) = (u16)h0; *(LAS u16*)(t + 16) = (u16)(h0 >> 16); *(LAS u16*)(t + 32) = (u16)h1; *(LAS u16*)(t + 48) = (u16)(h1 >> 16);
            }
        }
        asm volatile("s_waitcnt lgkmcnt(0)" ::: "memory");
        SBAR(); BAR(); SBAR();
        u32x4 Kp = *(LAS const u32x4*)(xs + wm * 2048 + lane * 16);
        u32x4 Vp = *(LAS const u32x4*)(xs + wm * 2048 + 1024 + lane * 16);
#pragma unroll
        for (int n = 0; n < 2; ++n) {
            const int cb = 7 * ct + n;
            char* dst = ho + (size_t)(32 + (cb >> 1)) * SLAB_B + (cb & 1) * 512 + (size_t)mb0 * 1024 + hl;
#pragma unroll
            for (int m = 0; m < 4; m += 2) glu_store(acc[m][6 + n], acc[m][8 + n], acc[m + 1][6 + n], acc[m + 1][8 + n], dst + m * 1024);
        }
        const bool odd = (i16 & 1) != 0;
        const int par = i16 & 1;
        const int dp0 = 16 + i16 - 4 * g - par, dc0 = i16 - 4 * g - par;
        const bool vp0 = dp0 <= 14, vp1 = dp0 - 2 <= 14, vc0 = dc0 >= 0, vc1 = dc0 - 2 >= 0;
        const float NEG = -__builtin_inff();
        const float sc = 0.17677669529663687f * 1.4426950408889634f;
        const bool first = (rt == 0) && (wm == 0);
        char* od = ho + (size_t)ct * SLAB_B + (size_t)mb0 * 1024 + (((g & 1) << 1) | (g >> 1)) * 256 + i16 * 16;
#pragma unroll
        for (int m = 0; m < 4; ++m) {
            const f32x4 z = {0.f, 0.f, 0.f, 0.f};
            f32x4 s0 = MFMA16(as_frag(Kp), as_frag(Qf[m]), z);
            f32x4 s1 = MFMA16(as_frag(Kf[m]), as_frag(Qf[m]), z);
            float a0 = (odd ? s0[1] : s0[0]) * sc, a1 = (odd ? s0[3] : s0[2]) * sc;
            float b0 = (odd ? s1[1] : s1[0]) * sc, b1 = (odd ? s1[3] : s1[2]) * sc;
            if (m == 0 && first) { a0 = -1e9f * 1.4426950408889634f; a1 = a0; }
            a0 = vp0 ? a0 : NEG; a1 = vp1 ? a1 : NEG; b0 = vc0 ? b0 : NEG; b1 = vc1 ? b1 : NEG;
            float mx = fmaxf(fmaxf(a0, a1), fmaxf(b0, b1));
            { auto t = __builtin_amdgcn_permlane16_swap(__float_as_uint(mx), __float_as_uint(mx), false, false); mx = fmaxf(__uint_as_float(t[0]), __uint_as_float(t[1])); }
            { auto t = __builtin_amdgcn_permlane32_swap(__float_as_uint(mx), __float_as_uint(mx), false, false); mx = fmaxf(__uint_as_float(t[0]), __uint_as_float(t[1])); }
            a0 = __builtin_amdgcn_exp2f(a0 - mx); a1 = __builtin_amdgcn_exp2f(a1 - mx); b0 = __builtin_amdgcn_exp2f(b0 - mx); b1 = __builtin_amdgcn_exp2f(b1 - mx);
            float sm = (a0 + a1) + (b0 + b1);
            { auto t = __builtin_amdgcn_permlane16_swap(__float_as_uint(sm), __float_as_uint(sm), false, false); sm = __uint_as_float(t[0]) + __uint_as_float(t[1]); }
            { auto t = __builtin_amdgcn_permlane32_swap(__float_as_uint(sm), __float_as_uint(sm), false, false); sm = __uint_as_float(t[0]) + __uint_as_float(t[1]); }
            const float rs = __builtin_amdgcn_rcpf(sm);
            a0 *= rs; a1 *= rs; b0 *= rs; b1 *= rs;
            const u32x4 Pf = {odd ? pk_bf16(0.f, a0) : pk_bf16(a0, 0.f), odd ? pk_bf16(0.f, a1) : pk_bf16(a1, 0.f),
                              odd ? pk_bf16(0.f, b0) : pk_bf16(b0, 0.f), odd ? pk_bf16(0.f, b1) : pk_bf16(b1, 0.f)};
            const u32x4 V0 = {Vp[0], Vp[1], Vh[m][0][0], Vh[m][0][1]}, V1 = {Vp[2], Vp[3], Vh[m][1][0], Vh[m][1][1]};
            const f32x4 o0 = MFMA16(as_frag(V0), as_frag(Pf), z);
            const f32x4 o1 = MFMA16(as_frag(V1), as_frag(Pf), z);
            unsigned x0 = pk_bf16(o0[0], o0[1]), x1 = pk_bf16(o0[2], o0[3]), y0 = pk_bf16(o1[0], o1[1]), y1 = pk_bf16(o1[2], o1[3]);
            auto r0 = __builtin_amdgcn_permlane16_swap(x0, y0, false, false);
            auto r1 = __builtin_amdgcn_permlane16_swap(x1, y1, false, false);
            st16(od + m * 1024, (u32x4){r0[0], r1[0], r0[1], r1[1]});
            Kp = Kf[m];
            Vp = (u32x4){Vh[m][0][0], Vh[m][0][1], Vh[m][1][0], Vh[m][1][1]};
        }
    }
    STAMP_SEG(5);
    asm volatile("s_waitcnt vmcnt(0)" ::: "memory");
    STAMP_SEG(6);
    STAMP_OUT(ws, 0, GRP * 4 + wm, lane);
}

__global__ __launch_bounds__(512, 2) void k_gemm1(const char* __restrict__ wqkv, const char* __restrict__ wg, const char* __restrict__ wu, const char* __restrict__ wo, const char* __restrict__ wd, char* __restrict__ ws) {
    extern __shared__ __attribute__((aligned(16))) char smem[];
    TL_ENTRY();
    LAS char* lds = (LAS char*)smem;
    const int tid = threadIdx.x, lane = tid & 63;
    const int wid = __builtin_amdgcn_readfirstlane(tid >> 6);
    const int bid = blockIdx.x, xcd = bid & 7, idx = bid >> 3;
    const int ct = 4 * xcd + (idx & 3), rt = idx >> 2;
    const unsigned ldsb = (unsigned)(size_t)lds;
    G1Ld L;
    {
        const int c = wid * 64 + lane, rho = c / 14, cc = c - rho * 14;
        const int k = (rho & ~12) | ((rho & 4) << 1) | ((rho & 8) >> 1);
        const int c2 = cc < 12 ? cc : 11;
        const bool last = wid == 7;
        L.pa = ws + WS_XN + (size_t)(16 * rt + 2 * wid) * 1024;
        L.stepa = (unsigned)SLAB_B;
        L.va = lane * 16;
        L.la = ldsb + (1 + 2 * wid) * 1024;
        L.p2 = last ? ws + WS_XN + (size_t)(rt == 0 ? 0 : 16 * rt - 1) * 1024 : wqkv + ct * 64;
        L.step2 = last ? (unsigned)SLAB_B : 32u * 3072u * 2u;
        L.v2 = last ? (unsigned)((rt == 0 ? (lane & 48) : lane) * 16) : (unsigned)(k * 6144 + (c2 >> 2) * 2048 + (c2 & 3) * 16);
        L.l2 = last ? ldsb : ldsb + G1_BOFF + wid * 1024;
        L.s2 = last ? G1_AST : G1_BST;
        L.pg = wg + ct * 224;
        L.pu = wu + ct * 224;
        L.stepg = 32u * 3584u * 2u;
        L.vg = last ? 0u : (unsigned)(k * 7168 + cc * 16);
        L.lg = last ? ldsb + G1_BOFF + 192 : ldsb + G1_BOFF + G1_REG + wid * 1024;
        L.lu = last ? ldsb + G1_BOFF + 192 : ldsb + G1_BOFF + 2 * G1_REG + wid * 1024;
        L.ma = ~0ull; L.m2 = ~0ull; L.mg = last ? 1ull : ~0ull;
    }
    const int wdo = bid * 28672 < 3584 * 2048 - 32768 ? bid * 28672 : 3584 * 2048 - 32768;
    L.warm = wid < 4 ? wo + bid * 8192 + wid * 2048 : wd + wdo + (wid & 3) * 2048;
    L.warmstep = wid < 4 ? 0u : 8192u;
    L.pf = ws + WS_XB + (size_t)bid * 32768 + (wid & 3) * 8192;
    if (wid < 4) g1_body<0>(L, lds, ws, rt, ct, wid & 3, lane);
    else g1_body<1>(L, lds, ws, rt, ct, wid & 3, lane);
    TL_EXIT(ws, 1);
}

#define POLL_SLEEP 2
constexpr int G2_ST = 16384;
constexpr int G2_NS = 6;
constexpr int G2_LDS = G2_NS * G2_ST;

struct G2St {
    const char* pa;
    const char* pw;
    const char* wdown;
    unsigned va, vw0, vw1;
    unsigned ldsw;
    int wst;
};
template <int LOFF> DEVI void g2_glds(const char* base, unsigned voff, unsigned lds) {
    asm volatile("s_add_u32 m0, %2, %3\n\ts_nop 0\n\tglobal_load_lds_dwordx4 %0, %1" :: "v"(voff), "s"(base), "s"(lds), "i"(LOFF) : "memory", "scc");
}
template <int OFF> DEVI void g2_lda(u32x4& dst, const char* base, unsigned voff) {
    asm volatile("global_load_dwordx4 %0, %1, %2 offset:%3" : "=v"(dst) : "v"(voff), "s"(base), "i"(OFF) : "memory");
}
template <int SLOT, int I> DEVI void g2_w(G2St& T) {
    if (I == 0) g2_glds<SLOT * G2_ST>(T.pw, T.vw0, T.ldsw);
    else { g2_glds<SLOT * G2_ST + 8192>(T.pw, T.vw1, T.ldsw); T.pw = (T.wst == 15) ? T.wdown : T.pw + 64 * 2048; T.wst += 1; OPAQUE_S(T.pw); }
}
template <int I> DEVI void g2_a(G2St& T, u32x4 (&A)[2]) {
    if (I == 0) g2_lda<0>(A[0], T.pa, T.va);
    else { g2_lda<1024>(A[1], T.pa, T.va); T.pa += 2 * SLAB_B; OPAQUE_S(T.pa); }
}
template <int SLOT> DEVI void g2_group(G2St& T, u32x4 (&A)[2]) { g2_w<SLOT, 0>(T); g2_w<SLOT, 1>(T); g2_a<0>(T, A); g2_a<1>(T, A); }

template <int SLOT> DEVI bf16x8 g2_ldb(LAS const char* (&pB)[2][4], int n) {
    return tr_frag(pB[SLOT / 3][n & 3], (SLOT % 3) * G2_ST + (n >> 2) * 8192, (SLOT % 3) * G2_ST + (n >> 2) * 8192 + 512);
}

template <int U, int WAIT, bool ISW, bool ISA, bool RL>
DEVI void g2_stage(f32x4 (&acc)[2][8], G2St& T, u32x4 (&Ac)[2], u32x4 (&An)[2], bf16x8 (&B)[8], LAS const char* (&pB)[2][4] STAMP_ARGS) {
    constexpr int NX = (U + 1) % G2_NS, IS = (U + 5) % G2_NS;
    asm volatile("s_waitcnt vmcnt(%2)" : "+v"(Ac[0]), "+v"(Ac[1]) : "i"(WAIT) : "memory");
    STAMP_SEGI(1);
    SBAR(); BAR(); MEMFENCE(); SBAR();
    STAMP_SEGI(2);
#define P2(n) do { acc[(n) & 1][n] = MFMA16(B[n], as_frag(Ac[(n) & 1]), acc[(n) & 1][n]); SBAR(); acc[~(n) & 1][n] = MFMA16(B[n], as_frag(Ac[~(n) & 1]), acc[~(n) & 1][n]); SBAR(); \
                   if (RL) { B[n] = g2_ldb<NX>(pB, n); SBAR(); } } while (0)
    P2(0); if (ISW) { g2_w<IS, 0>(T); SBAR(); }
    P2(1);
    P2(2); if (ISW) { g2_w<IS, 1>(T); SBAR(); }
    P2(3);
    P2(4); if (ISA) { g2_a<0>(T, An); SBAR(); }
    P2(5);
    P2(6); if (ISA) { g2_a<1>(T, An); SBAR(); }
    P2(7);
#undef P2
    STAMP_SEGI(3);
}

__global__ __launch_bounds__(512, 2) void k_gemm2(const u16* __restrict__ x, const char* __restrict__ wo, const char* __restrict__ wd, char* __restrict__ ws, u16* __restrict__ out) {
    extern __shared__ __attribute__((aligned(16))) char smem[];
    TL_ENTRY();
    LAS char* lds = (LAS char*)smem;
    const int tid = threadIdx.x, lane = tid & 63;
    const int wid = __builtin_amdgcn_readfirstlane(tid >> 6);
    const int kg = wid >> 2, wm = wid & 3;
    const int bid = blockIdx.x, kh = bid & 1, quad = (bid >> 1) & 3, idx = bid >> 3;
    const int rt = 8 * (quad >> 1) + (idx >> 2), ct = 4 * (quad & 1) + (idx & 3);
    const int g = lane >> 4, i16 = lane & 15, q = i16 >> 2, p = lane & 3;

    G2St T;
    T.pa = ws + WS_HO + (size_t)(72 * kh + kg) * SLAB_B + (size_t)(8 * rt + 2 * wm) * 1024;
    T.va = lane * 16;
    T.wdown = wd + ct * 256;
    T.pw = kh ? T.wdown + (size_t)(2304 - 1024) * 2048 : wo + ct * 256;
    T.wst = 36 * kh;
    {
        const int r = lane >> 3, cp = lane & 7, c = cp ^ ((r & 2) | ((wid & 1) << 2));
        const int keep = kh ^ kg ^ 1;
        T.vw0 = (unsigned)((8 * wid + r) * 2048 + c * 16 + keep * 128);
        T.vw1 = T.vw0 ^ 128;
    }
    T.ldsw = (unsigned)(size_t)lds + wid * 1024;

    STAMP_DECL STAMP_START();
    u32x4 A0[2], A1[2], A2[2], A3[2], A4[2], A5[2];
    g2_w<0, 0>(T); g2_w<0, 1>(T);
    g2_group<1>(T, A0);

    LAS const char* pB[2][4];
#pragma unroll
    for (int n = 0; n < 4; ++n) {
        pB[0][n] = lds + kg * 4096 + (8 * g + q) * 128 + (((2 * n + (p >> 1)) ^ ((q & 2) | ((g & 1) << 2))) * 16) + (p & 1) * 8;
        pB[1][n] = pB[0][n] + 3 * G2_ST;
    }
    f32x4 acc[2][8];
#pragma unroll
    for (int m = 0; m < 2; ++m)
#pragma unroll
        for (int n = 0; n < 8; ++n) acc[m][n] = (f32x4){0.f, 0.f, 0.f, 0.f};
    g2_group<2>(T, A1);
    asm volatile("s_waitcnt vmcnt(8)" ::: "memory");
    SBAR(); BAR(); MEMFENCE(); SBAR();
    bf16x8 B[8];
#pragma unroll
    for (int n = 0; n < 8; ++n) B[n] = g2_ldb<0>(pB, n);
    SBAR();
    g2_group<3>(T, A2); g2_group<4>(T, A3);
    SBAR();
    STAMP_SEG(0);
#pragma unroll 1
    for (int it = 0; it < 5; ++it) {
        g2_stage<0, 12, true, true, true>(acc, T, A0, A4, B, pB STAMP_PASS);
        g2_stage<1, 12, true, true, true>(acc, T, A1, A5, B, pB STAMP_PASS);
        g2_stage<2, 12, true, true, true>(acc, T, A2, A0, B, pB STAMP_PASS);
        g2_stage<3, 12, true, true, true>(acc, T, A3, A1, B, pB STAMP_PASS);
        g2_stage<4, 12, true, true, true>(acc, T, A4, A2, B, pB STAMP_PASS);
        g2_stage<5, 12, true, true, true>(acc, T, A5, A3, B, pB STAMP_PASS);
    }
    g2_stage<0, 12, true, true, true>(acc, T, A0, A4, B, pB STAMP_PASS);
    g2_stage<1, 12, false, true, true>(acc, T, A1, A5, B, pB STAMP_PASS);
    const int orow = 128 * rt + 32 * wm + i16, ocol = 128 * ct + 64 * kh;
    u32x4 xr[4];
    {
        const u16* xp = x + (size_t)orow * D_ + ocol + (g & 1) * 16 + (g >> 1) * 8;
        const u16* xq = xp + 16 * D_;
        asm volatile("global_load_dwordx4 %0, %4, off\n\tglobal_load_dwordx4 %1, %4, off offset:64\n\tglobal_load_dwordx4 %2, %5, off\n\tglobal_load_dwordx4 %3, %5, off offset:64"
                     : "=&v"(xr[0]), "=&v"(xr[1]), "=&v"(xr[2]), "=&v"(xr[3]) : "v"(xp), "v"(xq) : "memory");
    }
    g2_stage<2, 14, false, false, true>(acc, T, A2, A0, B, pB STAMP_PASS);
    g2_stage<3, 10, false, false, true>(acc, T, A3, A1, B, pB STAMP_PASS);
    g2_stage<4, 6, false, false, true>(acc, T, A4, A2, B, pB STAMP_PASS);
    g2_stage<5, 4, false, false, false>(acc, T, A5, A3, B, pB STAMP_PASS);
    STAMP_SEG(3);

    {
        LAS char* xs = lds + wid * 8192 + lane * 16;
#pragma unroll
        for (int m = 0; m < 2; ++m)
#pragma unroll
            for (int j = 0; j < 4; ++j) *(LAS f32x4*)(xs + (m * 4 + j) * 1024) = acc[m][4 + j];
    }
    asm volatile("s_waitcnt vmcnt(0)" : "+v"(xr[0]), "+v"(xr[1]), "+v"(xr[2]), "+v"(xr[3]) :: "memory");
    WAIT_LGKM0();
    MEMFENCE(); SBAR(); BAR(); MEMFENCE(); SBAR();
    LAS const char* xl = lds + (wid ^ 4) * 8192 + lane * 16;
    char* xb = ws + WS_XB + wm * 8192 + lane * 16;
    if (kg == 0) {
        char* dst = xb + (size_t)bid * 32768;
#pragma unroll
        for (int m = 0; m < 2; ++m)
#pragma unroll
            for (int j = 0; j < 4; ++j) {
                u32x4 w = __builtin_bit_cast(u32x4, acc[m][j] + *(LAS const f32x4*)(xl + (m * 4 + j) * 1024));
#pragma unroll
                for (int e = 0; e < 4; ++e) w[e] = w[e] < 0xfffffffeu ? w[e] : 0xfffffffeu;
                asm volatile("global_store_dwordx4 %0, %1, off offset:%2 sc1\n\ts_nop 1" :: "v"(dst + m * 4096), "v"(w), "i"(j * 1024) : "memory");
            }
        STAMP_SEG(4);
    } else {
#pragma unroll
        for (int m = 0; m < 2; ++m)
#pragma unroll
            for (int j = 0; j < 4; ++j) acc[m][j] += *(LAS const f32x4*)(xl + (m * 4 + j) * 1024);
        STAMP_SEG(4);
        u32x4 rc[8];
        const char* src = xb + (size_t)(bid ^ 1) * 32768;
        unsigned spin = 0;
        {
            const char* last = src - lane * 16 + 8188;
            for (;; ++spin) {
                unsigned w;
                asm volatile("global_load_dword %0, %1, off sc1\n\ts_waitcnt vmcnt(0)" : "=&v"(w) : "v"(last) : "memory");
                if (__builtin_amdgcn_readfirstlane(w) != 0xffffffffu || spin > (1u << 22)) break;
                __builtin_amdgcn_s_sleep(POLL_SLEEP);
            }
        }
        for (;; ++spin) {
            asm volatile("global_load_dwordx4 %0, %8, off sc1\n\tglobal_load_dwordx4 %1, %8, off offset:1024 sc1\n\tglobal_load_dwordx4 %2, %8, off offset:2048 sc1\n\tglobal_load_dwordx4 %3, %8, off offset:3072 sc1\n\t"
                         "global_load_dwordx4 %4, %9, off sc1\n\tglobal_load_dwordx4 %5, %9, off offset:1024 sc1\n\tglobal_load_dwordx4 %6, %9, off offset:2048 sc1\n\tglobal_load_dwordx4 %7, %9, off offset:3072 sc1\n\ts_waitcnt vmcnt(0)"
                         : "=&v"(rc[0]), "=&v"(rc[1]), "=&v"(rc[2]), "=&v"(rc[3]), "=&v"(rc[4]), "=&v"(rc[5]), "=&v"(rc[6]), "=&v"(rc[7]) : "v"(src), "v"(src + 4096) : "memory");
            unsigned mx = 0;
#pragma unroll
            for (int j = 0; j < 8; ++j) { const unsigned a = rc[j][0] > rc[j][1] ? rc[j][0] : rc[j][1], b = rc[j][2] > rc[j][3] ? rc[j][2] : rc[j][3]; mx = mx > a ? mx : a; mx = mx > b ? mx : b; }
            if (!__any(mx == 0xffffffffu) || spin > (1u << 22)) break;
            __builtin_amdgcn_s_sleep(4);
        }
        u16* orp = out + (size_t)orow * D_ + ocol + (g & 1) * 16 + (g >> 1) * 8;
#pragma unroll
        for (int m = 0; m < 2; ++m)
#pragma unroll
            for (int j = 0; j < 4; j += 2) {
                const f32x4 s0 = acc[m][j] + __builtin_bit_cast(f32x4, rc[m * 4 + j]), s1 = acc[m][j + 1] + __builtin_bit_cast(f32x4, rc[m * 4 + j + 1]);
                float v[8];
#pragma unroll
                for (int e = 0; e < 4; ++e) { auto r = __builtin_amdgcn_permlane16_swap(__float_as_uint(s0[e]), __float_as_uint(s1[e]), false, false); v[e] = __uint_as_float(r[0]); v[4 + e] = __uint_as_float(r[1]); }
                const u32x4 xv = xr[m * 2 + (j >> 1)];
                u32x4 o;
#pragma unroll
                for (int e = 0; e < 4; ++e) o[e] = pk_bf16(v[2 * e] + bf_lo(xv[e]), v[2 * e + 1] + bf_hi(xv[e]));
                *(u32x4*)(orp + m * 16 * D_ + 16 * j) = o;
            }
    }
    STAMP_SEG(5);
    asm volatile("s_waitcnt vmcnt(0)" ::: "memory");
    STAMP_SEG(6);
    STAMP_OUT(ws, 1, wid, lane);
    TL_EXIT(ws, 2);
}

__global__ void k_probe(char* ws) {
    __shared__ unsigned long long sh[6][256];
    const int t = threadIdx.x;
    for (int k = 0; k < 3; ++k) {
        const unsigned long long* p = (const unsigned long long*)(ws + (140u << 20) + k * (1u << 20));
        unsigned long long e = ~0ull, x = 0;
        for (int i = t; i < 2048; i += 256) { e = e < p[2 * i] ? e : p[2 * i]; x = x > p[2 * i + 1] ? x : p[2 * i + 1]; }
        sh[2 * k][t] = e; sh[2 * k + 1][t] = x;
    }
    unsigned long long seg = 0;
    { const unsigned* q = (const unsigned*)(ws + (128u << 20) + PROBE_KERN * (1u << 20)); seg = q[(t * 8 + 4) * 8 + PROBE_SEG]; }
    __shared__ unsigned long long sg[256]; sg[t] = seg;
    __syncthreads();
    if (t == 0) {
        unsigned long long v[6], s = 0;
        for (int k = 0; k < 6; ++k) { v[k] = sh[k][0]; for (int i = 1; i < 256; ++i) v[k] = (k & 1) ? (v[k] > sh[k][i] ? v[k] : sh[k][i]) : (v[k] < sh[k][i] ? v[k] : sh[k][i]); }
        for (int i = 0; i < 256; ++i) s += sg[i];
        unsigned long long d = PROBE_I < 0 ? 0 : (PROBE_I < 5 ? v[PROBE_I + 1] - v[PROBE_I] : s / 256 / PROBE_DIV);
        if (d > 20000) d = 20000;
        const unsigned long long t0 = __builtin_amdgcn_s_memrealtime();
        while (__builtin_amdgcn_s_memrealtime() - t0 < d * PROBE_K) __builtin_amdgcn_s_sleep(1);
    }
}

extern "C" void kernel_launch(void* const* d_in, const int* in_sizes, int n_in,
                              void* d_out, int out_size, void* d_ws, size_t ws_size,
                              hipStream_t stream) {
    const u16* x = (const u16*)d_in[0];
    const char* wqkv = (const char*)d_in[1];
    const char* wo = (const char*)d_in[2];
    const char* wg = (const char*)d_in[4];
    const char* wu = (const char*)d_in[5];
    const char* wd = (const char*)d_in[6];
    char* ws = (char*)d_ws;
    (void)hipFuncSetAttribute((const void*)k_gemm1, hipFuncAttributeMaxDynamicSharedMemorySize, G1_LDS);
    (void)hipFuncSetAttribute((const void*)k_gemm2, hipFuncAttributeMaxDynamicSharedMemorySize, G2_LDS);
    k_ln<<<256, 512, 0, stream>>>(x, ws);
    k_gemm1<<<256, 512, G1_LDS, stream>>>(wqkv, wg, wu, wo, wd, ws);
    k_gemm2<<<256, 512, G2_LDS, stream>>>(x, wo, wd, ws, (u16*)d_out);
    k_probe<<<1, 256, 0, stream>>>(ws);
}
```

```cpp
#define STAMPS
#define AB_NOINNER
#define PROBE_I -1
#define PROBE_K 1
#define PROBE_KERN 0
#define PROBE_SEG 0
#define PROBE_DIV 10
#define PROBE_WAVE 4
#include <hip/hip_runtime.h>
#include <hip/hip_bf16.h>
#include <stdint.h>

typedef __bf16 bf16x8 __attribute__((ext_vector_type(8)));
typedef __bf16 bf16x4 __attribute__((ext_vector_type(4)));
typedef __bf16 bf16x2 __attribute__((ext_vector_type(2)));
typedef float f32x4 __attribute__((ext_vector_type(4)));
typedef float f32x2 __attribute__((ext_vector_type(2)));
typedef unsigned u32x4 __attribute__((ext_vector_type(4)));
typedef unsigned u32x2 __attribute__((ext_vector_type(2)));
typedef unsigned short u16;
#define LAS __attribute__((address_space(3)))
#define GAS __attribute__((address_space(1)))
#define DEVI __device__ __forceinline__

constexpr int S_ = 2048, D_ = 1024, F_ = 3584;
constexpr int MB_ = S_ / 16;
constexpr size_t SLAB_B = (size_t)MB_ * 1024;
constexpr size_t WS_XN = 0;
constexpr size_t WS_HO = 32 * SLAB_B;
constexpr size_t WS_XB = WS_HO + 144 * SLAB_B;

DEVI unsigned pk_bf16(float lo, float hi) {
    bf16x2 v = {(__bf16)lo, (__bf16)hi};
    return __builtin_bit_cast(unsigned, v);
}
DEVI float bf_lo(unsigned u) { return __uint_as_float(u << 16); }
DEVI float bf_hi(unsigned u) { return __uint_as_float(u & 0xffff0000u); }
DEVI float rbf(float f) { return (float)(__bf16)f; }
DEVI bf16x8 as_frag(u32x4 v) { return __builtin_bit_cast(bf16x8, v); }
#define MFMA16(a, b, c) __builtin_amdgcn_mfma_f32_16x16x32_bf16((a), (b), (c), 0, 0, 0)
#define SBAR() __builtin_amdgcn_sched_barrier(0)
#define BAR() __builtin_amdgcn_s_barrier()

#define STAMP(var) do { SBAR(); asm volatile("s_memtime %0\n\ts_waitcnt lgkmcnt(0)" : "=s"(var) :: "memory"); SBAR(); } while (0)
#define STAMP_DECL unsigned long long st_a, st_b, st_r0, st_r1; unsigned st_sum[8] = {0, 0, 0, 0, 0, 0, 0, 0}; asm volatile("s_memrealtime %0\n\ts_waitcnt lgkmcnt(0)" : "=s"(st_r0) :: "memory");
#define STAMP_ARGS , unsigned long long& st_a, unsigned long long& st_b, unsigned (&st_sum)[8]
#define STAMP_PASS , st_a, st_b, st_sum
#define STAMP_SEG(i) do { STAMP(st_b); st_sum[i] += (unsigned)(st_b - st_a); st_a = st_b; } while (0)
#define STAMP_START() STAMP(st_a)
#define STAMP_SEGI(i)
#define STAMP_OUT(ws, kern, wid, lane) do { asm volatile("s_memrealtime %0\n\ts_waitcnt lgkmcnt(0)" : "=s"(st_r1) :: "memory"); st_sum[7] = (unsigned)(st_r1 - st_r0); if (lane == 0) { unsigned* o_ = (unsigned*)((char*)(ws) + (128u << 20) + (kern) * (1u << 20)) + (blockIdx.x * 8 + (wid)) * 8; for (int i_ = 0; i_ < 8; ++i_) o_[i_] = st_sum[i_]; } } while (0)
#define TL_ENTRY() unsigned long long tl_0; asm volatile("s_memrealtime %0\n\ts_waitcnt lgkmcnt(0)" : "=s"(tl_0) :: "memory");
#define TL_EXIT(ws, kern) do { unsigned long long tl_1; asm volatile("s_waitcnt vmcnt(0)\n\ts_memrealtime %0\n\ts_waitcnt lgkmcnt(0)" : "=s"(tl_1) :: "memory"); \
    if ((threadIdx.x & 63) == 0) { unsigned long long* o_ = (unsigned long long*)((char*)(ws) + (140u << 20) + (kern) * (1u << 20)) + (blockIdx.x * 8 + (threadIdx.x >> 6)) * 2; o_[0] = tl_0; o_[1] = tl_1; } } while (0)

DEVI bf16x8 tr_frag(LAS const char* p, int off0, int off1) {
    bf16x4 lo = __builtin_amdgcn_ds_read_tr16_b64_v4bf16((LAS bf16x4*)(p + off0));
    bf16x4 hi = __builtin_amdgcn_ds_read_tr16_b64_v4bf16((LAS bf16x4*)(p + off1));
    return __builtin_shufflevector(lo, hi, 0, 1, 2, 3, 4, 5, 6, 7);
}

DEVI void glds16(const char* sbase, unsigned voff, unsigned ldsdst) {
    unsigned keep;
    asm volatile("s_mov_b32 %0, m0\n\ts_mov_b32 m0, %3\n\ts_nop 0\n\tglobal_load_lds_dwordx4 %1, %2\n\ts_mov_b32 m0, %0"
                 : "=&s"(keep) : "v"(voff), "s"(sbase), "s"(ldsdst) : "memory");
}

__global__ __launch_bounds__(512) void k_ln(const u16* __restrict__ x, char* __restrict__ ws) {
    __shared__ float red[2][8][8];
    TL_ENTRY();
    const int tid = threadIdx.x, w = tid >> 6, l = tid & 63, r = l & 7, c8 = l >> 3;
    const int row = blockIdx.x * 8 + r;
    u32x4 v[2];
#pragma unroll
    for (int i = 0; i < 2; ++i) v[i] = *(const u32x4*)(x + (size_t)row * D_ + (16 * w + 8 * i + c8) * 8);
    float f[16];
#pragma unroll
    for (int i = 0; i < 2; ++i)
#pragma unroll
        for (int j = 0; j < 4; ++j) { f[i * 8 + 2 * j] = bf_lo(v[i][j]); f[i * 8 + 2 * j + 1] = bf_hi(v[i][j]); }
    float s = 0.f;
#pragma unroll
    for (int j = 0; j < 16; ++j) s += f[j];
    s += __shfl_xor(s, 8); s += __shfl_xor(s, 16); s += __shfl_xor(s, 32);
    if (c8 == 0) red[0][w][r] = s;
    __syncthreads();
    float tot = 0.f;
#pragma unroll
    for (int k = 0; k < 8; ++k) tot += red[0][k][r];
    const float mean = tot * (1.0f / D_);
    float q = 0.f;
#pragma unroll
    for (int j = 0; j < 16; ++j) { const float d = f[j] - mean; q += d * d; }
    q += __shfl_xor(q, 8); q += __shfl_xor(q, 16); q += __shfl_xor(q, 32);
    if (c8 == 0) red[1][w][r] = q;
    __syncthreads();
    float qt = 0.f;
#pragma unroll
    for (int k = 0; k < 8; ++k) qt += red[1][k][r];
    const float mb = rbf(mean);
    const float vb = rbf(qt * (1.0f / D_));
    const float den = rbf(sqrtf(rbf(vb + 1e-5f)));
    const float rc = 1.0f / den;
    const int mblk = row >> 4, rr = row & 15;
#pragma unroll
    for (int i = 0; i < 2; ++i) {
        u32x4 o;
#pragma unroll
        for (int j = 0; j < 4; ++j) {
            float y[2];
#pragma unroll
            for (int e = 0; e < 2; ++e) {
                const float d = rbf(f[i * 8 + 2 * j + e] - mb);
                float qq = d * rc;
                qq = fmaf(fmaf(-qq, den, d), rc, qq);
                y[e] = qq;
            }
            o[j] = pk_bf16(y[0], y[1]);
        }
        const int ch = 16 * w + 8 * i + c8, slab = ch >> 2, g = ch & 3;
        asm volatile("global_store_dwordx4 %0, %1, off sc1\n\ts_nop 1" :: "v"(ws + WS_XN + slab * SLAB_B + (size_t)mblk * 1024 + (g * 16 + rr) * 16), "v"(o) : "memory");
    }
    TL_EXIT(ws, 0);
}

constexpr int G1_AST = 17 * 1024;
constexpr int G1_BST = 21 * 1024;
constexpr int G1_REG = 7168;
constexpr int G1_BOFF = 4 * G1_AST;
constexpr int G1_XOFF = G1_BOFF + 4 * G1_BST;
constexpr int G1_LDS = G1_XOFF + 8192;

typedef unsigned long long u64;
struct G1Ld {
    const char *pa, *p2, *pg, *pu;
    unsigned stepa, step2, stepg;
    unsigned va, v2, vg;
    unsigned la, l2, s2, lg, lu;
    const char* warm; unsigned warmstep;
    char* pf;
    u64 ma, m2, mg;
};

#define WAIT_LGKM0() __builtin_amdgcn_s_waitcnt(0xC07F)
#define MEMFENCE() asm volatile("" ::: "memory")
#define OPAQUE_S(x) asm volatile("" : "+s"(x))

template <int LOFF, bool TWO>
DEVI void g1_piece(const char* base, unsigned voff, unsigned lds, u64 msk) {
    asm volatile("s_add_u32 m0, %2, %4\n\ts_mov_b64 exec, %3\n\tglobal_load_lds_dwordx4 %0, %1\n\t"
                 ".if %5\n\tglobal_load_lds_dwordx4 %0, %1 offset:1024\n\t.endif\n\ts_mov_b64 exec, -1"
                 :: "v"(voff), "s"(base), "s"(lds), "s"(msk), "i"(LOFF), "i"(TWO ? 1 : 0) : "memory", "scc");
}
template <int SLOT> DEVI void g1_pa(G1Ld& L) { g1_piece<SLOT * G1_AST, true>(L.pa, L.va, L.la, L.ma); L.pa += L.stepa; OPAQUE_S(L.pa); }
template <int SLOT> DEVI void g1_p2(G1Ld& L) { g1_piece<0, false>(L.p2, L.v2, L.l2 + SLOT * L.s2, L.m2); L.p2 += L.step2; OPAQUE_S(L.p2); }
template <int SLOT> DEVI void g1_pg(G1Ld& L) { g1_piece<SLOT * G1_BST, false>(L.pg, L.vg, L.lg, L.mg); L.pg += L.stepg; OPAQUE_S(L.pg); }
template <int SLOT> DEVI void g1_pu(G1Ld& L) { g1_piece<SLOT * G1_BST, false>(L.pu, L.vg, L.lu, L.mg); L.pu += L.stepg; OPAQUE_S(L.pu); }
template <int SLOT> DEVI void g1_issue(G1Ld& L) { g1_pa<SLOT>(L); g1_p2<SLOT>(L); g1_pg<SLOT>(L); g1_pu<SLOT>(L); }
DEVI void g1_tail(G1Ld& L, bool tail, const char* warm, unsigned warmstep) {
    L.p2 = tail ? L.p2 - L.step2 : L.p2; L.pg = tail ? L.pg - L.stepg : L.pg; L.pu = tail ? L.pu - L.stepg : L.pu;
    L.step2 = tail ? 0u : L.step2; L.stepg = tail ? 0u : L.stepg;
    L.m2 = tail ? 1ull : L.m2; L.mg = tail ? 1ull : L.mg;
    L.pa = tail ? warm : L.pa; L.stepa = tail ? warmstep : L.stepa;
}

struct G1Ptr { LAS const char *pA, *pH, *pB01, *pB23, *pBh01, *pBh23; };

template <int GRP, int SL> DEVI bf16x8 g1_ldb(const G1Ptr& P, int n) {
    LAS const char* pB = (SL < 2) ? P.pB01 : P.pB23;
    const int off = (SL & 1) * G1_BST + (GRP == 0 ? (n < 6 ? n * 32 : (n < 8 ? G1_REG + (n - 6) * 32 : 2 * G1_REG + (n - 8) * 32))
                                                  : (n < 5 ? G1_REG + (2 + n) * 32 : 2 * G1_REG + (n - 3) * 32));
    return tr_frag(pB, off, off + 8 * 224);
}
template <int SL> DEVI bf16x8 g1_lda(const G1Ptr& P, int m) { return *(LAS const bf16x8*)(P.pA + SL * G1_AST + (1 + m) * 1024); }
template <int SL> DEVI void g1_ldh(const G1Ptr& P, bf16x8& Ah, bf16x8& Bh) {
    Ah = *(LAS const bf16x8*)(P.pH + SL * G1_AST);
    Bh = tr_frag((SL < 2) ? P.pBh01 : P.pBh23, (SL & 1) * G1_BST, (SL & 1) * G1_BST + 8 * 224);
}

template <int GRP, int ST>
DEVI void g1_stage(f32x4 (&acc)[4][10], f32x4& acch, bf16x8 (&A)[4], bf16x8 (&B)[10], bf16x8& Ah, bf16x8& Bh, G1Ld& L, bool tail, const G1Ptr& P STAMP_ARGS) {
    constexpr int NX = (ST + 1) & 3, IS = (ST + 3) & 3;
#define MM(m, n) do { if (GRP == 0 && ((n) == 4 || (n) == 5)) acc[m][n] = MFMA16(A[m], B[n], acc[m][n]); else acc[m][n] = MFMA16(B[n], A[m], acc[m][n]); SBAR(); } while (0)
#define RB(n) do { B[n] = g1_ldb<GRP, NX>(P, n); SBAR(); } while (0)
#define RA(m) do { A[m] = g1_lda<NX>(P, m); SBAR(); } while (0)
#define G0(x) do { if (GRP == 0) { x; SBAR(); } } while (0)
    MM(0, 0); MM(1, 0); G0(g1_pa<IS>(L));
    MM(1, 1); MM(0, 1); G0(g1_p2<IS>(L));
    MM(0, 2); MM(1, 2); G0(g1_pg<IS>(L));
    MM(1, 3); MM(0, 3); G0(g1_pu<IS>(L));
    MM(0, 4); MM(1, 4);
    G0(acch = MFMA16(Bh, Ah, acch));
    asm volatile("s_waitcnt vmcnt(10)" ::: "memory");
    WAIT_LGKM0();
    MEMFENCE(); SBAR();
    BAR();
    MEMFENCE(); SBAR();
    if (GRP == 1) {
        if (ST == 0) g1_tail(L, tail, L.warm, L.warmstep);
        g1_issue<ST>(L);
        if (ST == 2) {
            const u32x4 ones = {0xffffffffu, 0xffffffffu, 0xffffffffu, 0xffffffffu};
            asm volatile("global_store_dwordx4 %0, %1, %2 sc1\n\ts_nop 1" :: "v"(L.va), "v"(ones), "s"(L.pf) : "memory");
            L.pf += 1024; OPAQUE_S(L.pf);
        }
        SBAR();
    }
    MM(2, 0); MM(3, 0); RB(0);
    MM(3, 1); MM(2, 1); RB(1);
    MM(2, 2); MM(3, 2); RB(2);
    MM(3, 3); MM(2, 3); RB(3);
    MM(2, 4); MM(3, 4); RB(4);
    MM(0, 5); MM(1, 5); G0(g1_ldh<NX>(P, Ah, Bh));
    MM(1, 6); MM(0, 6);
    MM(0, 7); MM(1, 7);
    MM(1, 8); MM(0, 8);
    MM(0, 9); RA(0); MM(1, 9); RA(1);
    MM(2, 5); MM(3, 5); RB(5);
    MM(3, 6); MM(2, 6); RB(6);
    MM(2, 7); MM(3, 7); RB(7);
    MM(3, 8); MM(2, 8); RB(8);
    MM(2, 9); RA(2); MM(3, 9); RA(3); RB(9);
#undef MM
#undef RB
#undef RA
#undef G0
}

DEVI void st16(char* dst, u32x4 v) {
    asm volatile("global_store_dwordx4 %0, %1, off sc1\n\ts_nop 1" :: "v"(dst), "v"(v) : "memory");
}

DEVI unsigned glu2(float g0, float g1, float u0, float u1) {
    const f32x2 t = (f32x2){g0, g1} * -1.4426950408889634f;
    const f32x2 d = (f32x2){__builtin_amdgcn_exp2f(t[0]), __builtin_amdgcn_exp2f(t[1])} + 1.0f;
    const f32x2 h = (f32x2){u0, u1} * (f32x2){__builtin_amdgcn_rcpf(d[0]), __builtin_amdgcn_rcpf(d[1])};
    return pk_bf16(h[0], h[1]);
}

DEVI void glu_store(const f32x4& g0, const f32x4& u0, const f32x4& g1, const f32x4& u1, char* dst) {
    unsigned a0 = glu2(g0[0], g0[1], u0[0], u0[1]), a1 = glu2(g0[2], g0[3], u0[2], u0[3]);
    unsigned b0 = glu2(g1[0], g1[1], u1[0], u1[1]), b1 = glu2(g1[2], g1[3], u1[2], u1[3]);
    auto r0 = __builtin_amdgcn_permlane16_swap(a0, b0, false, false);
    auto r1 = __builtin_amdgcn_permlane16_swap(a1, b1, false, false);
    st16(dst, (u32x4){r0[0], r1[0], r0[1], r1[1]});
}

template <int GRP>
DEVI void g1_body(G1Ld& L, LAS char* lds, char* ws, int rt, int ct, int wm, int lane) {
    const int g = lane >> 4, i16 = lane & 15, q = i16 >> 2, p = lane & 3;
    G1Ptr P;
    P.pA = lds + wm * 4096 + lane * 16;
    P.pH = lds + lane * 16;
    const int brow = (16 * (g >> 1) + 4 * (g & 1) + q) * 224 + p * 8;
    P.pB01 = lds + G1_BOFF + brow;
    P.pB23 = P.pB01 + 2 * G1_BST;
    P.pBh01 = P.pB01 + (2 + wm) * 32;
    P.pBh23 = P.pB23 + (2 + wm) * 32;
    f32x4 acc[4][10], acch = {0.f, 0.f, 0.f, 0.f};
#pragma unroll
    for (int m = 0; m < 4; ++m)
#pragma unroll
        for (int n = 0; n < 10; ++n) acc[m][n] = (f32x4){0.f, 0.f, 0.f, 0.f};

    STAMP_DECL STAMP_START();
    g1_issue<0>(L); g1_issue<1>(L);
    asm volatile("s_waitcnt vmcnt(5)" ::: "memory");
    SBAR(); BAR(); MEMFENCE(); SBAR();
    bf16x8 A[4], B[10], Ah, Bh;
#pragma unroll
    for (int n = 0; n < 10; ++n) { if (n < 4) A[n] = g1_lda<0>(P, n); B[n] = g1_ldb<GRP, 0>(P, n); }
    if (GRP == 0) g1_ldh<0>(P, Ah, Bh);
    SBAR();
    g1_issue<2>(L);
    if (GRP == 1) g1_issue<3>(L);
    SBAR();
    STAMP_SEG(0);
#pragma unroll 1
    for (int it = 0; it < 8; ++it) {
        g1_stage<GRP, 0>(acc, acch, A, B, Ah, Bh, L, it == 7, P STAMP_PASS);
        if (GRP == 0) g1_tail(L, it == 7, L.warm, L.warmstep);
        g1_stage<GRP, 1>(acc, acch, A, B, Ah, Bh, L, it == 7, P STAMP_PASS);
        g1_stage<GRP, 2>(acc, acch, A, B, Ah, Bh, L, it == 7, P STAMP_PASS);
        g1_stage<GRP, 3>(acc, acch, A, B, Ah, Bh, L, it == 7, P STAMP_PASS);
    }
    STAMP_SEG(1);

    char* ho = ws + WS_HO;
    const int mb0 = 16 * rt + 4 * wm;
    const unsigned hl = (unsigned)((g & 1) * 1024 + (g >> 1) * 256 + i16 * 16);
    if (GRP == 1) {
        SBAR(); BAR(); SBAR();
#pragma unroll
        for (int n = 0; n < 5; ++n) {
            const int cb = 7 * ct + 2 + n;
            char* dst = ho + (size_t)(32 + (cb >> 1)) * SLAB_B + (cb & 1) * 512 + (size_t)mb0 * 1024 + hl;
#pragma unroll
            for (int m = 0; m < 4; m += 2) glu_store(acc[m][n], acc[m][5 + n], acc[m + 1][n], acc[m + 1][5 + n], dst + m * 1024);
        }
    } else {
        u32x4 Qf[4], Kf[4]; u32x2 Vh[4][2];
#pragma unroll
        for (int m = 0; m < 4; ++m) {
            Qf[m] = (u32x4){pk_bf16(acc[m][0][0], acc[m][0][1]), pk_bf16(acc[m][0][2], acc[m][0][3]), pk_bf16(acc[m][1][0], acc[m][1][1]), pk_bf16(acc[m][1][2], acc[m][1][3])};
            Kf[m] = (u32x4){pk_bf16(acc[m][2][0], acc[m][2][1]), pk_bf16(acc[m][2][2], acc[m][2][3]), pk_bf16(acc[m][3][0], acc[m][3][1]), pk_bf16(acc[m][3][2], acc[m][3][3])};
#pragma unroll
            for (int b = 0; b < 2; ++b) Vh[m][b] = (u32x2){pk_bf16(acc[m][4 + b][0], acc[m][4 + b][1]), pk_bf16(acc[m][4 + b][2], acc[m][4 + b][3])};
        }
        LAS char* xs = lds + G1_XOFF;
        if (wm < 3) {
            *(LAS u32x4*)(xs + (wm + 1) * 2048 + lane * 16) = Kf[3];
            *(LAS u32x4*)(xs + (wm + 1) * 2048 + 1024 + lane * 16) = (u32x4){Vh[3][0][0], Vh[3][0][1], Vh[3][1][0], Vh[3][1][1]};
        }
        {
            const unsigned h0 = pk_bf16(acch[0], acch[1]), h1 = pk_bf16(acch[2], acch[3]);
            if (wm < 2) {
                *(LAS u32x2*)(xs + lane * 16 + wm * 8) = (u32x2){h0, h1};
            } else {
                LAS char* t = xs + 1024 + ((i16 >> 2) * 16 + 4 * g) * 16 + (wm - 2) * 8 + (i16 & 3) * 2;
                *(LAS u16*)(t) = (u16)h0; *(LAS u16*)(t + 16) = (u16)(h0 >> 16); *(LAS u16*)(t + 32) = (u16)h1; *(LAS u16*)(t + 48) = (u16)(h1 >> 16);
            }
        }
        asm volatile("s_waitcnt lgkmcnt(0)" ::: "memory");
        SBAR(); BAR(); SBAR();
        u32x4 Kp = *(LAS const u32x4*)(xs + wm * 2048 + lane * 16);
        u32x4 Vp = *(LAS const u32x4*)(xs + wm * 2048 + 1024 + lane * 16);
#pragma unroll
        for (int n = 0; n < 2; ++n) {
            const int cb = 7 * ct + n;
            char* dst = ho + (size_t)(32 + (cb >> 1)) * SLAB_B + (cb & 1) * 512 + (size_t)mb0 * 1024 + hl;
#pragma unroll
            for (int m = 0; m < 4; m += 2) glu_store(acc[m][6 + n], acc[m][8 + n], acc[m + 1][6 + n], acc[m + 1][8 + n], dst + m * 1024);
        }
        const bool odd = (i16 & 1) != 0;
        const int par = i16 & 1;
        const int dp0 = 16 + i16 - 4 * g - par, dc0 = i16 - 4 * g - par;
        const bool vp0 = dp0 <= 14, vp1 = dp0 - 2 <= 14, vc0 = dc0 >= 0, vc1 = dc0 - 2 >= 0;
        const float NEG = -__builtin_inff();
        const float sc = 0.17677669529663687f * 1.4426950408889634f;
        const bool first = (rt == 0) && (wm == 0);
        char* od = ho + (size_t)ct * SLAB_B + (size_t)mb0 * 1024 + (((g & 1) << 1) | (g >> 1)) * 256 + i16 * 16;
#pragma unroll
        for (int m = 0; m < 4; ++m) {
            const f32x4 z = {0.f, 0.f, 0.f, 0.f};
            f32x4 s0 = MFMA16(as_frag(Kp), as_frag(Qf[m]), z);
            f32x4 s1 = MFMA16(as_frag(Kf[m]), as_frag(Qf[m]), z);
            float a0 = (odd ? s0[1] : s0[0]) * sc, a1 = (odd ? s0[3] : s0[2]) * sc;
            float b0 = (odd ? s1[1] : s1[0]) * sc, b1 = (odd ? s1[3] : s1[2]) * sc;
            if (m == 0 && first) { a0 = -1e9f * 1.4426950408889634f; a1 = a0; }
            a0 = vp0 ? a0 : NEG; a1 = vp1 ? a1 : NEG; b0 = vc0 ? b0 : NEG; b1 = vc1 ? b1 : NEG;
            float mx = fmaxf(fmaxf(a0, a1), fmaxf(b0, b1));
            { auto t = __builtin_amdgcn_permlane16_swap(__float_as_uint(mx), __float_as_uint(mx), false, false); mx = fmaxf(__uint_as_float(t[0]), __uint_as_float(t[1])); }
            { auto t = __builtin_amdgcn_permlane32_swap(__float_as_uint(mx), __float_as_uint(mx), false, false); mx = fmaxf(__uint_as_float(t[0]), __uint_as_float(t[1])); }
            a0 = __builtin_amdgcn_exp2f(a0 - mx); a1 = __builtin_amdgcn_exp2f(a1 - mx); b0 = __builtin_amdgcn_exp2f(b0 - mx); b1 = __builtin_amdgcn_exp2f(b1 - mx);
            float sm = (a0 + a1) + (b0 + b1);
            { auto t = __builtin_amdgcn_permlane16_swap(__float_as_uint(sm), __float_as_uint(sm), false, false); sm = __uint_as_float(t[0]) + __uint_as_float(t[1]); }
            { auto t = __builtin_amdgcn_permlane32_swap(__float_as_uint(sm), __float_as_uint(sm), false, false); sm = __uint_as_float(t[0]) + __uint_as_float(t[1]); }
            const float rs = __builtin_amdgcn_rcpf(sm);
            a0 *= rs; a1 *= rs; b0 *= rs; b1 *= rs;
            const u32x4 Pf = {odd ? pk_bf16(0.f, a0) : pk_bf16(a0, 0.f), odd ? pk_bf16(0.f, a1) : pk_bf16(a1, 0.f),
                              odd ? pk_bf16(0.f, b0) : pk_bf16(b0, 0.f), odd ? pk_bf16(0.f, b1) : pk_bf16(b1, 0.f)};
            const u32x4 V0 = {Vp[0], Vp[1], Vh[m][0][0], Vh[m][0][1]}, V1 = {Vp[2], Vp[3], Vh[m][1][0], Vh[m][1][1]};
            const f32x4 o0 = MFMA16(as_frag(V0), as_frag(Pf), z);
            const f32x4 o1 = MFMA16(as_frag(V1), as_frag(Pf), z);
            unsigned x0 = pk_bf16(o0[0], o0[1]), x1 = pk_bf16(o0[2], o0[3]), y0 = pk_bf16(o1[0], o1[1]), y1 = pk_bf16(o1[2], o1[3]);
            auto r0 = __builtin_amdgcn_permlane16_swap(x0, y0, false, false);
            auto r1 = __builtin_amdgcn_permlane16_swap(x1, y1, false, false);
            st16(od + m * 1024, (u32x4){r0[0], r1[0], r0[1], r1[1]});
            Kp = Kf[m];
            Vp = (u32x4){Vh[m][0][0], Vh[m][0][1], Vh[m][1][0], Vh[m][1][1]};
        }
    }
    STAMP_SEG(5);
    asm volatile("s_waitcnt vmcnt(0)" ::: "memory");
    STAMP_SEG(6);
    STAMP_OUT(ws, 0, GRP * 4 + wm, lane);
}

__global__ __launch_bounds__(512, 2) void k_gemm1(const char* __restrict__ wqkv, const char* __restrict__ wg, const char* __restrict__ wu, const char* __restrict__ wo, const char* __restrict__ wd, char* __restrict__ ws) {
    extern __shared__ __attribute__((aligned(16))) char smem[];
    TL_ENTRY();
    LAS char* lds = (LAS char*)smem;
    const int tid = threadIdx.x, lane = tid & 63;
    const int wid = __builtin_amdgcn_readfirstlane(tid >> 6);
    const int bid = blockIdx.x, xcd = bid & 7, idx = bid >> 3;
    const int ct = 4 * xcd + (idx & 3), rt = idx >> 2;
    const unsigned ldsb = (unsigned)(size_t)lds;
    G1Ld L;
    {
        const int c = wid * 64 + lane, rho = c / 14, cc = c - rho * 14;
        const int k = (rho & ~12) | ((rho & 4) << 1) | ((rho & 8) >> 1);
        const int c2 = cc < 12 ? cc : 11;
        const bool last = wid == 7;
        L.pa = ws + WS_XN + (size_t)(16 * rt + 2 * wid) * 1024;
        L.stepa = (unsigned)SLAB_B;
        L.va = lane * 16;
        L.la = ldsb + (1 + 2 * wid) * 1024;
        L.p2 = last ? ws + WS_XN + (size_t)(rt == 0 ? 0 : 16 * rt - 1) * 1024 : wqkv + ct * 64;
        L.step2 = last ? (unsigned)SLAB_B : 32u * 3072u * 2u;
        L.v2 = last ? (unsigned)((rt == 0 ? (lane & 48) : lane) * 16) : (unsigned)(k * 6144 + (c2 >> 2) * 2048 + (c2 & 3) * 16);
        L.l2 = last ? ldsb : ldsb + G1_BOFF + wid * 1024;
        L.s2 = last ? G1_AST : G1_BST;
        L.pg = wg + ct * 224;
        L.pu = wu + ct * 224;
        L.stepg = 32u * 3584u * 2u;
        L.vg = last ? 0u : (unsigned)(k * 7168 + cc * 16);
        L.lg = last ? ldsb + G1_BOFF + 192 : ldsb + G1_BOFF + G1_REG + wid * 1024;
        L.lu = last ? ldsb + G1_BOFF + 192 : ldsb + G1_BOFF + 2 * G1_REG + wid * 1024;
        L.ma = ~0ull; L.m2 = ~0ull; L.mg = last ? 1ull : ~0ull;
    }
    const int wdo = bid * 28672 < 3584 * 2048 - 32768 ? bid * 28672 : 3584 * 2048 - 32768;
    L.warm = wid < 4 ? wo + bid * 8192 + wid * 2048 : wd + wdo + (wid & 3) * 2048;
    L.warmstep = wid < 4 ? 0u : 8192u;
    L.pf = ws + WS_XB + (size_t)bid * 32768 + (wid & 3) * 8192;
    if (wid < 4) g1_body<0>(L, lds, ws, rt, ct, wid & 3, lane);
    else g1_body<1>(L, lds, ws, rt, ct, wid & 3, lane);
    TL_EXIT(ws, 1);
}

#define POLL_SLEEP 2
constexpr int G2_ST = 16384;
constexpr int G2_NS = 6;
constexpr int G2_LDS = G2_NS * G2_ST;

struct G2St {
    const char* pa;
    const char* pw;
    const char* wdown;
    unsigned va, vw0, vw1;
    unsigned ldsw;
    int wst;
};
template <int LOFF> DEVI void g2_glds(const char* base, unsigned voff, unsigned lds) {
    asm volatile("s_add_u32 m0, %2, %3\n\ts_nop 0\n\tglobal_load_lds_dwordx4 %0, %1" :: "v"(voff), "s"(base), "s"(lds), "i"(LOFF) : "memory", "scc");
}
template <int OFF> DEVI void g2_lda(u32x4& dst, const char* base, unsigned voff) {
    asm volatile("global_load_dwordx4 %0, %1, %2 offset:%3" : "=v"(dst) : "v"(voff), "s"(base), "i"(OFF) : "memory");
}
template <int SLOT, int I> DEVI void g2_w(G2St& T) {
    if (I == 0) g2_glds<SLOT * G2_ST>(T.pw, T.vw0, T.ldsw);
    else { g2_glds<SLOT * G2_ST + 8192>(T.pw, T.vw1, T.ldsw); T.pw = (T.wst == 15) ? T.wdown : T.pw + 64 * 2048; T.wst += 1; OPAQUE_S(T.pw); }
}
template <int I> DEVI void g2_a(G2St& T, u32x4 (&A)[2]) {
    if (I == 0) g2_lda<0>(A[0], T.pa, T.va);
    else { g2_lda<1024>(A[1], T.pa, T.va); T.pa += 2 * SLAB_B; OPAQUE_S(T.pa); }
}
template <int SLOT> DEVI void g2_group(G2St& T, u32x4 (&A)[2]) { g2_w<SLOT, 0>(T); g2_w<SLOT, 1>(T); g2_a<0>(T, A); g2_a<1>(T, A); }

template <int SLOT> DEVI bf16x8 g2_ldb(LAS const char* (&pB)[2][4], int n) {
    return tr_frag(pB[SLOT / 3][n & 3], (SLOT % 3) * G2_ST + (n >> 2) * 8192, (SLOT % 3) * G2_ST + (n >> 2) * 8192 + 512);
}

template <int U, int WAIT, bool ISW, bool ISA, bool RL>
DEVI void g2_stage(f32x4 (&acc)[2][8], G2St& T, u32x4 (&Ac)[2], u32x4 (&An)[2], bf16x8 (&B)[8], LAS const char* (&pB)[2][4] STAMP_ARGS) {
    constexpr int NX = (U + 1) % G2_NS, IS = (U + 5) % G2_NS;
    asm volatile("s_waitcnt vmcnt(%2)" : "+v"(Ac[0]), "+v"(Ac[1]) : "i"(WAIT) : "memory");
    STAMP_SEGI(1);
    SBAR(); BAR(); MEMFENCE(); SBAR();
    STAMP_SEGI(2);
#define P2(n) do { acc[(n) & 1][n] = MFMA16(B[n], as_frag(Ac[(n) & 1]), acc[(n) & 1][n]); SBAR(); acc[~(n) & 1][n] = MFMA16(B[n], as_frag(Ac[~(n) & 1]), acc[~(n) & 1][n]); SBAR(); \
                   if (RL) { B[n] = g2_ldb<NX>(pB, n); SBAR(); } } while (0)
    P2(0); if (ISW) { g2_w<IS, 0>(T); SBAR(); }
    P2(1);
    P2(2); if (ISW) { g2_w<IS, 1>(T); SBAR(); }
    P2(3);
    P2(4); if (ISA) { g2_a<0>(T, An); SBAR(); }
    P2(5);
    P2(6); if (ISA) { g2_a<1>(T, An); SBAR(); }
    P2(7);
#undef P2
    STAMP_SEGI(3);
}

__global__ __launch_bounds__(512, 2) void k_gemm2(const u16* __restrict__ x, const char* __restrict__ wo, const char* __restrict__ wd, char* __restrict__ ws, u16* __restrict__ out) {
    extern __shared__ __attribute__((aligned(16))) char smem[];
    TL_ENTRY();
    LAS char* lds = (LAS char*)smem;
    const int tid = threadIdx.x, lane = tid & 63;
    const int wid = __builtin_amdgcn_readfirstlane(tid >> 6);
    const int kg = wid >> 2, wm = wid & 3;
    const int bid = blockIdx.x, kh = bid & 1, quad = (bid >> 1) & 3, idx = bid >> 3;
    const int rt = 8 * (quad >> 1) + (idx >> 2), ct = 4 * (quad & 1) + (idx & 3);
    const int g = lane >> 4, i16 = lane & 15, q = i16 >> 2, p = lane & 3;

    G2St T;
    T.pa = ws + WS_HO + (size_t)(72 * kh + kg) * SLAB_B + (size_t)(8 * rt + 2 * wm) * 1024;
    T.va = lane * 16;
    T.wdown = wd + ct * 256;
    T.pw = kh ? T.wdown + (size_t)(2304 - 1024) * 2048 : wo + ct * 256;
    T.wst = 36 * kh;
    {
        const int r = lane >> 3, cp = lane & 7, c = cp ^ ((r & 2) | ((wid & 1) << 2));
        const int keep = kh ^ kg ^ 1;
        T.vw0 = (unsigned)((8 * wid + r) * 2048 + c * 16 + keep * 128);
        T.vw1 = T.vw0 ^ 128;
    }
    T.ldsw = (unsigned)(size_t)lds + wid * 1024;

    STAMP_DECL STAMP_START();
    u32x4 A0[2], A1[2], A2[2], A3[2], A4[2], A5[2];
    g2_w<0, 0>(T); g2_w<0, 1>(T);
    g2_group<1>(T, A0);

    LAS const char* pB[2][4];
#pragma unroll
    for (int n = 0; n < 4; ++n) {
        pB[0][n] = lds + kg * 4096 + (8 * g + q) * 128 + (((2 * n + (p >> 1)) ^ ((q & 2) | ((g & 1) << 2))) * 16) + (p & 1) * 8;
        pB[1][n] = pB[0][n] + 3 * G2_ST;
    }
    f32x4 acc[2][8];
#pragma unroll
    for (int m = 0; m < 2; ++m)
#pragma unroll
        for (int n = 0; n < 8; ++n) acc[m][n] = (f32x4){0.f, 0.f, 0.f, 0.f};
    g2_group<2>(T, A1);
    asm volatile("s_waitcnt vmcnt(8)" ::: "memory");
    SBAR(); BAR(); MEMFENCE(); SBAR();
    bf16x8 B[8];
#pragma unroll
    for (int n = 0; n < 8; ++n) B[n] = g2_ldb<0>(pB, n);
    SBAR();
    g2_group<3>(T, A2); g2_group<4>(T, A3);
    SBAR();
    STAMP_SEG(0);
#pragma unroll 1
    for (int it = 0; it < 5; ++it) {
        g2_stage<0, 12, true, true, true>(acc, T, A0, A4, B, pB STAMP_PASS);
        g2_stage<1, 12, true, true, true>(acc, T, A1, A5, B, pB STAMP_PASS);
        g2_stage<2, 12, true, true, true>(acc, T, A2, A0, B, pB STAMP_PASS);
        g2_stage<3, 12, true, true, true>(acc, T, A3, A1, B, pB STAMP_PASS);
        g2_stage<4, 12, true, true, true>(acc, T, A4, A2, B, pB STAMP_PASS);
        g2_stage<5, 12, true, true, true>(acc, T, A5, A3, B, pB STAMP_PASS);
    }
    g2_stage<0, 12, true, true, true>(acc, T, A0, A4, B, pB STAMP_PASS);
    g2_stage<1, 12, false, true, true>(acc, T, A1, A5, B, pB STAMP_PASS);
    const int orow = 128 * rt + 32 * wm + i16, ocol = 128 * ct + 64 * kh;
    u32x4 xr[4];
    {
        const u16* xp = x + (size_t)orow * D_ + ocol + (g & 1) * 16 + (g >> 1) * 8;
        const u16* xq = xp + 16 * D_;
        asm volatile("global_load_dwordx4 %0, %4, off\n\tglobal_load_dwordx4 %1, %4, off offset:64\n\tglobal_load_dwordx4 %2, %5, off\n\tglobal_load_dwordx4 %3, %5, off offset:64"
                     : "=&v"(xr[0]), "=&v"(xr[1]), "=&v"(xr[2]), "=&v"(xr[3]) : "v"(xp), "v"(xq) : "memory");
    }
    g2_stage<2, 14, false, false, true>(acc, T, A2, A0, B, pB STAMP_PASS);
    g2_stage<3, 10, false, false, true>(acc, T, A3, A1, B, pB STAMP_PASS);
    g2_stage<4, 6, false, false, true>(acc, T, A4, A2, B, pB STAMP_PASS);
    g2_stage<5, 4, false, false, false>(acc, T, A5, A3, B, pB STAMP_PASS);
    STAMP_SEG(3);

    {
        LAS char* xs = lds + wid * 8192 + lane * 16;
#pragma unroll
        for (int m = 0; m < 2; ++m)
#pragma unroll
            for (int j = 0; j < 4; ++j) *(LAS f32x4*)(xs + (m * 4 + j) * 1024) = acc[m][4 + j];
    }
    asm volatile("s_waitcnt vmcnt(0)" : "+v"(xr[0]), "+v"(xr[1]), "+v"(xr[2]), "+v"(xr[3]) :: "memory");
    WAIT_LGKM0();
    MEMFENCE(); SBAR(); BAR(); MEMFENCE(); SBAR();
    LAS const char* xl = lds + (wid ^ 4) * 8192 + lane * 16;
    char* xb = ws + WS_XB + wm * 8192 + lane * 16;
    if (kg == 0) {
        char* dst = xb + (size_t)bid * 32768;
#pragma unroll
        for (int m = 0; m < 2; ++m)
#pragma unroll
            for (int j = 0; j < 4; ++j) {
                u32x4 w = __builtin_bit_cast(u32x4, acc[m][j] + *(LAS const f32x4*)(xl + (m * 4 + j) * 1024));
#pragma unroll
                for (int e = 0; e < 4; ++e) w[e] = w[e] < 0xfffffffeu ? w[e] : 0xfffffffeu;
                asm volatile("global_store_dwordx4 %0, %1, off offset:%2 sc1\n\ts_nop 1" :: "v"(dst + m * 4096), "v"(w), "i"(j * 1024) : "memory");
            }
        STAMP_SEG(4);
    } else {
#pragma unroll
        for (int m = 0; m < 2; ++m)
#pragma unroll
            for (int j = 0; j < 4; ++j) acc[m][j] += *(LAS const f32x4*)(xl + (m * 4 + j) * 1024);
        STAMP_SEG(4);
        u32x4 rc[8];
        const char* src = xb + (size_t)(bid ^ 1) * 32768;
        unsigned spin = 0;
        {
            const char* last = src - lane * 16 + 8188;
            for (;; ++spin) {
                unsigned w;
                asm volatile("global_load_dword %0, %1, off sc1\n\ts_waitcnt vmcnt(0)" : "=&v"(w) : "v"(last) : "memory");
                if (__builtin_amdgcn_readfirstlane(w) != 0xffffffffu || spin > (1u << 22)) break;
                __builtin_amdgcn_s_sleep(POLL_SLEEP);
            }
        }
        for (;; ++spin) {
            asm volatile("global_load_dwordx4 %0, %8, off sc1\n\tglobal_load_dwordx4 %1, %8, off offset:1024 sc1\n\tglobal_load_dwordx4 %2, %8, off offset:2048 sc1\n\tglobal_load_dwordx4 %3, %8, off offset:3072 sc1\n\t"
                         "global_load_dwordx4 %4, %9, off sc1\n\tglobal_load_dwordx4 %5, %9, off offset:1024 sc1\n\tglobal_load_dwordx4 %6, %9, off offset:2048 sc1\n\tglobal_load_dwordx4 %7, %9, off offset:3072 sc1\n\ts_waitcnt vmcnt(0)"
                         : "=&v"(rc[0]), "=&v"(rc[1]), "=&v"(rc[2]), "=&v"(rc[3]), "=&v"(rc[4]), "=&v"(rc[5]), "=&v"(rc[6]), "=&v"(rc[7]) : "v"(src), "v"(src + 4096) : "memory");
            unsigned mx = 0;
#pragma unroll
            for (int j = 0; j < 8; ++j) { const unsigned a = rc[j][0] > rc[j][1] ? rc[j][0] : rc[j][1], b = rc[j][2] > rc[j][3] ? rc[j][2] : rc[j][3]; mx = mx > a ? mx : a; mx = mx > b ? mx : b; }
            if (!__any(mx == 0xffffffffu) || spin > (1u << 22)) break;
            __builtin_amdgcn_s_sleep(4);
        }
        u16* orp = out + (size_t)orow * D_ + ocol + (g & 1) * 16 + (g >> 1) * 8;
#pragma unroll
        for (int m = 0; m < 2; ++m)
#pragma unroll
            for (int j = 0; j < 4; j += 2) {
                const f32x4 s0 = acc[m][j] + __builtin_bit_cast(f32x4, rc[m * 4 + j]), s1 = acc[m][j + 1] + __builtin_bit_cast(f32x4, rc[m * 4 + j + 1]);
                float v[8];
#pragma unroll
                for (int e = 0; e < 4; ++e) { auto r = __builtin_amdgcn_permlane16_swap(__float_as_uint(s0[e]), __float_as_uint(s1[e]), false, false); v[e] = __uint_as_float(r[0]); v[4 + e] = __uint_as_float(r[1]); }
                const u32x4 xv = xr[m * 2 + (j >> 1)];
                u32x4 o;
#pragma unroll
                for (int e = 0; e < 4; ++e) o[e] = pk_bf16(v[2 * e] + bf_lo(xv[e]), v[2 * e + 1] + bf_hi(xv[e]));
                *(u32x4*)(orp + m * 16 * D_ + 16 * j) = o;
            }
    }
    STAMP_SEG(5);
    asm volatile("s_waitcnt vmcnt(0)" ::: "memory");
    STAMP_SEG(6);
    STAMP_OUT(ws, 1, wid, lane);
    TL_EXIT(ws, 2);
}

__global__ void k_probe(char* ws) {
    const int t = threadIdx.x;
    unsigned long long v[6];
    for (int k = 0; k < 3; ++k) {
        const unsigned long long* p = (const unsigned long long*)(ws + (140u << 20) + k * (1u << 20));
        unsigned long long e = ~0ull, x = 0;
#pragma unroll
        for (int i = 0; i < 32; ++i) { const unsigned long long a = p[2 * (i * 64 + t)], b = p[2 * (i * 64 + t) + 1]; e = e < a ? e : a; x = x > b ? x : b; }
        for (int o = 32; o; o >>= 1) { const unsigned long long a = __shfl_xor(e, o), b = __shfl_xor(x, o); e = e < a ? e : a; x = x > b ? x : b; }
        v[2 * k] = e; v[2 * k + 1] = x;
    }
    unsigned long long s = 0;
    { const unsigned* q = (const unsigned*)(ws + (128u << 20) + PROBE_KERN * (1u << 20)); for (int i = 0; i < 4; ++i) s += q[((i * 64 + t) * 8 + PROBE_WAVE) * 8 + PROBE_SEG]; }
    for (int o = 32; o; o >>= 1) s += __shfl_xor(s, o);
    unsigned long long all = (v[0] ^ v[1] ^ v[2] ^ v[3] ^ v[4] ^ v[5] ^ s) == 0x123456789abcdefull ? 1 : 0;
    unsigned long long d = PROBE_I < 0 ? 0 : (PROBE_I < 5 ? v[PROBE_I < 5 && PROBE_I >= 0 ? PROBE_I + 1 : 0] - v[PROBE_I < 5 && PROBE_I >= 0 ? PROBE_I : 0] : (PROBE_I == 5 ? v[5] - v[0] : s / 256 / PROBE_DIV));
    d += all;
    if (d > 20000) d = 20000;
    const unsigned long long t0 = __builtin_amdgcn_s_memrealtime();
    while (__builtin_amdgcn_s_memrealtime() - t0 < d * PROBE_K) __builtin_amdgcn_s_sleep(1);
}

extern "C" void kernel_launch(void* const* d_in, const int* in_sizes, int n_in,
                              void* d_out, int out_size, void* d_ws, size_t ws_size,
                              hipStream_t stream) {
    const u16* x = (const u16*)d_in[0];
    const char* wqkv = (const char*)d_in[1];
    const char* wo = (const char*)d_in[2];
    const char* wg = (const char*)d_in[4];
    const char* wu = (const char*)d_in[5];
    const char* wd = (const char*)d_in[6];
    char* ws = (char*)d_ws;
    (void)hipFuncSetAttribute((const void*)k_gemm1, hipFuncAttributeMaxDynamicSharedMemorySize, G1_LDS);
    (void)hipFuncSetAttribute((const void*)k_gemm2, hipFuncAttributeMaxDynamicSharedMemorySize, G2_LDS);
    k_ln<<<256, 512, 0, stream>>>(x, ws);
    k_gemm1<<<256, 512, G1_LDS, stream>>>(wqkv, wg, wu, wo, wd, ws);
    k_gemm2<<<256, 512, G2_LDS, stream>>>(x, wo, wd, ws, (u16*)d_out);
    k_probe<<<1, 64, 0, stream>>>(ws);
}
```

```cpp
#include <hip/hip_runtime.h>
#include <hip/hip_bf16.h>
#include <stdint.h>

typedef __bf16 bf16x8 __attribute__((ext_vector_type(8)));
typedef __bf16 bf16x4 __attribute__((ext_vector_type(4)));
typedef __bf16 bf16x2 __attribute__((ext_vector_type(2)));
typedef float f32x4 __attribute__((ext_vector_type(4)));
typedef float f32x2 __attribute__((ext_vector_type(2)));
typedef unsigned u32x4 __attribute__((ext_vector_type(4)));
typedef unsigned u32x2 __attribute__((ext_vector_type(2)));
typedef unsigned short u16;
#define LAS __attribute__((address_space(3)))
#define GAS __attribute__((address_space(1)))
#define DEVI __device__ __forceinline__

constexpr int S_ = 2048, D_ = 1024, F_ = 3584;
constexpr int MB_ = S_ / 16;
constexpr size_t SLAB_B = (size_t)MB_ * 1024;
constexpr size_t WS_XN = 0;
constexpr size_t WS_HO = 32 * SLAB_B;
constexpr size_t WS_XB = WS_HO + 144 * SLAB_B;
constexpr size_t WS_FL = WS_XB + 256 * 32768;

DEVI unsigned pk_bf16(float lo, float hi) {
    bf16x2 v = {(__bf16)lo, (__bf16)hi};
    return __builtin_bit_cast(unsigned, v);
}
DEVI float bf_lo(unsigned u) { return __uint_as_float(u << 16); }
DEVI float bf_hi(unsigned u) { return __uint_as_float(u & 0xffff0000u); }
DEVI float rbf(float f) { return (float)(__bf16)f; }
DEVI bf16x8 as_frag(u32x4 v) { return __builtin_bit_cast(bf16x8, v); }
#define MFMA16(a, b, c) __builtin_amdgcn_mfma_f32_16x16x32_bf16((a), (b), (c), 0, 0, 0)
#define SBAR() __builtin_amdgcn_sched_barrier(0)
#define MFMA_ACC(c, a, b) asm volatile("v_mfma_f32_16x16x32_bf16 %0, %1, %2, %0" : "+v"(c) : "v"(a), "v"(b))
#define MFMA_DRAIN() asm volatile("s_nop 15" ::: "memory")
#define BAR() __builtin_amdgcn_s_barrier()

#define TL_ENTRY()
#define TL_EXIT(ws, kern)
#define STAMP_DECL
#define STAMP_ARGS
#define STAMP_PASS
#define STAMP_SEG(i)
#define STAMP_START()
#define STAMP_SEGI(i)
#define STAMP_OUT(ws, kern, wid, lane)

DEVI int fresh_lane() { int l; asm volatile("v_mbcnt_lo_u32_b32 %0, -1, 0\n\tv_mbcnt_hi_u32_b32 %0, -1, %0" : "=v"(l)); return l; }

DEVI bf16x8 tr_frag(LAS const char* p, int off0, int off1) {
    bf16x4 lo = __builtin_amdgcn_ds_read_tr16_b64_v4bf16((LAS bf16x4*)(p + off0));
    bf16x4 hi = __builtin_amdgcn_ds_read_tr16_b64_v4bf16((LAS bf16x4*)(p + off1));
    return __builtin_shufflevector(lo, hi, 0, 1, 2, 3, 4, 5, 6, 7);
}

DEVI void glds16(const char* sbase, unsigned voff, unsigned ldsdst) {
    unsigned keep;
    asm volatile("s_mov_b32 %0, m0\n\ts_mov_b32 m0, %3\n\ts_nop 0\n\tglobal_load_lds_dwordx4 %1, %2\n\ts_mov_b32 m0, %0"
                 : "=&s"(keep) : "v"(voff), "s"(sbase), "s"(ldsdst) : "memory");
}

__global__ __launch_bounds__(512) void k_ln(const u16* __restrict__ x, char* __restrict__ ws) {
    __shared__ float red[2][8][8];
    TL_ENTRY();
    const int tid = threadIdx.x, w = tid >> 6, l = tid & 63, r = l & 7, c8 = l >> 3;
    const int row = blockIdx.x * 8 + r;
    if (tid < 16) ((unsigned*)(ws + WS_FL))[blockIdx.x * 16 + tid] = 0u;
    u32x4 v[2];
#pragma unroll
    for (int i = 0; i < 2; ++i) v[i] = *(const u32x4*)(x + (size_t)row * D_ + (16 * w + 8 * i + c8) * 8);
    float f[16];
#pragma unroll
    for (int i = 0; i < 2; ++i)
#pragma unroll
        for (int j = 0; j < 4; ++j) { f[i * 8 + 2 * j] = bf_lo(v[i][j]); f[i * 8 + 2 * j + 1] = bf_hi(v[i][j]); }
    float s = 0.f;
#pragma unroll
    for (int j = 0; j < 16; ++j) s += f[j];
    s += __shfl_xor(s, 8); s += __shfl_xor(s, 16); s += __shfl_xor(s, 32);
    if (c8 == 0) red[0][w][r] = s;
    __syncthreads();
    float tot = 0.f;
#pragma unroll
    for (int k = 0; k < 8; ++k) tot += red[0][k][r];
    const float mean = tot * (1.0f / D_);
    float q = 0.f;
#pragma unroll
    for (int j = 0; j < 16; ++j) { const float d = f[j] - mean; q += d * d; }
    q += __shfl_xor(q, 8); q += __shfl_xor(q, 16); q += __shfl_xor(q, 32);
    if (c8 == 0) red[1][w][r] = q;
    __syncthreads();
    float qt = 0.f;
#pragma unroll
    for (int k = 0; k < 8; ++k) qt += red[1][k][r];
    const float mb = rbf(mean);
    const float vb = rbf(qt * (1.0f / D_));
    const float den = rbf(sqrtf(rbf(vb + 1e-5f)));
    const float rc = 1.0f / den;
    const int mblk = row >> 4, rr = row & 15;
#pragma unroll
    for (int i = 0; i < 2; ++i) {
        u32x4 o;
#pragma unroll
        for (int j = 0; j < 4; ++j) {
            float y[2];
#pragma unroll
            for (int e = 0; e < 2; ++e) {
                const float d = rbf(f[i * 8 + 2 * j + e] - mb);
                float qq = d * rc;
                qq = fmaf(fmaf(-qq, den, d), rc, qq);
                y[e] = qq;
            }
            o[j] = pk_bf16(y[0], y[1]);
        }
        const int ch = 16 * w + 8 * i + c8, slab = ch >> 2, g = ch & 3;
        asm volatile("global_store_dwordx4 %0, %1, off sc1\n\ts_nop 1" :: "v"(ws + WS_XN + slab * SLAB_B + (size_t)mblk * 1024 + (g * 16 + rr) * 16), "v"(o) : "memory");
    }
    TL_EXIT(ws, 0);
}

constexpr int G1_AST = 17 * 1024;
constexpr int G1_BST = 21 * 1024;
constexpr int G1_REG = 7168;
constexpr int G1_BOFF = 4 * G1_AST;
constexpr int G1_XOFF = G1_BOFF + 4 * G1_BST;
constexpr int G1_LDS = G1_XOFF + 8192;

typedef unsigned long long u64;
#define FRAG_REGS "v192", "v193", "v194", "v195", "v196", "v197", "v198", "v199", "v200", "v201", "v202", "v203", "v204", "v205", "v206", "v207", "v208", "v209", "v210", "v211", "v212", "v213", "v214", "v215", "v216", "v217", "v218", "v219", "v220", "v221", "v222", "v223", "v224", "v225", "v226", "v227", "v228", "v229", "v230", "v231", "v232", "v233", "v234", "v235", "v236", "v237", "v238", "v239", "v240", "v241", "v242", "v243", "v244", "v245", "v246", "v247", "v248", "v249", "v250", "v251", "v252", "v253", "v254", "v255"
constexpr int FA = 192, FB = 208, FAH = 248, FBH = 252;
struct G1Ld {
    const char *pa, *p2, *pg, *pu;
    unsigned stepa, step2, stepg;
    unsigned va, v2, vg;
    unsigned la, l2, s2, lg, lu;
    const char* warm; unsigned lw, ldum;
    char* pf;
    u64 ma, m2, mg;
};

#define WAIT_LGKM0() __builtin_amdgcn_s_waitcnt(0xC07F)
#define MEMFENCE() asm volatile("" ::: "memory")
#define OPAQUE_S(x) asm volatile("" : "+s"(x))

template <int LOFF, bool TWO>
DEVI void g1_piece(const char* base, unsigned voff, unsigned lds, u64 msk) {
    asm volatile("s_add_u32 m0, %2, %4\n\ts_mov_b64 exec, %3\n\tglobal_load_lds_dwordx4 %0, %1\n\t"
                 ".if %5\n\tglobal_load_lds_dwordx4 %0, %1 offset:1024\n\t.endif\n\ts_mov_b64 exec, -1"
                 :: "v"(voff), "s"(base), "s"(lds), "s"(msk), "i"(LOFF), "i"(TWO ? 1 : 0) : "memory", "scc");
}
template <int SLOT> DEVI void g1_pa(G1Ld& L) { g1_piece<SLOT * G1_AST, true>(L.pa, L.va, L.la, L.ma); L.pa += L.stepa; OPAQUE_S(L.pa); }
template <int SLOT> DEVI void g1_p2(G1Ld& L) { g1_piece<0, false>(L.p2, L.v2, L.l2 + SLOT * L.s2, L.m2); L.p2 += L.step2; OPAQUE_S(L.p2); }
template <int SLOT> DEVI void g1_pg(G1Ld& L) { g1_piece<SLOT * G1_BST, false>(L.pg, L.vg, L.lg, L.mg); L.pg += L.stepg; OPAQUE_S(L.pg); }
template <int SLOT> DEVI void g1_pu(G1Ld& L) { g1_piece<SLOT * G1_BST, false>(L.pu, L.vg, L.lu, L.mg); L.pu += L.stepg; OPAQUE_S(L.pu); }
template <int SLOT> DEVI void g1_issue(G1Ld& L) { g1_pa<SLOT>(L); g1_p2<SLOT>(L); g1_pg<SLOT>(L); g1_pu<SLOT>(L); }
template <int GRP> DEVI void g1_tail(G1Ld& L, bool tail) {
    L.p2 = tail ? L.p2 - L.step2 : L.p2; L.pg = tail ? L.pg - L.stepg : L.pg; L.pu = tail ? L.pu - L.stepg : L.pu;
    L.step2 = tail ? 0u : L.step2; L.stepg = tail ? 0u : L.stepg;
    L.m2 = tail ? 1ull : L.m2; L.mg = tail ? 1ull : L.mg;
    if (GRP == 0) L.pa = tail ? L.warm : L.pa;
    else { L.pa = tail ? L.pa - L.stepa : L.pa; L.ma = tail ? 1ull : L.ma; L.warm = tail ? L.warm - 1024 : L.warm; L.lw = tail ? L.ldum : L.lw; }
    L.stepa = tail ? 0u : L.stepa;
}

struct G1Ptr { unsigned pA, pH, pB01, pB23, pBh01, pBh23; };

template <int REG, int OFF> DEVI void ds_tr(unsigned base) { asm volatile("ds_read_b64_tr_b16 v[%c1:%c2], %0 offset:%c3" :: "v"(base), "i"(REG), "i"(REG + 1), "i"(OFF) : "memory"); }
template <int REG, int OFF> DEVI void ds_128(unsigned base) { asm volatile("ds_read_b128 v[%c1:%c2], %0 offset:%c3" :: "v"(base), "i"(REG), "i"(REG + 3), "i"(OFF) : "memory"); }
template <int RA_, int RB_> DEVI void mfma_fr(f32x4& c) { asm volatile("v_mfma_f32_16x16x32_bf16 %0, v[%c1:%c2], v[%c3:%c4], %0" : "+v"(c) : "i"(RA_), "i"(RA_ + 3), "i"(RB_), "i"(RB_ + 3)); }
template <int GRP, int SL, int n, int h> DEVI void g1_ldb(const G1Ptr& P) {
    constexpr int off = (SL & 1) * G1_BST + (GRP == 0 ? (n < 6 ? n * 32 : (n < 8 ? G1_REG + (n - 6) * 32 : 2 * G1_REG + (n - 8) * 32))
                                                      : (n < 5 ? G1_REG + (2 + n) * 32 : 2 * G1_REG + (n - 3) * 32));
    ds_tr<FB + 4 * n + 2 * h, off + h * 8 * 224>((SL < 2) ? P.pB01 : P.pB23);
}
template <int SL, int m> DEVI void g1_lda(const G1Ptr& P) { ds_128<FA + 4 * m, SL * G1_AST + (1 + m) * 1024>(P.pA); }
template <int SL> DEVI void g1_ldah(const G1Ptr& P) { ds_128<FAH, SL * G1_AST>(P.pH); }
template <int SL, int h> DEVI void g1_ldbh(const G1Ptr& P) { ds_tr<FBH + 2 * h, (SL & 1) * G1_BST + h * 8 * 224>((SL < 2) ? P.pBh01 : P.pBh23); }

template <int GRP, int ST>
DEVI void g1_stage(f32x4 (&acc)[4][10], f32x4& acch, G1Ld& L, bool tail, const G1Ptr& P) {
    constexpr int NX = (ST + 1) & 3, IS = (ST + 3) & 3;
#define MM(m, n) do { if (GRP == 0 && ((n) == 4 || (n) == 5)) mfma_fr<FA + 4 * (m), FB + 4 * (n)>(acc[m][n]); else mfma_fr<FB + 4 * (n), FA + 4 * (m)>(acc[m][n]); } while (0)
#define RB(n, h) g1_ldb<GRP, NX, n, h>(P)
#define RC(n, h) g1_ldb<GRP, ST, n, h>(P)
#define RA(m) g1_lda<NX, m>(P)
#define G0(x) do { if (GRP == 0) { x; } } while (0)
    asm volatile("s_waitcnt lgkmcnt(9)" ::: "memory");
    MM(0, 0); RC(8, 0);
    asm volatile("s_waitcnt lgkmcnt(9)" ::: "memory");
    MM(1, 0); RC(8, 1);
    MM(1, 1); RC(9, 0); MM(0, 1); RC(9, 1);
    MM(0, 2); G0(g1_pa<IS>(L)); MM(1, 2);
    MM(1, 3); G0(g1_p2<IS>(L)); MM(0, 3); G0(g1_pg<IS>(L));
    MM(0, 4); G0(g1_pu<IS>(L)); MM(1, 4);
    G0((mfma_fr<FBH, FAH>(acch)));
    asm volatile("s_waitcnt vmcnt(10) lgkmcnt(0)\n\ts_barrier" ::: "memory", FRAG_REGS);
    if (GRP == 1) {
        if (ST == 0) g1_tail<1>(L, tail);
        g1_issue<ST>(L);
        if (ST == 1) { g1_piece<0, false>(L.warm, L.va, L.lw, L.ma); L.warm += 1024; OPAQUE_S(L.warm); }
        if (ST == 2) {
            const u32x4 ones = {0xffffffffu, 0xffffffffu, 0xffffffffu, 0xffffffffu};
            asm volatile("global_store_dwordx4 %0, %1, %2 sc1\n\ts_nop 1" :: "v"(L.va), "v"(ones), "s"(L.pf) : "memory", FRAG_REGS);
            L.pf += 1024; OPAQUE_S(L.pf);
        }
    }
    MM(2, 0); MM(3, 0); RB(0, 0);
    MM(3, 1); RB(0, 1); MM(2, 1); RB(1, 0);
    MM(2, 2); RB(1, 1); MM(3, 2); RB(2, 0);
    MM(3, 3); RB(2, 1); MM(2, 3); RB(3, 0);
    MM(2, 4); RB(3, 1); MM(3, 4); RB(4, 0);
    MM(0, 5); RB(4, 1); MM(1, 5); G0(g1_ldah<NX>(P));
    MM(1, 6); G0((g1_ldbh<NX, 0>(P))); MM(0, 6); G0((g1_ldbh<NX, 1>(P)));
    MM(0, 7); MM(1, 7);
    MM(1, 8); MM(0, 8);
    MM(0, 9); RA(0); MM(1, 9); RA(1);
    MM(2, 5); MM(3, 5); RB(5, 0);
    MM(3, 6); RB(5, 1); MM(2, 6); RB(6, 0);
    MM(2, 7); RB(6, 1); MM(3, 7); RB(7, 0);
    MM(3, 8); RB(7, 1); MM(2, 8);
    MM(2, 9); RA(2); MM(3, 9); RA(3);
#undef MM
#undef RB
#undef RC
#undef RA
#undef G0
}

DEVI void st16(char* dst, u32x4 v) {
    asm volatile("global_store_dwordx4 %0, %1, off sc1\n\ts_nop 1" :: "v"(dst), "v"(v) : "memory");
}

DEVI unsigned glu2(float g0, float g1, float u0, float u1) {
    const f32x2 t = (f32x2){g0, g1} * -1.4426950408889634f;
    const f32x2 d = (f32x2){__builtin_amdgcn_exp2f(t[0]), __builtin_amdgcn_exp2f(t[1])} + 1.0f;
    const f32x2 h = (f32x2){u0, u1} * (f32x2){__builtin_amdgcn_rcpf(d[0]), __builtin_amdgcn_rcpf(d[1])};
    return pk_bf16(h[0], h[1]);
}

DEVI void glu_store(const f32x4& g0, const f32x4& u0, const f32x4& g1, const f32x4& u1, char* dst) {
    unsigned a0 = glu2(g0[0], g0[1], u0[0], u0[1]), a1 = glu2(g0[2], g0[3], u0[2], u0[3]);
    unsigned b0 = glu2(g1[0], g1[1], u1[0], u1[1]), b1 = glu2(g1[2], g1[3], u1[2], u1[3]);
    auto r0 = __builtin_amdgcn_permlane16_swap(a0, b0, false, false);
    auto r1 = __builtin_amdgcn_permlane16_swap(a1, b1, false, false);
    st16(dst, (u32x4){r0[0], r1[0], r0[1], r1[1]});
    SBAR();
}

#define O_FLAG_AT 0
#define POLL_SLEEP 8
#define G2_DELTA 5
#define G2_RCV 34
constexpr int G2_ST = 16384;
constexpr int G2_NS = 6;
constexpr int G2_XOFF = G2_NS * G2_ST;
constexpr int G2_F = 36 - G2_DELTA, G2_T0 = 24, G2_XST = 32, G2_H = 8;
constexpr unsigned SPIN_CAP = 1u << 17;
static_assert(G2_F - 4 > G2_T0 && 64 * (G2_F - G2_H) <= 16 * 112 && G2_T0 + 4 < G2_F && G2_RCV > G2_F && G2_RCV <= 35 && G2_XOFF + 32768 <= G1_XOFF, "");
constexpr int g2_gops(int s) { return (s + 5 <= 35 ? 2 : 0) + (s + 4 <= 35 ? (s + 4 < G2_F ? 2 : 4) : 0); }
constexpr int g2_xops(int s) { return (s == 1 || s == G2_T0 ? 1 : 0) + (s == G2_F ? 4 : 0) + (s == G2_XST ? 2 : 0) + (s == G2_RCV ? 4 : 0); }
constexpr int g2_after_poll2(int t) { int n = 0; for (int s = (t - 3 > G2_T0 + 1 ? t - 3 : G2_T0 + 1); s < t; ++s) n += g2_gops(s) + g2_xops(s); return n; }
constexpr int g2_wait(int t) {
    return t == 0 ? 6 : t == 1 ? 9 : t == 2 ? 12 : t == 3 ? 14 : t == 4 ? 8
         : t == G2_F - 4 ? g2_after_poll2(t)
         : g2_xops(t - 4) + g2_gops(t - 3) + g2_xops(t - 3) + g2_gops(t - 2) + g2_xops(t - 2) + g2_gops(t - 1) + g2_xops(t - 1);
}
constexpr int g2_grp(int t) { return t < G2_F ? t : G2_F + 2 * (t - G2_F); }

struct G2W {
    const char *pw, *pwh, *pwmid;
    unsigned vw0, vw1, vw2, ldsw;
};
struct G2St {
    G2W W;
    const char *pa, *pah, *pamid;
    unsigned va0, va1;
    LAS const char* pB[2][4];
    LAS char* xw; LAS const char* xr;
    char* snd; const char* rcv; const u16* xp;
    char* myflag; const char* hflags; unsigned hfo; u32x2 hf; bool poller;
    u32x4 A6[6][2], AT[36 + G2_DELTA - G2_T0 - 4][2];
    bf16x8 B[8];
    f32x4 acc[2][8];
    u32x4 xres[2], rc[4];
};
template <int G> DEVI u32x4 (&g2_set(G2St& T))[2] { if constexpr (G < G2_T0 + 4) return T.A6[G % 6]; else return T.AT[G - G2_T0 - 4]; }
template <int LOFF> DEVI void g2_glds(const char* base, unsigned voff, unsigned lds) {
    asm volatile("s_add_u32 m0, %2, %3\n\ts_nop 0\n\tglobal_load_lds_dwordx4 %0, %1" :: "v"(voff), "s"(base), "s"(lds), "i"(LOFF) : "memory", "scc");
}
DEVI void g2_lda(u32x4& dst, const char* base, unsigned voff) {
    asm volatile("global_load_dwordx4 %0, %1, %2 sc1" : "=v"(dst) : "v"(voff), "s"(base) : "memory");
}
template <int ST, int I> DEVI void g2_w(G2W& W) {
    constexpr int SLOT = ST % G2_NS;
    if (I == 0) { if (ST == G2_H) W.pw = W.pwh; if (ST == G2_F) W.pw = W.pwmid; g2_glds<SLOT * G2_ST>(W.pw, W.vw0, W.ldsw); }
    else { g2_glds<SLOT * G2_ST + 8192>(W.pw, ST < G2_F ? W.vw1 : W.vw2, W.ldsw); W.pw += (ST < G2_F ? 64 : 128) * 2048; OPAQUE_S(W.pw); }
}
template <int G, int I> DEVI void g2_a(G2St& T) {
    if (I == 0) { if (G == G2_H) T.pa = T.pah; if (G == G2_F) T.pa = T.pamid; g2_lda(g2_set<G>(T)[0], T.pa, T.va0); }
    else { g2_lda(g2_set<G>(T)[1], T.pa, T.va1); T.pa += 2 * SLAB_B; OPAQUE_S(T.pa); }
}
template <int ST> DEVI bf16x8 g2_ldb(G2St& T, int n) {
    constexpr int SLOT = ST % G2_NS;
    return tr_frag(T.pB[SLOT / 3][n & 3], (SLOT % 3) * G2_ST + (n >> 2) * 8192, (SLOT % 3) * G2_ST + (n >> 2) * 8192 + 512);
}
DEVI void flag_set(char* p) { const unsigned one = 1u, zero = 0u; asm volatile("s_mov_b64 exec, 1\n\tglobal_store_dword %0, %1, %2 sc1\n\ts_mov_b64 exec, -1" :: "v"(zero), "v"(one), "s"(p) : "memory"); }
DEVI void flags_wait(u32x2 f, const char* base, unsigned voff) {
    for (unsigned spin = 0; __any(f[0] == 0u || f[1] == 0u) && spin < SPIN_CAP; ++spin) {
        if (spin) __builtin_amdgcn_s_sleep(POLL_SLEEP);
        asm volatile("global_load_dwordx2 %0, %1, %2 sc1\n\ts_waitcnt vmcnt(0)" : "=&v"(f) : "v"(voff), "s"(base) : "memory");
    }
}

template <int t>
DEVI void g2_stage(G2St& T) {
    constexpr bool TB = t >= G2_F, ISW = t + 5 <= 35, ISA = t + 4 <= 35, ISA4 = ISA && t + 4 >= G2_F, RL = t < 35;
    constexpr int G0 = g2_grp(t), G1 = TB ? G0 + 1 : G0, GN = g2_grp(ISA ? t + 4 : 0), GN1 = ISA4 ? GN + 1 : GN;
    u32x4 (&Aa)[2] = g2_set<G0>(T); u32x4 (&Ab)[2] = g2_set<G1>(T);
    if (TB) asm volatile("s_waitcnt vmcnt(%4)" : "+v"(Aa[0]), "+v"(Aa[1]), "+v"(Ab[0]), "+v"(Ab[1]) : "i"(g2_wait(t)) : "memory");
    else if (t == 4 || t == G2_F - 4) asm volatile("s_waitcnt vmcnt(%3)" : "+v"(Aa[0]), "+v"(Aa[1]), "+v"(T.hf) : "i"(g2_wait(t)) : "memory");
    else asm volatile("s_waitcnt vmcnt(%2)" : "+v"(Aa[0]), "+v"(Aa[1]) : "i"(g2_wait(t)) : "memory");
    if (t == 0) flag_set(T.myflag);
    if ((t == 4 || t == G2_F - 4) && T.poller) flags_wait(T.hf, T.hflags, T.hfo);
    if (t == G2_F) WAIT_LGKM0();
    MEMFENCE(); SBAR(); BAR(); MEMFENCE(); SBAR();
    f32x4 ho[4];
    if (t == G2_F) {
#pragma unroll
        for (int j = 0; j < 4; ++j) ho[j] = *(LAS const f32x4*)(T.xr + j * 1024);
        SBAR();
    }
#define P2(n) do { constexpr int c_ = TB ? ((n) & 3) : (n); u32x4 (&A_)[2] = (n) < 4 ? Aa : Ab; \
                   T.acc[(n) & 1][c_] = MFMA16(T.B[n], as_frag(A_[(n) & 1]), T.acc[(n) & 1][c_]); SBAR(); T.acc[~(n) & 1][c_] = MFMA16(T.B[n], as_frag(A_[~(n) & 1]), T.acc[~(n) & 1][c_]); SBAR(); \
                   if (RL) { T.B[n] = g2_ldb<t + 1>(T, n); SBAR(); } } while (0)
    P2(0); if (ISW) { g2_w<t + 5, 0>(T.W); SBAR(); }
    P2(1);
    P2(2); if (ISW) { g2_w<t + 5, 1>(T.W); SBAR(); }
    P2(3); if (ISA4) { g2_a<GN, 0>(T); SBAR(); }
    P2(4); if (ISA4) { g2_a<GN, 1>(T); SBAR(); } else if (ISA) { g2_a<GN, 0>(T); SBAR(); }
    P2(5); if (ISA4) { g2_a<GN1, 0>(T); SBAR(); }
    P2(6); if (ISA4) { g2_a<GN1, 1>(T); SBAR(); } else if (ISA) { g2_a<GN, 1>(T); SBAR(); }
    P2(7);
#undef P2
    if (t == G2_T0) T.hfo ^= 64u;
    if (t == 1 || t == G2_T0) { asm volatile("global_load_dwordx2 %0, %1, %2 sc1" : "=v"(T.hf) : "v"(T.hfo), "s"(T.hflags) : "memory"); SBAR(); }
    if (t == G2_F - 1) {
#pragma unroll
        for (int j = 0; j < 4; ++j) *(LAS f32x4*)(T.xw + j * 1024) = T.acc[1][4 + j];
        SBAR();
    }
    if (t == G2_F) {
#pragma unroll
        for (int j = 0; j < 4; ++j) {
            u32x4 w = __builtin_bit_cast(u32x4, T.acc[0][4 + j] + ho[j]);
#pragma unroll
            for (int e = 0; e < 4; ++e) w[e] = w[e] < 0xfffffffeu ? w[e] : 0xfffffffeu;
            asm volatile("global_store_dwordx4 %0, %1, off offset:%2 sc1\n\ts_nop 1" :: "v"(T.snd), "v"(w), "i"(j * 1024) : "memory");
        }
        SBAR();
    }
    if (t == G2_XST) { asm volatile("global_load_dwordx4 %0, %2, off\n\tglobal_load_dwordx4 %1, %2, off offset:64" : "=&v"(T.xres[0]), "=&v"(T.xres[1]) : "v"(T.xp) : "memory"); SBAR(); }
    if (t == G2_RCV) {
        asm volatile("global_load_dwordx4 %0, %4, off sc1\n\tglobal_load_dwordx4 %1, %4, off offset:1024 sc1\n\tglobal_load_dwordx4 %2, %4, off offset:2048 sc1\n\tglobal_load_dwordx4 %3, %4, off offset:3072 sc1"
                     : "=&v"(T.rc[0]), "=&v"(T.rc[1]), "=&v"(T.rc[2]), "=&v"(T.rc[3]) : "v"(T.rcv) : "memory");
        SBAR();
    }
}
template <int t, int E> DEVI void g2_run(G2St& T) { g2_stage<t>(T); if constexpr (t + 1 < E) g2_run<t + 1, E>(T); }

DEVI unsigned bsel(unsigned m, unsigned a, unsigned b) { return (m & a) | (~m & b); }
typedef short s16x4 __attribute__((ext_vector_type(4)));
#define MFMA16K16(a, b, c) __builtin_amdgcn_mfma_f32_16x16x16bf16_1k(__builtin_bit_cast(s16x4, a), __builtin_bit_cast(s16x4, b), (c), 0, 0, 0)
template <bool MAX> DEVI float rows_reduce(const float (&v)[4]) {
    auto a = __builtin_amdgcn_permlane16_swap(__float_as_uint(v[0]), __float_as_uint(v[1]), false, false);
    auto b = __builtin_amdgcn_permlane16_swap(__float_as_uint(v[2]), __float_as_uint(v[3]), false, false);
    const float t = MAX ? fmaxf(__uint_as_float(a[0]), __uint_as_float(a[1])) : __uint_as_float(a[0]) + __uint_as_float(a[1]);
    const float u = MAX ? fmaxf(__uint_as_float(b[0]), __uint_as_float(b[1])) : __uint_as_float(b[0]) + __uint_as_float(b[1]);
    auto c = __builtin_amdgcn_permlane32_swap(__float_as_uint(t), __float_as_uint(u), false, false);
    return MAX ? fmaxf(__uint_as_float(c[0]), __uint_as_float(c[1])) : __uint_as_float(c[0]) + __uint_as_float(c[1]);
}
DEVI void rows_spread(float w, float (&v)[4]) {
    auto c = __builtin_amdgcn_permlane32_swap(__float_as_uint(w), __float_as_uint(w), false, false);
    auto a = __builtin_amdgcn_permlane16_swap(c[0], c[0], false, false);
    auto b = __builtin_amdgcn_permlane16_swap(c[1], c[1], false, false);
    v[0] = __uint_as_float(a[0]); v[1] = __uint_as_float(a[1]); v[2] = __uint_as_float(b[0]); v[3] = __uint_as_float(b[1]);
}

template <int GRP>
DEVI void g1_body(G1Ld& L, G2W& W, LAS char* lds, char* ws, int rt, int ct, int wm, int kh, const char* oflags STAMP_ARGS) {
    G1Ptr P;
    {
        const int lane = fresh_lane(), g = lane >> 4, q = (lane & 15) >> 2, p = lane & 3;
        const unsigned ldsb = (unsigned)(size_t)lds;
        P.pA = ldsb + wm * 4096 + lane * 16;
        P.pH = ldsb + lane * 16;
        const int brow = (16 * (g >> 1) + 4 * (g & 1) + q) * 224 + p * 8;
        P.pB01 = ldsb + G1_BOFF + brow;
        P.pB23 = P.pB01 + 2 * G1_BST;
        P.pBh01 = P.pB01 + (2 + wm) * 32;
        P.pBh23 = P.pB23 + (2 + wm) * 32;
    }
    f32x4 acc[4][10], acch = {0.f, 0.f, 0.f, 0.f};
#pragma unroll
    for (int m = 0; m < 4; ++m)
#pragma unroll
        for (int n = 0; n < 10; ++n) acc[m][n] = (f32x4){0.f, 0.f, 0.f, 0.f};

    g1_issue<0>(L); g1_issue<1>(L);
#pragma unroll
    for (int m = 0; m < 4; ++m)
        asm volatile("" : "+v"(acc[m][0]), "+v"(acc[m][1]), "+v"(acc[m][2]), "+v"(acc[m][3]), "+v"(acc[m][4]), "+v"(acc[m][5]), "+v"(acc[m][6]), "+v"(acc[m][7]), "+v"(acc[m][8]), "+v"(acc[m][9]), "+v"(acch));
    asm volatile("s_waitcnt vmcnt(5)\n\ts_barrier" ::: "memory", FRAG_REGS);
    g1_ldb<GRP, 0, 0, 0>(P); g1_ldb<GRP, 0, 0, 1>(P); g1_ldb<GRP, 0, 1, 0>(P); g1_ldb<GRP, 0, 1, 1>(P); g1_ldb<GRP, 0, 2, 0>(P); g1_ldb<GRP, 0, 2, 1>(P);
    g1_ldb<GRP, 0, 3, 0>(P); g1_ldb<GRP, 0, 3, 1>(P); g1_ldb<GRP, 0, 4, 0>(P); g1_ldb<GRP, 0, 4, 1>(P);
    if (GRP == 0) { g1_ldah<0>(P); g1_ldbh<0, 0>(P); g1_ldbh<0, 1>(P); }
    g1_lda<0, 0>(P); g1_lda<0, 1>(P);
    g1_ldb<GRP, 0, 5, 0>(P); g1_ldb<GRP, 0, 5, 1>(P); g1_ldb<GRP, 0, 6, 0>(P); g1_ldb<GRP, 0, 6, 1>(P); g1_ldb<GRP, 0, 7, 0>(P); g1_ldb<GRP, 0, 7, 1>(P);
    g1_lda<0, 2>(P); g1_lda<0, 3>(P);
    g1_issue<2>(L);
    if (GRP == 1) g1_issue<3>(L);
    STAMP_SEG(0);
#pragma unroll 1
    for (int it = 0; it < 8; ++it) {
        g1_stage<GRP, 0>(acc, acch, L, it == 7, P);
        if (GRP == 0) g1_tail<0>(L, it == 7);
        g1_stage<GRP, 1>(acc, acch, L, it == 7, P);
        g1_stage<GRP, 2>(acc, acch, L, it == 7, P);
        g1_stage<GRP, 3>(acc, acch, L, it == 7, P);
    }
    asm volatile("s_waitcnt vmcnt(0) lgkmcnt(0)\n\ts_nop 15" ::: "memory", FRAG_REGS);
    SBAR();
    STAMP_SEG(0);

    const int lane = fresh_lane(), g = lane >> 4, i16 = lane & 15;
    char* ho = ws + WS_HO;
    const int mb0 = 16 * rt + 4 * wm;
    const unsigned hl = (unsigned)((g & 1) * 1024 + (g >> 1) * 256 + i16 * 16);
    {
        const int wid = GRP * 4 + wm, r = lane >> 3, c = (lane & 7) ^ ((r & 2) | ((wid & 1) << 2));
        W.vw0 = (unsigned)((8 * wid + r) * 2048 + c * 16 + kh * 128);
        W.vw1 = W.vw0 ^ 128;
        W.vw2 = W.vw0 + 64 * 2048;
    }
#define G2_PRE() do { g2_w<0, 0>(W); g2_w<0, 1>(W); g2_w<1, 0>(W); g2_w<1, 1>(W); g2_w<2, 0>(W); g2_w<2, 1>(W); g2_w<3, 0>(W); g2_w<3, 1>(W); g2_w<4, 0>(W); g2_w<4, 1>(W); } while (0)
    if (GRP == 1) {
        SBAR(); BAR(); MEMFENCE(); SBAR();
        G2_PRE();
#pragma unroll
        for (int n = 0; n < 5; ++n) {
            const int cb = 7 * ct + 2 + n;
            char* dst = ho + (size_t)(32 + (cb >> 1)) * SLAB_B + (cb & 1) * 512 + (size_t)mb0 * 1024 + hl;
#pragma unroll
            for (int m = 0; m < 4; m += 2) glu_store(acc[m][n], acc[m][5 + n], acc[m + 1][n], acc[m + 1][5 + n], dst + m * 1024);
        }
        asm volatile("s_waitcnt vmcnt(10)" ::: "memory");
    } else {
        u32x4 Qf[4], Kf[4]; u32x2 Vh[4][2];
#pragma unroll
        for (int m = 0; m < 4; ++m) {
            Qf[m] = (u32x4){pk_bf16(acc[m][0][0], acc[m][0][1]), pk_bf16(acc[m][0][2], acc[m][0][3]), pk_bf16(acc[m][1][0], acc[m][1][1]), pk_bf16(acc[m][1][2], acc[m][1][3])};
            Kf[m] = (u32x4){pk_bf16(acc[m][2][0], acc[m][2][1]), pk_bf16(acc[m][2][2], acc[m][2][3]), pk_bf16(acc[m][3][0], acc[m][3][1]), pk_bf16(acc[m][3][2], acc[m][3][3])};
#pragma unroll
            for (int b = 0; b < 2; ++b) Vh[m][b] = (u32x2){pk_bf16(acc[m][4 + b][0], acc[m][4 + b][1]), pk_bf16(acc[m][4 + b][2], acc[m][4 + b][3])};
        }
        LAS char* xs = lds + G1_XOFF;
        if (wm < 3) {
            *(LAS u32x4*)(xs + (wm + 1) * 2048 + lane * 16) = Kf[3];
            *(LAS u32x4*)(xs + (wm + 1) * 2048 + 1024 + lane * 16) = (u32x4){Vh[3][0][0], Vh[3][0][1], Vh[3][1][0], Vh[3][1][1]};
        }
        {
            const unsigned h0 = pk_bf16(acch[0], acch[1]), h1 = pk_bf16(acch[2], acch[3]);
            if (wm < 2) {
                *(LAS u32x2*)(xs + lane * 16 + wm * 8) = (u32x2){h0, h1};
            } else {
                LAS char* t = xs + 1024 + ((i16 >> 2) * 16 + 4 * g) * 16 + (wm - 2) * 8 + (i16 & 3) * 2;
                *(LAS u16*)(t) = (u16)h0; *(LAS u16*)(t + 16) = (u16)(h0 >> 16); *(LAS u16*)(t + 32) = (u16)h1; *(LAS u16*)(t + 48) = (u16)(h1 >> 16);
            }
        }
        asm volatile("s_waitcnt lgkmcnt(0)" ::: "memory");
        SBAR(); BAR(); MEMFENCE(); SBAR();
        const u32x4 Kp = *(LAS const u32x4*)(xs + wm * 2048 + lane * 16);
        const u32x4 Vp = *(LAS const u32x4*)(xs + wm * 2048 + 1024 + lane * 16);
        const int dc = i16 - 4 * g - (i16 & 1);
        unsigned modd = (i16 & 1) ? ~0u : 0u, mc0 = dc >= 0 ? ~0u : 0u, mc1 = dc >= 2 ? ~0u : 0u;
        asm volatile("" : "+v"(modd), "+v"(mc0), "+v"(mc1));
        const unsigned half = (modd & 0xffff0000u) | (~modd & 0x0000ffffu);
        const unsigned pp0 = half & ~mc0, pp1 = half & ~mc1, pc0 = half & mc0, pc1 = half & mc1;
        const float sc = 0.17677669529663687f * 1.4426950408889634f;
        const f32x4 z = {0.f, 0.f, 0.f, 0.f};
        float e0[4], e1[4], red[4];
#pragma unroll
        for (int m = 0; m < 4; ++m) {
            u32x4 s0 = __builtin_bit_cast(u32x4, MFMA16(as_frag(m ? Kf[m ? m - 1 : 0] : Kp), as_frag(Qf[m]), z));
            const u32x4 s1 = __builtin_bit_cast(u32x4, MFMA16(as_frag(Kf[m]), as_frag(Qf[m]), z));
            if (m == 0 && rt == 0 && wm == 0) { const unsigned c = __float_as_uint(-1e9f / 0.17677669529663687f); s0 = (u32x4){c, c, c, c}; }
            e0[m] = __uint_as_float(bsel(modd, bsel(mc0, s1[1], s0[1]), bsel(mc0, s1[0], s0[0])));
            e1[m] = __uint_as_float(bsel(modd, bsel(mc1, s1[3], s0[3]), bsel(mc1, s1[2], s0[2])));
            red[m] = fmaxf(e0[m], e1[m]);
        }
        {
            rows_spread(rows_reduce<true>(red), red);
        }
#pragma unroll
        for (int m = 0; m < 4; ++m) {
            e0[m] = __builtin_amdgcn_exp2f((e0[m] - red[m]) * sc); e1[m] = __builtin_amdgcn_exp2f((e1[m] - red[m]) * sc);
            red[m] = e0[m] + e1[m];
        }
        {
            float w = __builtin_amdgcn_rcpf(rows_reduce<false>(red));
            rows_spread(w, red);
        }
        char* od = ho + (size_t)ct * SLAB_B + (size_t)mb0 * 1024 + (((g & 1) << 1) | (g >> 1)) * 256 + i16 * 16;
#pragma unroll
        for (int m = 0; m < 4; ++m) {
            const float p0 = e0[m] * red[m], p1 = e1[m] * red[m];
            const unsigned w0 = pk_bf16(p0, p0), w1 = pk_bf16(p1, p1);
            const u32x2 Pp = {w0 & pp0, w1 & pp1}, Pc = {w0 & pc0, w1 & pc1};
            const u32x2 Vp0 = m ? Vh[m ? m - 1 : 0][0] : (u32x2){Vp[0], Vp[1]}, Vp1 = m ? Vh[m ? m - 1 : 0][1] : (u32x2){Vp[2], Vp[3]};
            const f32x4 o0 = MFMA16K16(Vh[m][0], Pc, MFMA16K16(Vp0, Pp, z));
            const f32x4 o1 = MFMA16K16(Vh[m][1], Pc, MFMA16K16(Vp1, Pp, z));
            unsigned x0 = pk_bf16(o0[0], o0[1]), x1 = pk_bf16(o0[2], o0[3]), y0 = pk_bf16(o1[0], o1[1]), y1 = pk_bf16(o1[2], o1[3]);
            auto r0 = __builtin_amdgcn_permlane16_swap(x0, y0, false, false);
            auto r1 = __builtin_amdgcn_permlane16_swap(x1, y1, false, false);
            st16(od + m * 1024, (u32x4){r0[0], r1[0], r0[1], r1[1]});
        }
        SBAR();
        G2_PRE();
#pragma unroll
        for (int n = 0; n < 2; ++n) {
            const int cb = 7 * ct + n;
            char* dst = ho + (size_t)(32 + (cb >> 1)) * SLAB_B + (cb & 1) * 512 + (size_t)mb0 * 1024 + hl;
#pragma unroll
            for (int m = 0; m < 4; m += 2) glu_store(acc[m][6 + n], acc[m][8 + n], acc[m + 1][6 + n], acc[m + 1][8 + n], dst + m * 1024);
            if (n == O_FLAG_AT) { asm volatile("s_waitcnt vmcnt(%0)" :: "i"(2 * O_FLAG_AT + 12) : "memory"); flag_set(ws + WS_FL + (rt * 4 + wm) * 512 + ct * 4); }
        }
        STAMP_SEG(1);
        if (wm == 0) flags_wait((u32x2){0u, 0u}, oflags, (unsigned)((lane >> 5) * 512 + (kh ^ 1) * 64 + (lane & 7) * 8));
        else asm volatile("s_waitcnt vmcnt(5)" ::: "memory");
    }
#undef G2_PRE
    STAMP_SEG(2);
}

__global__ __launch_bounds__(512, 2) void k_main(const char* __restrict__ wqkv, const char* __restrict__ wg, const char* __restrict__ wu, const char* __restrict__ wo, const char* __restrict__ wd, char* __restrict__ ws,
                                                const u16* __restrict__ x, u16* __restrict__ out) {
    extern __shared__ __attribute__((aligned(16))) char smem[];
    TL_ENTRY();
    asm volatile("" : "+s"(wqkv), "+s"(wg), "+s"(wu), "+s"(wo), "+s"(wd), "+s"(ws), "+s"(x), "+s"(out));
    LAS char* lds = (LAS char*)smem;
    const int tid = threadIdx.x, lane = tid & 63;
    const int wid = __builtin_amdgcn_readfirstlane(tid >> 6);
    const int bid = blockIdx.x, xcd = bid & 7, idx = bid >> 3;
    const int ct = 4 * xcd + (idx & 3), rt = idx >> 2;
    const unsigned ldsb = (unsigned)(size_t)lds;
    G1Ld L;
    {
        const int c = wid * 64 + lane, rho = c / 14, cc = c - rho * 14;
        const int k = (rho & ~12) | ((rho & 4) << 1) | ((rho & 8) >> 1);
        const int c2 = cc < 12 ? cc : 11;
        const bool last = wid == 7;
        L.pa = ws + WS_XN + (size_t)(16 * rt + 2 * wid) * 1024;
        L.stepa = (unsigned)SLAB_B;
        L.va = lane * 16;
        L.la = ldsb + (1 + 2 * wid) * 1024;
        L.p2 = last ? ws + WS_XN + (size_t)(rt == 0 ? 0 : 16 * rt - 1) * 1024 : wqkv + ct * 64;
        L.step2 = last ? (unsigned)SLAB_B : 32u * 3072u * 2u;
        L.v2 = last ? (unsigned)((rt == 0 ? (lane & 48) : lane) * 16) : (unsigned)(k * 6144 + (c2 >> 2) * 2048 + (c2 & 3) * 16);
        L.l2 = last ? ldsb : ldsb + G1_BOFF + wid * 1024;
        L.s2 = last ? G1_AST : G1_BST;
        L.pg = wg + ct * 224;
        L.pu = wu + ct * 224;
        L.stepg = 32u * 3584u * 2u;
        L.vg = last ? 0u : (unsigned)(k * 7168 + cc * 16);
        L.lg = last ? ldsb + G1_BOFF + 192 : ldsb + G1_BOFF + G1_REG + wid * 1024;
        L.lu = last ? ldsb + G1_BOFF + 192 : ldsb + G1_BOFF + 2 * G1_REG + wid * 1024;
        L.ma = ~0ull; L.m2 = ~0ull; L.mg = last ? 1ull : ~0ull;
    }
    L.warm = wid < 4 ? wo + bid * 8192 + wid * 2048 : wd + bid * 28672 + (wid & 3) * 7168;
    L.lw = ldsb + G1_XOFF + (wid & 3) * 1024; L.ldum = ldsb + G1_BOFF + 192;
    L.pf = ws + WS_XB + (size_t)bid * 32768 + (wid & 3) * 8192;
    const int kh = (bid >> 2) & 1, side = kh ^ 1, rt2 = 2 * rt + ((bid >> 1) & 1), ct2 = 4 * (bid & 1) + (idx & 3);
    const int kg = wid >> 2, wm = wid & 3;
    const char* flq = ws + WS_FL + (rt * 4 + (bid & 2)) * 512;
    G2St T;
    T.W.pw = wo + (size_t)(side * 512) * 2048 + ct2 * 256;
    T.W.pwh = wd + (size_t)(side ? 3584 - 64 * (G2_F - G2_H) : 0) * 2048 + ct2 * 256;
    T.W.pwmid = wd + (size_t)(64 * (G2_F - G2_H)) * 2048 + ct2 * 256;
    T.W.ldsw = ldsb + wid * 1024;
    STAMP_DECL STAMP_START();
    if (wid < 4) g1_body<0>(L, T.W, lds, ws, rt, ct, wm, kh, flq STAMP_PASS);
    else g1_body<1>(L, T.W, lds, ws, rt, ct, wm, kh, flq STAMP_PASS);

    const int lane2 = fresh_lane(), g = lane2 >> 4, i16 = lane2 & 15, q = i16 >> 2, p = lane2 & 3;
    {
        const char* a0 = ws + WS_HO + (size_t)kg * SLAB_B + (size_t)(8 * rt2 + 2 * wm) * 1024;
        T.pa = a0 + (size_t)(16 * side) * SLAB_B;
        T.pah = a0 + (size_t)(side ? 144 - 2 * (G2_F - G2_H) : 32) * SLAB_B;
        T.pamid = a0 + (size_t)(32 + 2 * (G2_F - G2_H)) * SLAB_B;
        T.va0 = lane2 * 16 + kg * 1024; T.va1 = T.va0 ^ 1024;
        T.myflag = ws + WS_FL + (rt * 4 + wm) * 512 + 256 + kg * 128 + ct * 4;
        T.poller = wid == 0;
        T.hflags = T.poller ? flq + 256 : ws + WS_XB + (size_t)bid * 32768;
        T.hfo = (unsigned)((lane2 >> 5) * 512 + ((lane2 >> 3) & 1) * 128 + side * 64 + (lane2 & 7) * 8);
    }
#pragma unroll
    for (int n = 0; n < 4; ++n) {
        T.pB[0][n] = lds + kg * 4096 + (8 * g + q) * 128 + (((2 * n + (p >> 1)) ^ ((q & 2) | ((g & 1) << 2))) * 16) + (p & 1) * 8;
        T.pB[1][n] = T.pB[0][n] + 3 * G2_ST;
        asm volatile("" : "+v"(T.pB[0][n]), "+v"(T.pB[1][n]));
    }
    MEMFENCE(); SBAR(); BAR(); MEMFENCE(); SBAR();
    g2_a<0, 0>(T); g2_a<0, 1>(T); g2_a<1, 0>(T); g2_a<1, 1>(T); g2_a<2, 0>(T); g2_a<2, 1>(T); g2_a<3, 0>(T); g2_a<3, 1>(T);
    SBAR();
#pragma unroll
    for (int m = 0; m < 2; ++m)
#pragma unroll
        for (int n = 0; n < 8; ++n) T.acc[m][n] = (f32x4){0.f, 0.f, 0.f, 0.f};
#pragma unroll
    for (int n = 0; n < 8; ++n) T.B[n] = g2_ldb<0>(T, n);
    SBAR();
    STAMP_SEG(3);
    g2_run<0, 6>(T);
    STAMP_SEG(4);
#pragma unroll 1
    for (int it = 1; it < G2_T0 / 6; ++it) g2_run<G2_T0 - 6, G2_T0>(T);
    const int orow = 128 * rt2 + 32 * wm + 16 * kg + i16, ocol = 128 * ct2 + 64 * kh + (g & 1) * 16 + (g >> 1) * 8;
    T.xw = lds + G2_XOFF + wid * 4096 + lane2 * 16;
    T.xr = lds + G2_XOFF + (wid ^ 4) * 4096 + lane2 * 16;
    T.snd = ws + WS_XB + (size_t)bid * 32768 + wid * 4096 + lane2 * 16;
    T.rcv = ws + WS_XB + (size_t)(bid ^ 4) * 32768 + wid * 4096 + lane2 * 16;
    T.xp = x + (size_t)orow * D_ + ocol;
    g2_run<G2_T0, 36>(T);
    STAMP_SEG(5);

#pragma unroll
    for (int j = 0; j < 4; ++j) *(LAS f32x4*)(T.xw + j * 1024) = T.acc[1][j];
    WAIT_LGKM0();
    MEMFENCE(); SBAR(); BAR(); MEMFENCE(); SBAR();
#pragma unroll
    for (int j = 0; j < 4; ++j) T.acc[0][j] += *(LAS const f32x4*)(T.xr + j * 1024);
    asm volatile("s_waitcnt vmcnt(0)" : "+v"(T.xres[0]), "+v"(T.xres[1]), "+v"(T.rc[0]), "+v"(T.rc[1]), "+v"(T.rc[2]), "+v"(T.rc[3]) :: "memory");
    for (unsigned spin = 0;; ++spin) {
        unsigned mx = 0;
#pragma unroll
        for (int j = 0; j < 4; ++j) { const unsigned a = T.rc[j][0] > T.rc[j][1] ? T.rc[j][0] : T.rc[j][1], b = T.rc[j][2] > T.rc[j][3] ? T.rc[j][2] : T.rc[j][3]; mx = mx > a ? mx : a; mx = mx > b ? mx : b; }
        if (!__any(mx == 0xffffffffu) || spin > SPIN_CAP) break;
        __builtin_amdgcn_s_sleep(4);
        asm volatile("global_load_dwordx4 %0, %4, off sc1\n\tglobal_load_dwordx4 %1, %4, off offset:1024 sc1\n\tglobal_load_dwordx4 %2, %4, off offset:2048 sc1\n\tglobal_load_dwordx4 %3, %4, off offset:3072 sc1\n\ts_waitcnt vmcnt(0)"
                     : "=&v"(T.rc[0]), "=&v"(T.rc[1]), "=&v"(T.rc[2]), "=&v"(T.rc[3]) : "v"(T.rcv) : "memory");
    }
    u16* orp = out + (size_t)orow * D_ + ocol;
#pragma unroll
    for (int j = 0; j < 4; j += 2) {
        const f32x4 s0 = T.acc[0][j] + __builtin_bit_cast(f32x4, T.rc[j]), s1 = T.acc[0][j + 1] + __builtin_bit_cast(f32x4, T.rc[j + 1]);
        float v[8];
#pragma unroll
        for (int e = 0; e < 4; ++e) { auto r = __builtin_amdgcn_permlane16_swap(__float_as_uint(s0[e]), __float_as_uint(s1[e]), false, false); v[e] = __uint_as_float(r[0]); v[4 + e] = __uint_as_float(r[1]); }
        const u32x4 xv = T.xres[j >> 1];
        u32x4 o;
#pragma unroll
        for (int e = 0; e < 4; ++e) o[e] = pk_bf16(v[2 * e] + bf_lo(xv[e]), v[2 * e + 1] + bf_hi(xv[e]));
        *(GAS u32x4*)(size_t)(orp + 16 * j) = o;
    }
    STAMP_SEG(6);
    STAMP_OUT(ws, 0, wid, lane2);
    STAMP_OUT(ws, 1, wid, lane2);
    TL_EXIT(ws, 1);
    TL_EXIT(ws, 2);
}

extern "C" void kernel_launch(void* const* d_in, const int* in_sizes, int n_in,
                              void* d_out, int out_size, void* d_ws, size_t ws_size,
                              hipStream_t stream) {
    const u16* x = (const u16*)d_in[0];
    const char* wqkv = (const char*)d_in[1];
    const char* wo = (const char*)d_in[2];
    const char* wg = (const char*)d_in[4];
    const char* wu = (const char*)d_in[5];
    const char* wd = (const char*)d_in[6];
    char* ws = (char*)d_ws;
    (void)hipFuncSetAttribute((const void*)k_main, hipFuncAttributeMaxDynamicSharedMemorySize, G1_LDS);
    k_ln<<<256, 512, 0, stream>>>(x, ws);
    k_main<<<256, 512, G1_LDS, stream>>>(wqkv, wg, wu, wo, wd, ws, x, (u16*)d_out);
}
```

```cpp
#include <hip/hip_runtime.h>
#include <hip/hip_bf16.h>
#include <stdint.h>

typedef __bf16 bf16x8 __attribute__((ext_vector_type(8)));
typedef __bf16 bf16x4 __attribute__((ext_vector_type(4)));
typedef __bf16 bf16x2 __attribute__((ext_vector_type(2)));
typedef float f32x4 __attribute__((ext_vector_type(4)));
typedef float f32x2 __attribute__((ext_vector_type(2)));
typedef unsigned u32x4 __attribute__((ext_vector_type(4)));
typedef unsigned u32x2 __attribute__((ext_vector_type(2)));
typedef unsigned short u16;
#define LAS __attribute__((address_space(3)))
#define GAS __attribute__((address_space(1)))
#define DEVI __device__ __forceinline__

constexpr int S_ = 2048, D_ = 1024, F_ = 3584;
constexpr int MB_ = S_ / 16;
constexpr size_t SLAB_B = (size_t)MB_ * 1024;
constexpr size_t WS_XN = 0;
constexpr size_t WS_HO = 32 * SLAB_B;
constexpr size_t WS_XB = WS_HO + 144 * SLAB_B;
constexpr size_t WS_FL = WS_XB + 256 * 32768;

DEVI unsigned pk_bf16(float lo, float hi) {
    bf16x2 v = {(__bf16)lo, (__bf16)hi};
    return __builtin_bit_cast(unsigned, v);
}
DEVI float bf_lo(unsigned u) { return __uint_as_float(u << 16); }
DEVI float bf_hi(unsigned u) { return __uint_as_float(u & 0xffff0000u); }
DEVI float rbf(float f) { return (float)(__bf16)f; }
DEVI bf16x8 as_frag(u32x4 v) { return __builtin_bit_cast(bf16x8, v); }
#define MFMA16(a, b, c) __builtin_amdgcn_mfma_f32_16x16x32_bf16((a), (b), (c), 0, 0, 0)
#define SBAR() __builtin_amdgcn_sched_barrier(0)
#define MFMA_ACC(c, a, b) asm volatile("v_mfma_f32_16x16x32_bf16 %0, %1, %2, %0" : "+v"(c) : "v"(a), "v"(b))
#define MFMA_DRAIN() asm volatile("s_nop 15" ::: "memory")
#define BAR() __builtin_amdgcn_s_barrier()

#define TL_ENTRY()
#define TL_EXIT(ws, kern)
#define STAMP_DECL
#define STAMP_ARGS
#define STAMP_PASS
#define STAMP_SEG(i)
#define STAMP_START()
#define STAMP_SEGI(i)
#define STAMP_OUT(ws, kern, wid, lane)

DEVI int fresh_lane() { int l; asm volatile("v_mbcnt_lo_u32_b32 %0, -1, 0\n\tv_mbcnt_hi_u32_b32 %0, -1, %0" : "=v"(l)); return l; }

DEVI bf16x8 tr_frag(LAS const char* p, int off0, int off1) {
    bf16x4 lo = __builtin_amdgcn_ds_read_tr16_b64_v4bf16((LAS bf16x4*)(p + off0));
    bf16x4 hi = __builtin_amdgcn_ds_read_tr16_b64_v4bf16((LAS bf16x4*)(p + off1));
    return __builtin_shufflevector(lo, hi, 0, 1, 2, 3, 4, 5, 6, 7);
}

DEVI void glds16(const char* sbase, unsigned voff, unsigned ldsdst) {
    unsigned keep;
    asm volatile("s_mov_b32 %0, m0\n\ts_mov_b32 m0, %3\n\ts_nop 0\n\tglobal_load_lds_dwordx4 %1, %2\n\ts_mov_b32 m0, %0"
                 : "=&s"(keep) : "v"(voff), "s"(sbase), "s"(ldsdst) : "memory");
}

__global__ __launch_bounds__(512) void k_ln(const u16* __restrict__ x, char* __restrict__ ws) {
    __shared__ float red[2][8][8];
    TL_ENTRY();
    const int tid = threadIdx.x, w = tid >> 6, l = tid & 63, r = l & 7, c8 = l >> 3;
    const int row = blockIdx.x * 8 + r;
    if (tid < 16) ((unsigned*)(ws + WS_FL))[blockIdx.x * 16 + tid] = 0u;
    u32x4 v[2];
#pragma unroll
    for (int i = 0; i < 2; ++i) v[i] = *(const u32x4*)(x + (size_t)row * D_ + (16 * w + 8 * i + c8) * 8);
    float f[16];
#pragma unroll
    for (int i = 0; i < 2; ++i)
#pragma unroll
        for (int j = 0; j < 4; ++j) { f[i * 8 + 2 * j] = bf_lo(v[i][j]); f[i * 8 + 2 * j + 1] = bf_hi(v[i][j]); }
    float s = 0.f;
#pragma unroll
    for (int j = 0; j < 16; ++j) s += f[j];
    s += __shfl_xor(s, 8); s += __shfl_xor(s, 16); s += __shfl_xor(s, 32);
    if (c8 == 0) red[0][w][r] = s;
    __syncthreads();
    float tot = 0.f;
#pragma unroll
    for (int k = 0; k < 8; ++k) tot += red[0][k][r];
    const float mean = tot * (1.0f / D_);
    float q = 0.f;
#pragma unroll
    for (int j = 0; j < 16; ++j) { const float d = f[j] - mean; q += d * d; }
    q += __shfl_xor(q, 8); q += __shfl_xor(q, 16); q += __shfl_xor(q, 32);
    if (c8 == 0) red[1][w][r] = q;
    __syncthreads();
    float qt = 0.f;
#pragma unroll
    for (int k = 0; k < 8; ++k) qt += red[1][k][r];
    const float mb = rbf(mean);
    const float vb = rbf(qt * (1.0f / D_));
    const float den = rbf(sqrtf(rbf(vb + 1e-5f)));
    const float rc = 1.0f / den;
    const int mblk = row >> 4, rr = row & 15;
#pragma unroll
    for (int i = 0; i < 2; ++i) {
        u32x4 o;
#pragma unroll
        for (int j = 0; j < 4; ++j) {
            float y[2];
#pragma unroll
            for (int e = 0; e < 2; ++e) {
                const float d = rbf(f[i * 8 + 2 * j + e] - mb);
                float qq = d * rc;
                qq = fmaf(fmaf(-qq, den, d), rc, qq);
                y[e] = qq;
            }
            o[j] = pk_bf16(y[0], y[1]);
        }
        const int ch = 16 * w + 8 * i + c8, slab = ch >> 2, g = ch & 3;
        asm volatile("global_store_dwordx4 %0, %1, off sc1\n\ts_nop 1" :: "v"(ws + WS_XN + slab * SLAB_B + (size_t)mblk * 1024 + (g * 16 + rr) * 16), "v"(o) : "memory");
    }
    TL_EXIT(ws, 0);
}

constexpr int G1_AST = 17 * 1024;
constexpr int G1_BST = 21 * 1024;
constexpr int G1_REG = 7168;
constexpr int G1_BOFF = 4 * G1_AST;
constexpr int G1_XOFF = G1_BOFF + 4 * G1_BST;
constexpr int G1_LDS = G1_XOFF + 8192;

typedef unsigned long long u64;
#define FRAG_REGS "v192", "v193", "v194", "v195", "v196", "v197", "v198", "v199", "v200", "v201", "v202", "v203", "v204", "v205", "v206", "v207", "v208", "v209", "v210", "v211", "v212", "v213", "v214", "v215", "v216", "v217", "v218", "v219", "v220", "v221", "v222", "v223", "v224", "v225", "v226", "v227", "v228", "v229", "v230", "v231", "v232", "v233", "v234", "v235", "v236", "v237", "v238", "v239", "v240", "v241", "v242", "v243", "v244", "v245", "v246", "v247", "v248", "v249", "v250", "v251", "v252", "v253", "v254", "v255"
constexpr int FA = 192, FB = 208, FAH = 248, FBH = 252;
struct G1Ld {
    const char *pa, *p2, *pg, *pu;
    unsigned stepa, step2, stepg;
    unsigned va, v2, vg;
    unsigned la, l2, s2, lg, lu;
    const char* warm; unsigned lw, ldum;
    char* pf;
    u64 ma, m2, mg;
};

#define WAIT_LGKM0() __builtin_amdgcn_s_waitcnt(0xC07F)
#define MEMFENCE() asm volatile("" ::: "memory")
#define OPAQUE_S(x) asm volatile("" : "+s"(x))

template <int LOFF, bool TWO>
DEVI void g1_piece(const char* base, unsigned voff, unsigned lds, u64 msk) {
    asm volatile("s_add_u32 m0, %2, %4\n\ts_mov_b64 exec, %3\n\tglobal_load_lds_dwordx4 %0, %1\n\t"
                 ".if %5\n\tglobal_load_lds_dwordx4 %0, %1 offset:1024\n\t.endif\n\ts_mov_b64 exec, -1"
                 :: "v"(voff), "s"(base), "s"(lds), "s"(msk), "i"(LOFF), "i"(TWO ? 1 : 0) : "memory", "scc");
}
template <int SLOT> DEVI void g1_pa(G1Ld& L) { g1_piece<SLOT * G1_AST, true>(L.pa, L.va, L.la, L.ma); L.pa += L.stepa; OPAQUE_S(L.pa); }
template <int SLOT> DEVI void g1_p2(G1Ld& L) { g1_piece<0, false>(L.p2, L.v2, L.l2 + SLOT * L.s2, L.m2); L.p2 += L.step2; OPAQUE_S(L.p2); }
template <int SLOT> DEVI void g1_pg(G1Ld& L) { g1_piece<SLOT * G1_BST, false>(L.pg, L.vg, L.lg, L.mg); L.pg += L.stepg; OPAQUE_S(L.pg); }
template <int SLOT> DEVI void g1_pu(G1Ld& L) { g1_piece<SLOT * G1_BST, false>(L.pu, L.vg, L.lu, L.mg); L.pu += L.stepg; OPAQUE_S(L.pu); }
template <int SLOT> DEVI void g1_issue(G1Ld& L) { g1_pa<SLOT>(L); g1_p2<SLOT>(L); g1_pg<SLOT>(L); g1_pu<SLOT>(L); }
template <int GRP> DEVI void g1_tail(G1Ld& L, bool tail) {
    L.p2 = tail ? L.p2 - L.step2 : L.p2; L.pg = tail ? L.pg - L.stepg : L.pg; L.pu = tail ? L.pu - L.stepg : L.pu;
    L.step2 = tail ? 0u : L.step2; L.stepg = tail ? 0u : L.stepg;
    L.m2 = tail ? 1ull : L.m2; L.mg = tail ? 1ull : L.mg;
    if (GRP == 0) L.pa = tail ? L.warm : L.pa;
    else { L.pa = tail ? L.pa - L.stepa : L.pa; L.ma = tail ? 1ull : L.ma; L.warm = tail ? L.warm - 1024 : L.warm; L.lw = tail ? L.ldum : L.lw; }
    L.stepa = tail ? 0u : L.stepa;
}

struct G1Ptr { unsigned pA, pH, pB01, pB23, pBh01, pBh23; };

template <int REG, int OFF> DEVI void ds_tr(unsigned base) { asm volatile("ds_read_b64_tr_b16 v[%c1:%c2], %0 offset:%c3" :: "v"(base), "i"(REG), "i"(REG + 1), "i"(OFF) : "memory"); }
template <int REG, int OFF> DEVI void ds_128(unsigned base) { asm volatile("ds_read_b128 v[%c1:%c2], %0 offset:%c3" :: "v"(base), "i"(REG), "i"(REG + 3), "i"(OFF) : "memory"); }
template <int RA_, int RB_> DEVI void mfma_fr(f32x4& c) { asm volatile("v_mfma_f32_16x16x32_bf16 %0, v[%c1:%c2], v[%c3:%c4], %0" : "+v"(c) : "i"(RA_), "i"(RA_ + 3), "i"(RB_), "i"(RB_ + 3)); }
template <int GRP, int SL, int n, int h> DEVI void g1_ldb(const G1Ptr& P) {
    constexpr int off = (SL & 1) * G1_BST + (GRP == 0 ? (n < 6 ? n * 32 : (n < 8 ? G1_REG + (n - 6) * 32 : 2 * G1_REG + (n - 8) * 32))
                                                      : (n < 5 ? G1_REG + (2 + n) * 32 : 2 * G1_REG + (n - 3) * 32));
    ds_tr<FB + 4 * n + 2 * h, off + h * 8 * 224>((SL < 2) ? P.pB01 : P.pB23);
}
template <int SL, int m> DEVI void g1_lda(const G1Ptr& P) { ds_128<FA + 4 * m, SL * G1_AST + (1 + m) * 1024>(P.pA); }
template <int SL> DEVI void g1_ldah(const G1Ptr& P) { ds_128<FAH, SL * G1_AST>(P.pH); }
template <int SL, int h> DEVI void g1_ldbh(const G1Ptr& P) { ds_tr<FBH + 2 * h, (SL & 1) * G1_BST + h * 8 * 224>((SL < 2) ? P.pBh01 : P.pBh23); }

template <int GRP, int ST>
DEVI void g1_stage(f32x4 (&acc)[4][10], f32x4& acch, G1Ld& L, bool tail, const G1Ptr& P) {
    constexpr int NX = (ST + 1) & 3, IS = (ST + 3) & 3;
#define MM(m, n) do { if (GRP == 0 && ((n) == 4 || (n) == 5)) mfma_fr<FA + 4 * (m), FB + 4 * (n)>(acc[m][n]); else mfma_fr<FB + 4 * (n), FA + 4 * (m)>(acc[m][n]); } while (0)
#define RB(n, h) g1_ldb<GRP, NX, n, h>(P)
#define RC(n, h) g1_ldb<GRP, ST, n, h>(P)
#define RA(m) g1_lda<NX, m>(P)
#define G0(x) do { if (GRP == 0) { x; } } while (0)
    asm volatile("s_waitcnt lgkmcnt(9)" ::: "memory");
    MM(0, 0); RC(8, 0);
    asm volatile("s_waitcnt lgkmcnt(9)" ::: "memory");
    MM(1, 0); RC(8, 1);
    MM(1, 1); RC(9, 0); MM(0, 1); RC(9, 1);
    MM(0, 2); G0(g1_pa<IS>(L)); MM(1, 2);
    MM(1, 3); G0(g1_p2<IS>(L)); MM(0, 3); G0(g1_pg<IS>(L));
    MM(0, 4); G0(g1_pu<IS>(L)); MM(1, 4);
    G0((mfma_fr<FBH, FAH>(acch)));
    asm volatile("s_waitcnt vmcnt(10) lgkmcnt(0)\n\ts_barrier" ::: "memory", FRAG_REGS);
    if (GRP == 1) {
        if (ST == 0) g1_tail<1>(L, tail);
        g1_issue<ST>(L);
        if (ST == 1) { g1_piece<0, false>(L.warm, L.va, L.lw, L.ma); L.warm += 1024; OPAQUE_S(L.warm); }
        if (ST == 2) {
            const u32x4 ones = {0xffffffffu, 0xffffffffu, 0xffffffffu, 0xffffffffu};
            asm volatile("global_store_dwordx4 %0, %1, %2 sc1\n\ts_nop 1" :: "v"(L.va), "v"(ones), "s"(L.pf) : "memory", FRAG_REGS);
            L.pf += 1024; OPAQUE_S(L.pf);
        }
    }
    MM(2, 0); MM(3, 0); RB(0, 0);
    MM(3, 1); RB(0, 1); MM(2, 1); RB(1, 0);
    MM(2, 2); RB(1, 1); MM(3, 2); RB(2, 0);
    MM(3, 3); RB(2, 1); MM(2, 3); RB(3, 0);
    MM(2, 4); RB(3, 1); MM(3, 4); RB(4, 0);
    MM(0, 5); RB(4, 1); MM(1, 5); G0(g1_ldah<NX>(P));
    MM(1, 6); G0((g1_ldbh<NX, 0>(P))); MM(0, 6); G0((g1_ldbh<NX, 1>(P)));
    MM(0, 7); MM(1, 7);
    MM(1, 8); MM(0, 8);
    MM(0, 9); RA(0); MM(1, 9); RA(1);
    MM(2, 5); MM(3, 5); RB(5, 0);
    MM(3, 6); RB(5, 1); MM(2, 6); RB(6, 0);
    MM(2, 7); RB(6, 1); MM(3, 7); RB(7, 0);
    MM(3, 8); RB(7, 1); MM(2, 8);
    MM(2, 9); RA(2); MM(3, 9); RA(3);
#undef MM
#undef RB
#undef RC
#undef RA
#undef G0
}

DEVI void st16(char* dst, u32x4 v) {
    asm volatile("global_store_dwordx4 %0, %1, off sc1\n\ts_nop 1" :: "v"(dst), "v"(v) : "memory");
}

DEVI unsigned glu2(float g0, float g1, float u0, float u1) {
    const f32x2 t = (f32x2){g0, g1} * -1.4426950408889634f;
    const f32x2 d = (f32x2){__builtin_amdgcn_exp2f(t[0]), __builtin_amdgcn_exp2f(t[1])} + 1.0f;
    const f32x2 h = (f32x2){u0, u1} * (f32x2){__builtin_amdgcn_rcpf(d[0]), __builtin_amdgcn_rcpf(d[1])};
    return pk_bf16(h[0], h[1]);
}

DEVI void glu_store(const f32x4& g0, const f32x4& u0, const f32x4& g1, const f32x4& u1, char* dst) {
    unsigned a0 = glu2(g0[0], g0[1], u0[0], u0[1]), a1 = glu2(g0[2], g0[3], u0[2], u0[3]);
    unsigned b0 = glu2(g1[0], g1[1], u1[0], u1[1]), b1 = glu2(g1[2], g1[3], u1[2], u1[3]);
    auto r0 = __builtin_amdgcn_permlane16_swap(a0, b0, false, false);
    auto r1 = __builtin_amdgcn_permlane16_swap(a1, b1, false, false);
    st16(dst, (u32x4){r0[0], r1[0], r0[1], r1[1]});
    SBAR();
}

#define O_FLAG_AT 1
#define POLL_SLEEP 8
#define G2_DELTA 5
#define G2_RCV 34
constexpr int G2_ST = 16384;
constexpr int G2_NS = 6;
constexpr int G2_XOFF = G2_NS * G2_ST;
constexpr int G2_F = 36 - G2_DELTA, G2_T0 = 24, G2_XST = 32, G2_H = 8;
constexpr unsigned SPIN_CAP = 1u << 17;
static_assert(G2_T0 + 4 < G2_F && G2_RCV > G2_F && G2_RCV <= 35 && G2_XOFF + 32768 <= G1_XOFF, "");
constexpr int g2_gops(int s) { return (s + 5 <= 35 ? 2 : 0) + (s + 4 <= 35 ? (s + 4 < G2_F ? 2 : 4) : 0); }
constexpr int g2_xops(int s) { return (s == 1 ? 1 : 0) + (s == G2_F ? 4 : 0) + (s == G2_XST ? 2 : 0) + (s == G2_RCV ? 4 : 0); }
constexpr int g2_wait(int t) {
    return t == 0 ? 6 : t == 1 ? 9 : t == 2 ? 12 : t == 3 ? 14 : t == 4 ? 8
         : g2_xops(t - 4) + g2_gops(t - 3) + g2_xops(t - 3) + g2_gops(t - 2) + g2_xops(t - 2) + g2_gops(t - 1) + g2_xops(t - 1);
}
constexpr int g2_grp(int t) { return t < G2_F ? t : G2_F + 2 * (t - G2_F); }

struct G2W {
    const char *pw, *pwh, *pwmid;
    unsigned vw0, vw1, vw2, ldsw;
};
struct G2St {
    G2W W;
    const char *pa, *pah, *pamid;
    unsigned va0, va1;
    LAS const char* pB[2][4];
    LAS char* xw; LAS const char* xr;
    char* snd; const char* rcv; const u16* xp;
    char* myflag; const char* hflags; unsigned hfo; u32x2 hf; bool poller;
    u32x4 A6[6][2], AT[36 + G2_DELTA - G2_T0 - 4][2];
    bf16x8 B[8];
    f32x4 acc[2][8];
    u32x4 xres[2], rc[4];
};
template <int G> DEVI u32x4 (&g2_set(G2St& T))[2] { if constexpr (G < G2_T0 + 4) return T.A6[G % 6]; else return T.AT[G - G2_T0 - 4]; }
template <int LOFF> DEVI void g2_glds(const char* base, unsigned voff, unsigned lds) {
    asm volatile("s_add_u32 m0, %2, %3\n\ts_nop 0\n\tglobal_load_lds_dwordx4 %0, %1" :: "v"(voff), "s"(base), "s"(lds), "i"(LOFF) : "memory", "scc");
}
DEVI void g2_lda(u32x4& dst, const char* base, unsigned voff) {
    asm volatile("global_load_dwordx4 %0, %1, %2 sc1" : "=v"(dst) : "v"(voff), "s"(base) : "memory");
}
template <int ST, int I> DEVI void g2_w(G2W& W) {
    constexpr int SLOT = ST % G2_NS;
    if (I == 0) { if (ST == G2_H) W.pw = W.pwh; if (ST == G2_F) W.pw = W.pwmid; g2_glds<SLOT * G2_ST>(W.pw, W.vw0, W.ldsw); }
    else { g2_glds<SLOT * G2_ST + 8192>(W.pw, ST < G2_F ? W.vw1 : W.vw2, W.ldsw); W.pw += (ST < G2_F ? 64 : 128) * 2048; OPAQUE_S(W.pw); }
}
template <int G, int I> DEVI void g2_a(G2St& T) {
    if (I == 0) { if (G == G2_H) T.pa = T.pah; if (G == G2_F) T.pa = T.pamid; g2_lda(g2_set<G>(T)[0], T.pa, T.va0); }
    else { g2_lda(g2_set<G>(T)[1], T.pa, T.va1); T.pa += 2 * SLAB_B; OPAQUE_S(T.pa); }
}
template <int ST> DEVI bf16x8 g2_ldb(G2St& T, int n) {
    constexpr int SLOT = ST % G2_NS;
    return tr_frag(T.pB[SLOT / 3][n & 3], (SLOT % 3) * G2_ST + (n >> 2) * 8192, (SLOT % 3) * G2_ST + (n >> 2) * 8192 + 512);
}
DEVI void flag_set(char* p) { const unsigned one = 1u, zero = 0u; asm volatile("s_mov_b64 exec, 1\n\tglobal_store_dword %0, %1, %2 sc1\n\ts_mov_b64 exec, -1" :: "v"(zero), "v"(one), "s"(p) : "memory"); }
DEVI void flags_wait(u32x2 f, const char* base, unsigned voff) {
    for (unsigned spin = 0; __any(f[0] == 0u || f[1] == 0u) && spin < SPIN_CAP; ++spin) {
        if (spin) __builtin_amdgcn_s_sleep(POLL_SLEEP);
        asm volatile("global_load_dwordx2 %0, %1, %2 sc1\n\ts_waitcnt vmcnt(0)" : "=&v"(f) : "v"(voff), "s"(base) : "memory");
    }
}

template <int t>
DEVI void g2_stage(G2St& T) {
    constexpr bool TB = t >= G2_F, ISW = t + 5 <= 35, ISA = t + 4 <= 35, ISA4 = ISA && t + 4 >= G2_F, RL = t < 35;
    constexpr int G0 = g2_grp(t), G1 = TB ? G0 + 1 : G0, GN = g2_grp(ISA ? t + 4 : 0), GN1 = ISA4 ? GN + 1 : GN;
    u32x4 (&Aa)[2] = g2_set<G0>(T); u32x4 (&Ab)[2] = g2_set<G1>(T);
    if (TB) asm volatile("s_waitcnt vmcnt(%4)" : "+v"(Aa[0]), "+v"(Aa[1]), "+v"(Ab[0]), "+v"(Ab[1]) : "i"(g2_wait(t)) : "memory");
    else if (t == 4) asm volatile("s_waitcnt vmcnt(%3)" : "+v"(Aa[0]), "+v"(Aa[1]), "+v"(T.hf) : "i"(g2_wait(t)) : "memory");
    else asm volatile("s_waitcnt vmcnt(%2)" : "+v"(Aa[0]), "+v"(Aa[1]) : "i"(g2_wait(t)) : "memory");
    if (t == 0) flag_set(T.myflag);
    if (t == 4 && T.poller) flags_wait(T.hf, T.hflags, T.hfo);
    if (t == G2_F) WAIT_LGKM0();
    MEMFENCE(); SBAR(); BAR(); MEMFENCE(); SBAR();
    f32x4 ho[4];
    if (t == G2_F) {
#pragma unroll
        for (int j = 0; j < 4; ++j) ho[j] = *(LAS const f32x4*)(T.xr + j * 1024);
        SBAR();
    }
#define P2(n) do { constexpr int c_ = TB ? ((n) & 3) : (n); u32x4 (&A_)[2] = (n) < 4 ? Aa : Ab; \
                   T.acc[(n) & 1][c_] = MFMA16(T.B[n], as_frag(A_[(n) & 1]), T.acc[(n) & 1][c_]); SBAR(); T.acc[~(n) & 1][c_] = MFMA16(T.B[n], as_frag(A_[~(n) & 1]), T.acc[~(n) & 1][c_]); SBAR(); \
                   if (RL) { T.B[n] = g2_ldb<t + 1>(T, n); SBAR(); } } while (0)
    P2(0); if (ISW) { g2_w<t + 5, 0>(T.W); SBAR(); }
    P2(1);
    P2(2); if (ISW) { g2_w<t + 5, 1>(T.W); SBAR(); }
    P2(3); if (ISA4) { g2_a<GN, 0>(T); SBAR(); }
    P2(4); if (ISA4) { g2_a<GN, 1>(T); SBAR(); } else if (ISA) { g2_a<GN, 0>(T); SBAR(); }
    P2(5); if (ISA4) { g2_a<GN1, 0>(T); SBAR(); }
    P2(6); if (ISA4) { g2_a<GN1, 1>(T); SBAR(); } else if (ISA) { g2_a<GN, 1>(T); SBAR(); }
    P2(7);
#undef P2
    if (t == 1) { asm volatile("global_load_dwordx2 %0, %1, %2 sc1" : "=v"(T.hf) : "v"(T.hfo), "s"(T.hflags) : "memory"); SBAR(); }
    if (t == G2_F - 1) {
#pragma unroll
        for (int j = 0; j < 4; ++j) *(LAS f32x4*)(T.xw + j * 1024) = T.acc[1][4 + j];
        SBAR();
    }
    if (t == G2_F) {
#pragma unroll
        for (int j = 0; j < 4; ++j) {
            u32x4 w = __builtin_bit_cast(u32x4, T.acc[0][4 + j] + ho[j]);
#pragma unroll
            for (int e = 0; e < 4; ++e) w[e] = w[e] < 0xfffffffeu ? w[e] : 0xfffffffeu;
            asm volatile("global_store_dwordx4 %0, %1, off offset:%2 sc1\n\ts_nop 1" :: "v"(T.snd), "v"(w), "i"(j * 1024) : "memory");
        }
        SBAR();
    }
    if (t == G2_XST) { asm volatile("global_load_dwordx4 %0, %2, off\n\tglobal_load_dwordx4 %1, %2, off offset:64" : "=&v"(T.xres[0]), "=&v"(T.xres[1]) : "v"(T.xp) : "memory"); SBAR(); }
    if (t == G2_RCV) {
        asm volatile("global_load_dwordx4 %0, %4, off sc1\n\tglobal_load_dwordx4 %1, %4, off offset:1024 sc1\n\tglobal_load_dwordx4 %2, %4, off offset:2048 sc1\n\tglobal_load_dwordx4 %3, %4, off offset:3072 sc1"
                     : "=&v"(T.rc[0]), "=&v"(T.rc[1]), "=&v"(T.rc[2]), "=&v"(T.rc[3]) : "v"(T.rcv) : "memory");
        SBAR();
    }
}
template <int t, int E> DEVI void g2_run(G2St& T) { g2_stage<t>(T); if constexpr (t + 1 < E) g2_run<t + 1, E>(T); }

DEVI unsigned bsel(unsigned m, unsigned a, unsigned b) { return (m & a) | (~m & b); }
typedef short s16x4 __attribute__((ext_vector_type(4)));
#define MFMA16K16(a, b, c) __builtin_amdgcn_mfma_f32_16x16x16bf16_1k(__builtin_bit_cast(s16x4, a), __builtin_bit_cast(s16x4, b), (c), 0, 0, 0)
template <bool MAX> DEVI float rows_reduce(const float (&v)[4]) {
    auto a = __builtin_amdgcn_permlane16_swap(__float_as_uint(v[0]), __float_as_uint(v[1]), false, false);
    auto b = __builtin_amdgcn_permlane16_swap(__float_as_uint(v[2]), __float_as_uint(v[3]), false, false);
    const float t = MAX ? fmaxf(__uint_as_float(a[0]), __uint_as_float(a[1])) : __uint_as_float(a[0]) + __uint_as_float(a[1]);
    const float u = MAX ? fmaxf(__uint_as_float(b[0]), __uint_as_float(b[1])) : __uint_as_float(b[0]) + __uint_as_float(b[1]);
    auto c = __builtin_amdgcn_permlane32_swap(__float_as_uint(t), __float_as_uint(u), false, false);
    return MAX ? fmaxf(__uint_as_float(c[0]), __uint_as_float(c[1])) : __uint_as_float(c[0]) + __uint_as_float(c[1]);
}
DEVI void rows_spread(float w, float (&v)[4]) {
    auto c = __builtin_amdgcn_permlane32_swap(__float_as_uint(w), __float_as_uint(w), false, false);
    auto a = __builtin_amdgcn_permlane16_swap(c[0], c[0], false, false);
    auto b = __builtin_amdgcn_permlane16_swap(c[1], c[1], false, false);
    v[0] = __uint_as_float(a[0]); v[1] = __uint_as_float(a[1]); v[2] = __uint_as_float(b[0]); v[3] = __uint_as_float(b[1]);
}

template <int GRP>
DEVI void g1_body(G1Ld& L, G2W& W, LAS char* lds, char* ws, int rt, int ct, int wm, int kh, const char* oflags STAMP_ARGS) {
    G1Ptr P;
    {
        const int lane = fresh_lane(), g = lane >> 4, q = (lane & 15) >> 2, p = lane & 3;
        const unsigned ldsb = (unsigned)(size_t)lds;
        P.pA = ldsb + wm * 4096 + lane * 16;
        P.pH = ldsb + lane * 16;
        const int brow = (16 * (g >> 1) + 4 * (g & 1) + q) * 224 + p * 8;
        P.pB01 = ldsb + G1_BOFF + brow;
        P.pB23 = P.pB01 + 2 * G1_BST;
        P.pBh01 = P.pB01 + (2 + wm) * 32;
        P.pBh23 = P.pB23 + (2 + wm) * 32;
    }
    f32x4 acc[4][10], acch = {0.f, 0.f, 0.f, 0.f};
#pragma unroll
    for (int m = 0; m < 4; ++m)
#pragma unroll
        for (int n = 0; n < 10; ++n) acc[m][n] = (f32x4){0.f, 0.f, 0.f, 0.f};

    g1_issue<0>(L); g1_issue<1>(L);
#pragma unroll
    for (int m = 0; m < 4; ++m)
        asm volatile("" : "+v"(acc[m][0]), "+v"(acc[m][1]), "+v"(acc[m][2]), "+v"(acc[m][3]), "+v"(acc[m][4]), "+v"(acc[m][5]), "+v"(acc[m][6]), "+v"(acc[m][7]), "+v"(acc[m][8]), "+v"(acc[m][9]), "+v"(acch));
    asm volatile("s_waitcnt vmcnt(5)\n\ts_barrier" ::: "memory", FRAG_REGS);
    g1_ldb<GRP, 0, 0, 0>(P); g1_ldb<GRP, 0, 0, 1>(P); g1_ldb<GRP, 0, 1, 0>(P); g1_ldb<GRP, 0, 1, 1>(P); g1_ldb<GRP, 0, 2, 0>(P); g1_ldb<GRP, 0, 2, 1>(P);
    g1_ldb<GRP, 0, 3, 0>(P); g1_ldb<GRP, 0, 3, 1>(P); g1_ldb<GRP, 0, 4, 0>(P); g1_ldb<GRP, 0, 4, 1>(P);
    if (GRP == 0) { g1_ldah<0>(P); g1_ldbh<0, 0>(P); g1_ldbh<0, 1>(P); }
    g1_lda<0, 0>(P); g1_lda<0, 1>(P);
    g1_ldb<GRP, 0, 5, 0>(P); g1_ldb<GRP, 0, 5, 1>(P); g1_ldb<GRP, 0, 6, 0>(P); g1_ldb<GRP, 0, 6, 1>(P); g1_ldb<GRP, 0, 7, 0>(P); g1_ldb<GRP, 0, 7, 1>(P);
    g1_lda<0, 2>(P); g1_lda<0, 3>(P);
    g1_issue<2>(L);
    if (GRP == 1) g1_issue<3>(L);
    STAMP_SEG(0);
#pragma unroll 1
    for (int it = 0; it < 8; ++it) {
        g1_stage<GRP, 0>(acc, acch, L, it == 7, P);
        if (GRP == 0) g1_tail<0>(L, it == 7);
        g1_stage<GRP, 1>(acc, acch, L, it == 7, P);
        g1_stage<GRP, 2>(acc, acch, L, it == 7, P);
        g1_stage<GRP, 3>(acc, acch, L, it == 7, P);
    }
    asm volatile("s_waitcnt vmcnt(0) lgkmcnt(0)\n\ts_nop 15" ::: "memory", FRAG_REGS);
    SBAR();
    STAMP_SEG(0);

    const int lane = fresh_lane(), g = lane >> 4, i16 = lane & 15;
    char* ho = ws + WS_HO;
    const int mb0 = 16 * rt + 4 * wm;
    const unsigned hl = (unsigned)((g & 1) * 1024 + (g >> 1) * 256 + i16 * 16);
    {
        const int wid = GRP * 4 + wm, r = lane >> 3, c = (lane & 7) ^ ((r & 2) | ((wid & 1) << 2));
        W.vw0 = (unsigned)((8 * wid + r) * 2048 + c * 16 + kh * 128);
        W.vw1 = W.vw0 ^ 128;
        W.vw2 = W.vw0 + 64 * 2048;
    }
#define G2_PRE() do { g2_w<0, 0>(W); g2_w<0, 1>(W); g2_w<1, 0>(W); g2_w<1, 1>(W); g2_w<2, 0>(W); g2_w<2, 1>(W); g2_w<3, 0>(W); g2_w<3, 1>(W); g2_w<4, 0>(W); g2_w<4, 1>(W); } while (0)
    if (GRP == 1) {
        SBAR(); BAR(); MEMFENCE(); SBAR();
        G2_PRE();
#pragma unroll
        for (int n = 0; n < 5; ++n) {
            const int cb = 7 * ct + 2 + n;
            char* dst = ho + (size_t)(32 + (cb >> 1)) * SLAB_B + (cb & 1) * 512 + (size_t)mb0 * 1024 + hl;
#pragma unroll
            for (int m = 0; m < 4; m += 2) glu_store(acc[m][n], acc[m][5 + n], acc[m + 1][n], acc[m + 1][5 + n], dst + m * 1024);
        }
        asm volatile("s_waitcnt vmcnt(10)" ::: "memory");
    } else {
        u32x4 Qf[4], Kf[4]; u32x2 Vh[4][2];
#pragma unroll
        for (int m = 0; m < 4; ++m) {
            Qf[m] = (u32x4){pk_bf16(acc[m][0][0], acc[m][0][1]), pk_bf16(acc[m][0][2], acc[m][0][3]), pk_bf16(acc[m][1][0], acc[m][1][1]), pk_bf16(acc[m][1][2], acc[m][1][3])};
            Kf[m] = (u32x4){pk_bf16(acc[m][2][0], acc[m][2][1]), pk_bf16(acc[m][2][2], acc[m][2][3]), pk_bf16(acc[m][3][0], acc[m][3][1]), pk_bf16(acc[m][3][2], acc[m][3][3])};
#pragma unroll
            for (int b = 0; b < 2; ++b) Vh[m][b] = (u32x2){pk_bf16(acc[m][4 + b][0], acc[m][4 + b][1]), pk_bf16(acc[m][4 + b][2], acc[m][4 + b][3])};
        }
        LAS char* xs = lds + G1_XOFF;
        if (wm < 3) {
            *(LAS u32x4*)(xs + (wm + 1) * 2048 + lane * 16) = Kf[3];
            *(LAS u32x4*)(xs + (wm + 1) * 2048 + 1024 + lane * 16) = (u32x4){Vh[3][0][0], Vh[3][0][1], Vh[3][1][0], Vh[3][1][1]};
        }
        {
            const unsigned h0 = pk_bf16(acch[0], acch[1]), h1 = pk_bf16(acch[2], acch[3]);
            if (wm < 2) {
                *(LAS u32x2*)(xs + lane * 16 + wm * 8) = (u32x2){h0, h1};
            } else {
                LAS char* t = xs + 1024 + ((i16 >> 2) * 16 + 4 * g) * 16 + (wm - 2) * 8 + (i16 & 3) * 2;
                *(LAS u16*)(t) = (u16)h0; *(LAS u16*)(t + 16) = (u16)(h0 >> 16); *(LAS u16*)(t + 32) = (u16)h1; *(LAS u16*)(t + 48) = (u16)(h1 >> 16);
            }
        }
        asm volatile("s_waitcnt lgkmcnt(0)" ::: "memory");
        SBAR(); BAR(); MEMFENCE(); SBAR();
        G2_PRE();
        const u32x4 Kp = *(LAS const u32x4*)(xs + wm * 2048 + lane * 16);
        const u32x4 Vp = *(LAS const u32x4*)(xs + wm * 2048 + 1024 + lane * 16);
        const int dc = i16 - 4 * g - (i16 & 1);
        unsigned modd = (i16 & 1) ? ~0u : 0u, mc0 = dc >= 0 ? ~0u : 0u, mc1 = dc >= 2 ? ~0u : 0u;
        asm volatile("" : "+v"(modd), "+v"(mc0), "+v"(mc1));
        const unsigned half = (modd & 0xffff0000u) | (~modd & 0x0000ffffu);
        const unsigned pp0 = half & ~mc0, pp1 = half & ~mc1, pc0 = half & mc0, pc1 = half & mc1;
        const float sc = 0.17677669529663687f * 1.4426950408889634f;
        const f32x4 z = {0.f, 0.f, 0.f, 0.f};
        float e0[4], e1[4], red[4];
#pragma unroll
        for (int m = 0; m < 4; ++m) {
            u32x4 s0 = __builtin_bit_cast(u32x4, MFMA16(as_frag(m ? Kf[m ? m - 1 : 0] : Kp), as_frag(Qf[m]), z));
            const u32x4 s1 = __builtin_bit_cast(u32x4, MFMA16(as_frag(Kf[m]), as_frag(Qf[m]), z));
            if (m == 0 && rt == 0 && wm == 0) { const unsigned c = __float_as_uint(-1e9f / 0.17677669529663687f); s0 = (u32x4){c, c, c, c}; }
            e0[m] = __uint_as_float(bsel(modd, bsel(mc0, s1[1], s0[1]), bsel(mc0, s1[0], s0[0])));
            e1[m] = __uint_as_float(bsel(modd, bsel(mc1, s1[3], s0[3]), bsel(mc1, s1[2], s0[2])));
            red[m] = fmaxf(e0[m], e1[m]);
        }
        {
            rows_spread(rows_reduce<true>(red), red);
        }
#pragma unroll
        for (int m = 0; m < 4; ++m) {
            e0[m] = __builtin_amdgcn_exp2f((e0[m] - red[m]) * sc); e1[m] = __builtin_amdgcn_exp2f((e1[m] - red[m]) * sc);
            red[m] = e0[m] + e1[m];
        }
        {
            float w = __builtin_amdgcn_rcpf(rows_reduce<false>(red));
            rows_spread(w, red);
        }
        char* od = ho + (size_t)ct * SLAB_B + (size_t)mb0 * 1024 + (((g & 1) << 1) | (g >> 1)) * 256 + i16 * 16;
#pragma unroll
        for (int m = 0; m < 4; ++m) {
            const float p0 = e0[m] * red[m], p1 = e1[m] * red[m];
            const unsigned w0 = pk_bf16(p0, p0), w1 = pk_bf16(p1, p1);
            const u32x2 Pp = {w0 & pp0, w1 & pp1}, Pc = {w0 & pc0, w1 & pc1};
            const u32x2 Vp0 = m ? Vh[m ? m - 1 : 0][0] : (u32x2){Vp[0], Vp[1]}, Vp1 = m ? Vh[m ? m - 1 : 0][1] : (u32x2){Vp[2], Vp[3]};
            const f32x4 o0 = MFMA16K16(Vh[m][0], Pc, MFMA16K16(Vp0, Pp, z));
            const f32x4 o1 = MFMA16K16(Vh[m][1], Pc, MFMA16K16(Vp1, Pp, z));
            unsigned x0 = pk_bf16(o0[0], o0[1]), x1 = pk_bf16(o0[2], o0[3]), y0 = pk_bf16(o1[0], o1[1]), y1 = pk_bf16(o1[2], o1[3]);
            auto r0 = __builtin_amdgcn_permlane16_swap(x0, y0, false, false);
            auto r1 = __builtin_amdgcn_permlane16_swap(x1, y1, false, false);
            st16(od + m * 1024, (u32x4){r0[0], r1[0], r0[1], r1[1]});
        }
#pragma unroll
        for (int n = 0; n < 2; ++n) {
            const int cb = 7 * ct + n;
            char* dst = ho + (size_t)(32 + (cb >> 1)) * SLAB_B + (cb & 1) * 512 + (size_t)mb0 * 1024 + hl;
#pragma unroll
            for (int m = 0; m < 4; m += 2) glu_store(acc[m][6 + n], acc[m][8 + n], acc[m + 1][6 + n], acc[m + 1][8 + n], dst + m * 1024);
            if (n == O_FLAG_AT) { asm volatile("s_waitcnt vmcnt(%0)" :: "i"(2 * O_FLAG_AT + 2) : "memory"); flag_set(ws + WS_FL + (rt * 4 + wm) * 512 + ct * 4); }
        }
        STAMP_SEG(1);
        if (wm == 0) flags_wait((u32x2){0u, 0u}, oflags, (unsigned)((lane >> 5) * 512 + kh * 64 + (lane & 7) * 8));
    }
#undef G2_PRE
    STAMP_SEG(2);
}

__global__ __launch_bounds__(512, 2) void k_main(const char* __restrict__ wqkv, const char* __restrict__ wg, const char* __restrict__ wu, const char* __restrict__ wo, const char* __restrict__ wd, char* __restrict__ ws,
                                                const u16* __restrict__ x, u16* __restrict__ out) {
    extern __shared__ __attribute__((aligned(16))) char smem[];
    TL_ENTRY();
    asm volatile("" : "+s"(wqkv), "+s"(wg), "+s"(wu), "+s"(wo), "+s"(wd), "+s"(ws), "+s"(x), "+s"(out));
    LAS char* lds = (LAS char*)smem;
    const int tid = threadIdx.x, lane = tid & 63;
    const int wid = __builtin_amdgcn_readfirstlane(tid >> 6);
    const int bid = blockIdx.x, xcd = bid & 7, idx = bid >> 3;
    const int ct = 4 * xcd + (idx & 3), rt = idx >> 2;
    const unsigned ldsb = (unsigned)(size_t)lds;
    G1Ld L;
    {
        const int c = wid * 64 + lane, rho = c / 14, cc = c - rho * 14;
        const int k = (rho & ~12) | ((rho & 4) << 1) | ((rho & 8) >> 1);
        const int c2 = cc < 12 ? cc : 11;
        const bool last = wid == 7;
        L.pa = ws + WS_XN + (size_t)(16 * rt + 2 * wid) * 1024;
        L.stepa = (unsigned)SLAB_B;
        L.va = lane * 16;
        L.la = ldsb + (1 + 2 * wid) * 1024;
        L.p2 = last ? ws + WS_XN + (size_t)(rt == 0 ? 0 : 16 * rt - 1) * 1024 : wqkv + ct * 64;
        L.step2 = last ? (unsigned)SLAB_B : 32u * 3072u * 2u;
        L.v2 = last ? (unsigned)((rt == 0 ? (lane & 48) : lane) * 16) : (unsigned)(k * 6144 + (c2 >> 2) * 2048 + (c2 & 3) * 16);
        L.l2 = last ? ldsb : ldsb + G1_BOFF + wid * 1024;
        L.s2 = last ? G1_AST : G1_BST;
        L.pg = wg + ct * 224;
        L.pu = wu + ct * 224;
        L.stepg = 32u * 3584u * 2u;
        L.vg = last ? 0u : (unsigned)(k * 7168 + cc * 16);
        L.lg = last ? ldsb + G1_BOFF + 192 : ldsb + G1_BOFF + G1_REG + wid * 1024;
        L.lu = last ? ldsb + G1_BOFF + 192 : ldsb + G1_BOFF + 2 * G1_REG + wid * 1024;
        L.ma = ~0ull; L.m2 = ~0ull; L.mg = last ? 1ull : ~0ull;
    }
    L.warm = wid < 4 ? wo + bid * 8192 + wid * 2048 : wd + bid * 28672 + (wid & 3) * 7168;
    L.lw = ldsb + G1_XOFF + (wid & 3) * 1024; L.ldum = ldsb + G1_BOFF + 192;
    L.pf = ws + WS_XB + (size_t)bid * 32768 + (wid & 3) * 8192;
    const int kh = bid & 1, quad = (bid >> 1) & 3, rt2 = 2 * rt + (quad >> 1), ct2 = 4 * (quad & 1) + (idx & 3);
    const int kg = wid >> 2, wm = wid & 3;
    const char* flq = ws + WS_FL + (rt * 4 + 2 * (quad >> 1)) * 512;
    G2St T;
    T.W.pw = wo + (size_t)(kh * 512) * 2048 + ct2 * 256;
    T.W.pwh = wd + (size_t)(kh ? 3584 - 64 * (G2_F - G2_H) : 0) * 2048 + ct2 * 256;
    T.W.pwmid = wd + (size_t)(64 * (G2_F - G2_H)) * 2048 + ct2 * 256;
    T.W.ldsw = ldsb + wid * 1024;
    STAMP_DECL STAMP_START();
    if (wid < 4) g1_body<0>(L, T.W, lds, ws, rt, ct, wm, kh, flq STAMP_PASS);
    else g1_body<1>(L, T.W, lds, ws, rt, ct, wm, kh, flq STAMP_PASS);

    const int lane2 = fresh_lane(), g = lane2 >> 4, i16 = lane2 & 15, q = i16 >> 2, p = lane2 & 3;
    {
        const char* a0 = ws + WS_HO + (size_t)kg * SLAB_B + (size_t)(8 * rt2 + 2 * wm) * 1024;
        T.pa = a0 + (size_t)(16 * kh) * SLAB_B;
        T.pah = a0 + (size_t)(kh ? 144 - 2 * (G2_F - G2_H) : 32) * SLAB_B;
        T.pamid = a0 + (size_t)(32 + 2 * (G2_F - G2_H)) * SLAB_B;
        T.va0 = lane2 * 16 + kg * 1024; T.va1 = T.va0 ^ 1024;
        T.myflag = ws + WS_FL + (rt * 4 + wm) * 512 + 256 + kg * 128 + ct * 4;
        T.poller = wid == 0;
        T.hflags = T.poller ? flq + 256 : ws + WS_XB + (size_t)bid * 32768;
        T.hfo = (unsigned)((lane2 >> 5) * 512 + (lane2 & 31) * 8);
    }
#pragma unroll
    for (int n = 0; n < 4; ++n) {
        T.pB[0][n] = lds + kg * 4096 + (8 * g + q) * 128 + (((2 * n + (p >> 1)) ^ ((q & 2) | ((g & 1) << 2))) * 16) + (p & 1) * 8;
        T.pB[1][n] = T.pB[0][n] + 3 * G2_ST;
        asm volatile("" : "+v"(T.pB[0][n]), "+v"(T.pB[1][n]));
    }
    MEMFENCE(); SBAR(); BAR(); MEMFENCE(); SBAR();
    g2_a<0, 0>(T); g2_a<0, 1>(T); g2_a<1, 0>(T); g2_a<1, 1>(T); g2_a<2, 0>(T); g2_a<2, 1>(T); g2_a<3, 0>(T); g2_a<3, 1>(T);
    SBAR();
#pragma unroll
    for (int m = 0; m < 2; ++m)
#pragma unroll
        for (int n = 0; n < 8; ++n) T.acc[m][n] = (f32x4){0.f, 0.f, 0.f, 0.f};
#pragma unroll
    for (int n = 0; n < 8; ++n) T.B[n] = g2_ldb<0>(T, n);
    SBAR();
    STAMP_SEG(3);
    g2_run<0, 6>(T);
    STAMP_SEG(4);
#pragma unroll 1
    for (int it = 1; it < G2_T0 / 6; ++it) g2_run<G2_T0 - 6, G2_T0>(T);
    const int orow = 128 * rt2 + 32 * wm + 16 * kg + i16, ocol = 128 * ct2 + 64 * kh + (g & 1) * 16 + (g >> 1) * 8;
    T.xw = lds + G2_XOFF + wid * 4096 + lane2 * 16;
    T.xr = lds + G2_XOFF + (wid ^ 4) * 4096 + lane2 * 16;
    T.snd = ws + WS_XB + (size_t)bid * 32768 + wid * 4096 + lane2 * 16;
    T.rcv = ws + WS_XB + (size_t)(bid ^ 1) * 32768 + wid * 4096 + lane2 * 16;
    T.xp = x + (size_t)orow * D_ + ocol;
    g2_run<G2_T0, 36>(T);
    STAMP_SEG(5);

#pragma unroll
    for (int j = 0; j < 4; ++j) *(LAS f32x4*)(T.xw + j * 1024) = T.acc[1][j];
    WAIT_LGKM0();
    MEMFENCE(); SBAR(); BAR(); MEMFENCE(); SBAR();
#pragma unroll
    for (int j = 0; j < 4; ++j) T.acc[0][j] += *(LAS const f32x4*)(T.xr + j * 1024);
    asm volatile("s_waitcnt vmcnt(0)" : "+v"(T.xres[0]), "+v"(T.xres[1]), "+v"(T.rc[0]), "+v"(T.rc[1]), "+v"(T.rc[2]), "+v"(T.rc[3]) :: "memory");
    for (unsigned spin = 0;; ++spin) {
        unsigned mx = 0;
#pragma unroll
        for (int j = 0; j < 4; ++j) { const unsigned a = T.rc[j][0] > T.rc[j][1] ? T.rc[j][0] : T.rc[j][1], b = T.rc[j][2] > T.rc[j][3] ? T.rc[j][2] : T.rc[j][3]; mx = mx > a ? mx : a; mx = mx > b ? mx : b; }
        if (!__any(mx == 0xffffffffu) || spin > SPIN_CAP) break;
        __builtin_amdgcn_s_sleep(4);
        asm volatile("global_load_dwordx4 %0, %4, off sc1\n\tglobal_load_dwordx4 %1, %4, off offset:1024 sc1\n\tglobal_load_dwordx4 %2, %4, off offset:2048 sc1\n\tglobal_load_dwordx4 %3, %4, off offset:3072 sc1\n\ts_waitcnt vmcnt(0)"
                     : "=&v"(T.rc[0]), "=&v"(T.rc[1]), "=&v"(T.rc[2]), "=&v"(T.rc[3]) : "v"(T.rcv) : "memory");
    }
    u16* orp = out + (size_t)orow * D_ + ocol;
#pragma unroll
    for (int j = 0; j < 4; j += 2) {
        const f32x4 s0 = T.acc[0][j] + __builtin_bit_cast(f32x4, T.rc[j]), s1 = T.acc[0][j + 1] + __builtin_bit_cast(f32x4, T.rc[j + 1]);
        float v[8];
#pragma unroll
        for (int e = 0; e < 4; ++e) { auto r = __builtin_amdgcn_permlane16_swap(__float_as_uint(s0[e]), __float_as_uint(s1[e]), false, false); v[e] = __uint_as_float(r[0]); v[4 + e] = __uint_as_float(r[1]); }
        const u32x4 xv = T.xres[j >> 1];
        u32x4 o;
#pragma unroll
        for (int e = 0; e < 4; ++e) o[e] = pk_bf16(v[2 * e] + bf_lo(xv[e]), v[2 * e + 1] + bf_hi(xv[e]));
        *(GAS u32x4*)(size_t)(orp + 16 * j) = o;
    }
    STAMP_SEG(6);
    STAMP_OUT(ws, 0, wid, lane2);
    STAMP_OUT(ws, 1, wid, lane2);
    TL_EXIT(ws, 1);
    TL_EXIT(ws, 2);
}

extern "C" void kernel_launch(void* const* d_in, const int* in_sizes, int n_in,
                              void* d_out, int out_size, void* d_ws, size_t ws_size,
                              hipStream_t stream) {
    const u16* x = (const u16*)d_in[0];
    const char* wqkv = (const char*)d_in[1];
    const char* wo = (const char*)d_in[2];
    const char* wg = (const char*)d_in[4];
    const char* wu = (const char*)d_in[5];
    const char* wd = (const char*)d_in[6];
    char* ws = (char*)d_ws;
    (void)hipFuncSetAttribute((const void*)k_main, hipFuncAttributeMaxDynamicSharedMemorySize, G1_LDS);
    k_ln<<<256, 512, 0, stream>>>(x, ws);
    k_main<<<256, 512, G1_LDS, stream>>>(wqkv, wg, wu, wo, wd, ws, x, (u16*)d_out);
}
```

```cpp
#include <hip/hip_runtime.h>
#include <hip/hip_bf16.h>
#include <stdint.h>

typedef __bf16 bf16x8 __attribute__((ext_vector_type(8)));
typedef __bf16 bf16x4 __attribute__((ext_vector_type(4)));
typedef __bf16 bf16x2 __attribute__((ext_vector_type(2)));
typedef float f32x4 __attribute__((ext_vector_type(4)));
typedef float f32x2 __attribute__((ext_vector_type(2)));
typedef unsigned u32x4 __attribute__((ext_vector_type(4)));
typedef unsigned u32x2 __attribute__((ext_vector_type(2)));
typedef unsigned short u16;
#define LAS __attribute__((address_space(3)))
#define GAS __attribute__((address_space(1)))
#define DEVI __device__ __forceinline__

constexpr int S_ = 2048, D_ = 1024, F_ = 3584;
constexpr int MB_ = S_ / 16;
constexpr size_t SLAB_B = (size_t)MB_ * 1024;
constexpr size_t WS_XN = 0;
constexpr size_t WS_HO = 32 * SLAB_B;
constexpr size_t WS_XB = WS_HO + 144 * SLAB_B;
constexpr size_t WS_FL = WS_XB + 256 * 32768;

DEVI unsigned pk_bf16(float lo, float hi) {
    bf16x2 v = {(__bf16)lo, (__bf16)hi};
    return __builtin_bit_cast(unsigned, v);
}
DEVI float bf_lo(unsigned u) { return __uint_as_float(u << 16); }
DEVI float bf_hi(unsigned u) { return __uint_as_float(u & 0xffff0000u); }
DEVI float rbf(float f) { return (float)(__bf16)f; }
DEVI bf16x8 as_frag(u32x4 v) { return __builtin_bit_cast(bf16x8, v); }
#define MFMA16(a, b, c) __builtin_amdgcn_mfma_f32_16x16x32_bf16((a), (b), (c), 0, 0, 0)
#define SBAR() __builtin_amdgcn_sched_barrier(0)
#define MFMA_ACC(c, a, b) asm volatile("v_mfma_f32_16x16x32_bf16 %0, %1, %2, %0" : "+v"(c) : "v"(a), "v"(b))
#define MFMA_NEW(c, a, b) asm volatile("v_mfma_f32_16x16x32_bf16 %0, %1, %2, 0" : "=&v"(c) : "v"(a), "v"(b))
#define MFMA_DRAIN() asm volatile("s_nop 15" ::: "memory")
#define BAR() __builtin_amdgcn_s_barrier()

#define TL_ENTRY()
#define TL_EXIT(ws, kern)
#define STAMP_DECL
#define STAMP_ARGS
#define STAMP_PASS
#define STAMP_ARGS2
#define STAMP_PASS2
#define STAMP_SEG(i)
#define STAMP_START()
#define STAMP_SEGI(i)
#define STAMP_OUT(ws, kern, wid, lane)

DEVI int fresh_lane() { int l; asm volatile("v_mbcnt_lo_u32_b32 %0, -1, 0\n\tv_mbcnt_hi_u32_b32 %0, -1, %0" : "=v"(l)); return l; }

DEVI bf16x8 tr_frag(LAS const char* p, int off0, int off1) {
    bf16x4 lo = __builtin_amdgcn_ds_read_tr16_b64_v4bf16((LAS bf16x4*)(p + off0));
    bf16x4 hi = __builtin_amdgcn_ds_read_tr16_b64_v4bf16((LAS bf16x4*)(p + off1));
    return __builtin_shufflevector(lo, hi, 0, 1, 2, 3, 4, 5, 6, 7);
}

DEVI void glds16(const char* sbase, unsigned voff, unsigned ldsdst) {
    unsigned keep;
    asm volatile("s_mov_b32 %0, m0\n\ts_mov_b32 m0, %3\n\ts_nop 0\n\tglobal_load_lds_dwordx4 %1, %2\n\ts_mov_b32 m0, %0"
                 : "=&s"(keep) : "v"(voff), "s"(sbase), "s"(ldsdst) : "memory");
}

__global__ __launch_bounds__(512) void k_ln(const u16* __restrict__ x, char* __restrict__ ws) {
    __shared__ float red[2][8][8];
    TL_ENTRY();
    const int tid = threadIdx.x, w = tid >> 6, l = tid & 63, r = l & 7, c8 = l >> 3;
    const int row = blockIdx.x * 8 + r;
    u32x4 v[2];
#pragma unroll
    for (int i = 0; i < 2; ++i) v[i] = *(const u32x4*)(x + (size_t)row * D_ + (16 * w + 8 * i + c8) * 8);
    float f[16];
#pragma unroll
    for (int i = 0; i < 2; ++i)
#pragma unroll
        for (int j = 0; j < 4; ++j) { f[i * 8 + 2 * j] = bf_lo(v[i][j]); f[i * 8 + 2 * j + 1] = bf_hi(v[i][j]); }
    float s = 0.f;
#pragma unroll
    for (int j = 0; j < 16; ++j) s += f[j];
    asm volatile("" : "+v"(s));
    {
        char* d = ws + WS_HO + (size_t)(blockIdx.x >> 7) * 64 * SLAB_B + (size_t)(blockIdx.x & 127) * 16384 + tid * 16;
        const u32x4 ones = {0xffffffffu, 0xffffffffu, 0xffffffffu, 0xffffffffu};
        asm volatile("global_store_dwordx4 %0, %2, off sc1\n\tglobal_store_dwordx4 %1, %2, off sc1\n\ts_nop 1" :: "v"(d), "v"(d + 8192), "v"(ones) : "memory");
    }
    s += __shfl_xor(s, 8); s += __shfl_xor(s, 16); s += __shfl_xor(s, 32);
    if (c8 == 0) red[0][w][r] = s;
    __syncthreads();
    float tot = 0.f;
#pragma unroll
    for (int k = 0; k < 8; ++k) tot += red[0][k][r];
    const float mean = tot * (1.0f / D_);
    float q = 0.f;
#pragma unroll
    for (int j = 0; j < 16; ++j) { const float d = f[j] - mean; q += d * d; }
    q += __shfl_xor(q, 8); q += __shfl_xor(q, 16); q += __shfl_xor(q, 32);
    if (c8 == 0) red[1][w][r] = q;
    __syncthreads();
    float qt = 0.f;
#pragma unroll
    for (int k = 0; k < 8; ++k) qt += red[1][k][r];
    const float mb = rbf(mean);
    const float vb = rbf(qt * (1.0f / D_));
    const float den = rbf(sqrtf(rbf(vb + 1e-5f)));
    const float rc = 1.0f / den;
    const int mblk = row >> 4, rr = row & 15;
#pragma unroll
    for (int i = 0; i < 2; ++i) {
        u32x4 o;
#pragma unroll
        for (int j = 0; j < 4; ++j) {
            float y[2];
#pragma unroll
            for (int e = 0; e < 2; ++e) {
                const float d = rbf(f[i * 8 + 2 * j + e] - mb);
                float qq = d * rc;
                qq = fmaf(fmaf(-qq, den, d), rc, qq);
                y[e] = qq;
            }
            o[j] = pk_bf16(y[0], y[1]);
        }
        const int ch = 16 * w + 8 * i + c8, slab = ch >> 2, g = ch & 3;
        asm volatile("global_store_dwordx4 %0, %1, off sc1\n\ts_nop 1" :: "v"(ws + WS_XN + slab * SLAB_B + (size_t)mblk * 1024 + (g * 16 + rr) * 16), "v"(o) : "memory");
    }
    if (tid < 16) ((unsigned*)(ws + WS_FL))[blockIdx.x * 16 + tid] = 0u;
    TL_EXIT(ws, 0);
}

constexpr int G1_AST = 17 * 1024;
constexpr int G1_BST = 21 * 1024;
constexpr int G1_REG = 7168;
constexpr int G1_BOFF = 4 * G1_AST;
constexpr int G1_XOFF = G1_BOFF + 4 * G1_BST;
constexpr int G1_LDS = G1_XOFF + 8192;

typedef unsigned long long u64;
#define FRAG_REGS "v192", "v193", "v194", "v195", "v196", "v197", "v198", "v199", "v200", "v201", "v202", "v203", "v204", "v205", "v206", "v207", "v208", "v209", "v210", "v211", "v212", "v213", "v214", "v215", "v216", "v217", "v218", "v219", "v220", "v221", "v222", "v223", "v224", "v225", "v226", "v227", "v228", "v229", "v230", "v231", "v232", "v233", "v234", "v235", "v236", "v237", "v238", "v239", "v240", "v241", "v242", "v243", "v244", "v245", "v246", "v247", "v248", "v249", "v250", "v251", "v252", "v253", "v254", "v255"
constexpr int FA = 192, FB = 208, FAH = 248, FBH = 252;
struct G1Ld {
    const char *pa, *p2, *pg, *pu;
    unsigned stepa, step2, stepg;
    unsigned va, v2, vg;
    unsigned la, l2, s2, lg, lu;
    const char* warm; unsigned lw, ldum;
    char* pf;
    u64 ma, m2, mg;
};

#define WAIT_LGKM0() __builtin_amdgcn_s_waitcnt(0xC07F)
#define MEMFENCE() asm volatile("" ::: "memory")
#define OPAQUE_S(x) asm volatile("" : "+s"(x))

template <int LOFF, bool TWO>
DEVI void g1_piece(const char* base, unsigned voff, unsigned lds, u64 msk) {
    asm volatile("s_add_u32 m0, %2, %4\n\ts_mov_b64 exec, %3\n\tglobal_load_lds_dwordx4 %0, %1\n\t"
                 ".if %5\n\tglobal_load_lds_dwordx4 %0, %1 offset:1024\n\t.endif\n\ts_mov_b64 exec, -1"
                 :: "v"(voff), "s"(base), "s"(lds), "s"(msk), "i"(LOFF), "i"(TWO ? 1 : 0) : "memory", "scc");
}
template <int SLOT> DEVI void g1_pa(G1Ld& L) { g1_piece<SLOT * G1_AST, true>(L.pa, L.va, L.la, L.ma); L.pa += L.stepa; OPAQUE_S(L.pa); }
template <int SLOT> DEVI void g1_p2(G1Ld& L) { g1_piece<0, false>(L.p2, L.v2, L.l2 + SLOT * L.s2, L.m2); L.p2 += L.step2; OPAQUE_S(L.p2); }
template <int SLOT> DEVI void g1_pg(G1Ld& L) { g1_piece<SLOT * G1_BST, false>(L.pg, L.vg, L.lg, L.mg); L.pg += L.stepg; OPAQUE_S(L.pg); }
template <int SLOT> DEVI void g1_pu(G1Ld& L) { g1_piece<SLOT * G1_BST, false>(L.pu, L.vg, L.lu, L.mg); L.pu += L.stepg; OPAQUE_S(L.pu); }
template <int SLOT> DEVI void g1_issue(G1Ld& L) { g1_pa<SLOT>(L); g1_p2<SLOT>(L); g1_pg<SLOT>(L); g1_pu<SLOT>(L); }
template <int GRP> DEVI void g1_tail(G1Ld& L, bool tail) {
    L.p2 = tail ? L.p2 - L.step2 : L.p2; L.pg = tail ? L.pg - L.stepg : L.pg; L.pu = tail ? L.pu - L.stepg : L.pu;
    L.step2 = tail ? 0u : L.step2; L.stepg = tail ? 0u : L.stepg;
    L.m2 = tail ? 1ull : L.m2; L.mg = tail ? 1ull : L.mg;
    if (GRP == 0) L.pa = tail ? L.warm : L.pa;
    else { L.pa = tail ? L.pa - L.stepa : L.pa; L.ma = tail ? 1ull : L.ma; L.warm = tail ? L.warm - 1024 : L.warm; L.lw = tail ? L.ldum : L.lw; }
    L.stepa = tail ? 0u : L.stepa;
}

struct G1Ptr { unsigned pA, pH, pB01, pB23, pBh01, pBh23; };

template <int REG, int OFF> DEVI void ds_tr(unsigned base) { asm volatile("ds_read_b64_tr_b16 v[%c1:%c2], %0 offset:%c3" :: "v"(base), "i"(REG), "i"(REG + 1), "i"(OFF) : "memory"); }
template <int REG, int OFF> DEVI void ds_128(unsigned base) { asm volatile("ds_read_b128 v[%c1:%c2], %0 offset:%c3" :: "v"(base), "i"(REG), "i"(REG + 3), "i"(OFF) : "memory"); }
template <int RA_, int RB_> DEVI void mfma_fr(f32x4& c) { asm volatile("v_mfma_f32_16x16x32_bf16 %0, v[%c1:%c2], v[%c3:%c4], %0" : "+v"(c) : "i"(RA_), "i"(RA_ + 3), "i"(RB_), "i"(RB_ + 3)); }
template <int GRP, int SL, int n, int h> DEVI void g1_ldb(const G1Ptr& P) {
    constexpr int off = (SL & 1) * G1_BST + (GRP == 0 ? (n < 6 ? n * 32 : (n < 8 ? G1_REG + (n - 1) * 32 : 2 * G1_REG + (n - 3) * 32))
                                                      : (n < 5 ? G1_REG + n * 32 : 2 * G1_REG + (n - 5) * 32));
    ds_tr<FB + 4 * n + 2 * h, off + h * 8 * 224>((SL < 2) ? P.pB01 : P.pB23);
}
template <int SL, int m> DEVI void g1_lda(const G1Ptr& P) { ds_128<FA + 4 * m, SL * G1_AST + (1 + m) * 1024>(P.pA); }
template <int SL> DEVI void g1_ldah(const G1Ptr& P) { ds_128<FAH, SL * G1_AST>(P.pH); }
template <int SL, int h> DEVI void g1_ldbh(const G1Ptr& P) { ds_tr<FBH + 2 * h, (SL & 1) * G1_BST + h * 8 * 224>((SL < 2) ? P.pBh01 : P.pBh23); }

template <int GRP, int ST>
DEVI void g1_stage(f32x4 (&acc)[4][10], f32x4& acch, G1Ld& L, bool tail, const G1Ptr& P) {
    constexpr int NX = (ST + 1) & 3, IS = (ST + 3) & 3;
#define MM(m, n) do { if (GRP == 0 && ((n) == 4 || (n) == 5)) mfma_fr<FA + 4 * (m), FB + 4 * (n)>(acc[m][n]); else mfma_fr<FB + 4 * (n), FA + 4 * (m)>(acc[m][n]); } while (0)
#define RB(n, h) g1_ldb<GRP, NX, n, h>(P)
#define RC(n, h) g1_ldb<GRP, ST, n, h>(P)
#define RA(m) g1_lda<NX, m>(P)
#define G0(x) do { if (GRP == 0) { x; } } while (0)
    asm volatile("s_waitcnt lgkmcnt(9)" ::: "memory");
    MM(0, 0); RC(8, 0);
    asm volatile("s_waitcnt lgkmcnt(9)" ::: "memory");
    MM(1, 0); RC(8, 1);
    MM(1, 1); RC(9, 0); MM(0, 1); RC(9, 1);
    MM(0, 2); G0(g1_pa<IS>(L)); MM(1, 2);
    MM(1, 3); G0(g1_p2<IS>(L)); MM(0, 3); G0(g1_pg<IS>(L));
    MM(0, 4); G0(g1_pu<IS>(L)); MM(1, 4);
    G0((mfma_fr<FBH, FAH>(acch)));
    asm volatile("s_waitcnt vmcnt(10) lgkmcnt(0)\n\ts_barrier" ::: "memory", FRAG_REGS);
    if (GRP == 1) {
        if (ST == 0) g1_tail<1>(L, tail);
        g1_issue<ST>(L);
        if (ST == 1) { g1_piece<0, false>(L.warm, L.va, L.lw, L.ma); L.warm += 1024; OPAQUE_S(L.warm); }
        if (ST == 2) {
            const u32x4 ones = {0xffffffffu, 0xffffffffu, 0xffffffffu, 0xffffffffu};
            asm volatile("global_store_dwordx4 %0, %1, %2 sc1\n\ts_nop 1" :: "v"(L.va), "v"(ones), "s"(L.pf) : "memory", FRAG_REGS);
            L.pf += 1024; OPAQUE_S(L.pf);
        }
    }
    MM(2, 0); MM(3, 0); RB(0, 0);
    MM(3, 1); RB(0, 1); MM(2, 1); RB(1, 0);
    MM(2, 2); RB(1, 1); MM(3, 2); RB(2, 0);
    MM(3, 3); RB(2, 1); MM(2, 3); RB(3, 0);
    MM(2, 4); RB(3, 1); MM(3, 4); RB(4, 0);
    MM(0, 5); RB(4, 1); MM(1, 5); G0(g1_ldah<NX>(P));
    MM(1, 6); G0((g1_ldbh<NX, 0>(P))); MM(0, 6); G0((g1_ldbh<NX, 1>(P)));
    MM(0, 7); MM(1, 7);
    MM(1, 8); MM(0, 8);
    MM(0, 9); RA(0); MM(1, 9); RA(1);
    MM(2, 5); MM(3, 5); RB(5, 0);
    MM(3, 6); RB(5, 1); MM(2, 6); RB(6, 0);
    MM(2, 7); RB(6, 1); MM(3, 7); RB(7, 0);
    MM(3, 8); RB(7, 1); MM(2, 8);
    MM(2, 9); RA(2); MM(3, 9); RA(3);
#undef MM
#undef RB
#undef RC
#undef RA
#undef G0
}

DEVI void st16(char* dst, u32x4 v) {
    asm volatile("global_store_dwordx4 %0, %1, off sc1\n\ts_nop 1" :: "v"(dst), "v"(v) : "memory");
}

DEVI unsigned glu2(float g0, float g1, float u0, float u1) {
    const f32x2 t = (f32x2){g0, g1} * -1.4426950408889634f;
    const f32x2 d = (f32x2){__builtin_amdgcn_exp2f(t[0]), __builtin_amdgcn_exp2f(t[1])} + 1.0f;
    const f32x2 h = (f32x2){u0, u1} * (f32x2){__builtin_amdgcn_rcpf(d[0]), __builtin_amdgcn_rcpf(d[1])};
    return pk_bf16(h[0], h[1]);
}

DEVI void glu_store(const f32x4& g0, const f32x4& u0, const f32x4& g1, const f32x4& u1, char* dst) {
    unsigned a0 = glu2(g0[0], g0[1], u0[0], u0[1]), a1 = glu2(g0[2], g0[3], u0[2], u0[3]);
    unsigned b0 = glu2(g1[0], g1[1], u1[0], u1[1]), b1 = glu2(g1[2], g1[3], u1[2], u1[3]);
    auto r0 = __builtin_amdgcn_permlane16_swap(a0, b0, false, false);
    auto r1 = __builtin_amdgcn_permlane16_swap(a1, b1, false, false);
    st16(dst, (u32x4){r0[0], r1[0], r0[1], r1[1]});
    SBAR();
}

#define POLL_SLEEP 8
#define G2_RCV 34
#define G2_PLACE 1
#define G2_NS 8
constexpr int G2_ST = 16384;
constexpr int G2_WL = G2_NS - 1;
constexpr int G2_XOFF = G2_NS <= 8 ? G2_NS * G2_ST : 0;
constexpr int G2_F = 32, G2_XST = 33;
constexpr int G2_FY = 8, G2_FZ = 12;
constexpr int G2_PA = 1, G2_PY = 9, G2_PZ = 15;
constexpr unsigned SPIN_CAP = 1u << 17;
static_assert(G2_RCV > G2_F && G2_RCV <= 35 && G2_NS >= 6 && G2_NS <= 9, "");
constexpr int g2_grp(int t) { return t < G2_F ? t : G2_F + 2 * (t - G2_F); }
constexpr int g2_afirst(int s) { return s < 2 ? 2 + 2 * s : s + 4 <= 35 ? g2_grp(s + 4) : 40; }
constexpr int g2_alast(int s) { return g2_afirst(s + 1); }
constexpr int g2_tops(int s) { return s == 0 || s == G2_FY || s == G2_FZ ? 1 : 0; }
constexpr int g2_wops(int s) { return s + G2_WL <= 35 ? 2 : 0; }
constexpr int g2_xops(int s) { return (s < 8 && (s & 1) ? 1 : 0) + (s == G2_PA || s == G2_PY || s == G2_PZ ? 1 : 0) + (s == G2_F ? 4 : 0) + (s == G2_XST ? 2 : 0) + (s == G2_RCV ? 4 : 0); }
constexpr int g2_wait(int t) {
    const int need = g2_grp(t + 1) - 1;
    int n = 4, last = need < 2 ? 2 * need + 2 : 0;
    for (int s = 0; s < t; ++s) {
        n += g2_tops(s) + g2_wops(s);
        if (s + G2_WL == t + 1 && n > last) last = n;
        for (int g = g2_afirst(s); g < g2_alast(s); ++g) { n += 2; if (g == need && n > last) last = n; }
        n += g2_xops(s);
        if ((t == 4 && s == G2_PA) || (t == 12 && s == G2_PY) || (t == 20 && s == G2_PZ) || (t == G2_FY && s == 3) || (t == G2_FZ && s == 7)) last = n > last ? n : last;
    }
    return n - last;
}

struct G2W {
    const char *pwo, *pwh, *pwd;
    unsigned vw0, vw1, vw2, ldsw;
};
struct G2St {
    G2W W;
    const char *pa, *pad;
    unsigned va0, va1;
    LAS const char* pB[(G2_NS + 3) / 4][4];
    LAS char* xw; LAS const char* xr;
    char* snd; const char* rcv; const u16* xp;
    f32x4 dg[2][4], du[2][4]; char* dd[2]; unsigned hl, hq[4];
    char *flagA, *flagZ; const char* flags; unsigned foa, foz; u32x2 hf; bool poller;
    u32x4 A[40][2];
    bf16x4 B[8][2];
    f32x4 acc[2][8];
    u32x4 xres[2], rc[4];
};
template <int LOFF> DEVI void g2_glds(const char* base, unsigned voff, unsigned lds) {
    asm volatile("s_add_u32 m0, %2, %3\n\ts_nop 0\n\tglobal_load_lds_dwordx4 %0, %1" :: "v"(voff), "s"(base), "s"(lds), "i"(LOFF) : "memory", "scc");
}
template <int ST, int I> DEVI void g2_w(const G2W& W) {
    constexpr int SLOT = ST % G2_NS;
    const char* src = ST < 8 ? W.pwo + ST * 64 * 2048 : ST < G2_F ? W.pwh + (((ST - 8) & 7) * 224 + ((ST - 8) >> 3) * 32) * 2048 : W.pwd + (ST - G2_F) * 896 * 2048;
    g2_glds<SLOT * G2_ST + I * 8192>(src, I == 0 ? W.vw0 : ST < G2_F ? W.vw1 : W.vw2, W.ldsw);
}
template <int G> DEVI const char* g2_asrc(const G2St& T) {
    return G < G2_F ? T.pa + (size_t)G * 2 * SLAB_B : T.pad + (size_t)(G - G2_F) * 2 * SLAB_B; }
template <int G, int I> DEVI void g2_a(G2St& T) {
    asm volatile("global_load_dwordx4 %0, %1, %2 sc1" : "=v"(T.A[G][I]) : "v"(I ? T.va1 : T.va0), "s"(g2_asrc<G>(T)) : "memory");
}
template <int ST> DEVI bf16x4 g2_ldb(G2St& T, int n, int h) {
    constexpr int SLOT = ST % G2_NS;
    return __builtin_amdgcn_ds_read_tr16_b64_v4bf16((LAS bf16x4*)(T.pB[SLOT / 4][n & 3] + (SLOT % 4) * G2_ST + (n >> 2) * 8192 + h * 512));
}
DEVI bf16x8 cat(bf16x4 lo, bf16x4 hi) { return __builtin_shufflevector(lo, hi, 0, 1, 2, 3, 4, 5, 6, 7); }
template <int OFF> DEVI void flag_set(char* p) { const unsigned one = 1u, zero = 0u; asm volatile("s_mov_b64 exec, 1\n\tglobal_store_dword %0, %1, %2 offset:%3 sc1\n\ts_mov_b64 exec, -1" :: "v"(zero), "v"(one), "s"(p), "i"(OFF) : "memory"); }
DEVI void flags_wait(u32x2 f, const char* base, unsigned voff) {
#pragma unroll 1
    for (unsigned spin = 0; __any(f[0] == 0u || f[1] == 0u) && spin < SPIN_CAP; ++spin) {
        if (spin) __builtin_amdgcn_s_sleep(POLL_SLEEP);
        asm volatile("global_load_dwordx2 %0, %1, %2 sc1\n\ts_waitcnt vmcnt(0)" : "=&v"(f) : "v"(voff), "s"(base) : "memory");
    }
}
DEVI unsigned umax4(u32x4 v) { const unsigned a = v[0] > v[1] ? v[0] : v[1], b = v[2] > v[3] ? v[2] : v[3]; return a > b ? a : b; }
DEVI u32x4 never_ones(u32x4 w) {
#pragma unroll
    for (int e = 0; e < 4; ++e) w[e] = w[e] < 0xfffffffeu ? w[e] : 0xfffffffeu;
    return w;
}

template <int KG, int t>
DEVI void g2_stage(G2St& T) {
    constexpr bool TB = t >= G2_F, ISW = g2_wops(t) != 0, RL = t < 35, CHK = t == 4 || t == 12 || t == 20;
    constexpr int G0 = g2_grp(t), G1 = TB ? G0 + 1 : G0, NA = g2_alast(t) - g2_afirst(t), GN = NA ? g2_afirst(t) : 0, GN1 = NA == 2 ? GN + 1 : GN;
    u32x4 (&Aa)[2] = T.A[G0]; u32x4 (&Ab)[2] = T.A[G1];
    if (TB) asm volatile("s_waitcnt vmcnt(%4)" : "+v"(Aa[0]), "+v"(Aa[1]), "+v"(Ab[0]), "+v"(Ab[1]) : "i"(g2_wait(t)) : "memory");
    else if (CHK) asm volatile("s_waitcnt vmcnt(%3)" : "+v"(Aa[0]), "+v"(Aa[1]), "+v"(T.hf) : "i"(g2_wait(t)) : "memory");
    else asm volatile("s_waitcnt vmcnt(%2)" : "+v"(Aa[0]), "+v"(Aa[1]) : "i"(g2_wait(t)) : "memory");
    if (t == 0) flag_set<0>(T.flagA);
    if (t == G2_FY) flag_set<128>(T.flagA);
    if (t == G2_FZ) flag_set<0>(T.flagZ);
    if (t < 8) {
#pragma unroll 1
        for (unsigned spin = 0; spin < SPIN_CAP; ++spin) {
            const unsigned a = umax4(Aa[0]), b = umax4(Aa[1]);
            if (!__any((a > b ? a : b) == 0xffffffffu)) break;
            __builtin_amdgcn_s_sleep(POLL_SLEEP);
            asm volatile("global_load_dwordx4 %0, %2, %4 sc1\n\tglobal_load_dwordx4 %1, %3, %4 sc1\n\ts_waitcnt vmcnt(0)" : "=&v"(Aa[0]), "=&v"(Aa[1]) : "v"(T.va0), "v"(T.va1), "s"(g2_asrc<G0>(T)) : "memory");
        }
    }
    if (KG == 0 && CHK && T.poller) flags_wait(T.hf, t == 12 ? T.flags + 128 : T.flags, t == 20 ? T.foz : T.foa);
    if (t == G2_F) WAIT_LGKM0();
    { MEMFENCE(); SBAR(); BAR(); MEMFENCE(); SBAR(); }
    f32x4 ho[4];
    if (t == G2_F) {
#pragma unroll
        for (int j = 0; j < 4; ++j) ho[j] = *(LAS const f32x4*)(T.xr + j * 1024);
        SBAR();
    }
#define AB2DS true
#define RL2(n, h) do { if (AB2DS && RL && (n) >= 0) { T.B[(n) >= 0 ? (n) : 0][h] = g2_ldb<t + 1>(T, (n) >= 0 ? (n) : 0, h); SBAR(); } } while (0)
#define P2(n) do { constexpr int c_ = TB ? ((n) & 3) : (n); u32x4 (&A_)[2] = (n) < 4 ? Aa : Ab; const bf16x8 b_ = cat(T.B[n][0], T.B[n][1]); \
                   if (t == 0) MFMA_NEW(T.acc[(n) & 1][c_], b_, A_[(n) & 1]); else MFMA_ACC(T.acc[(n) & 1][c_], b_, A_[(n) & 1]); SBAR(); RL2((n) - 1, 1); D((n) * 2); \
                   if (t == 0) MFMA_NEW(T.acc[~(n) & 1][c_], b_, A_[~(n) & 1]); else MFMA_ACC(T.acc[~(n) & 1][c_], b_, A_[~(n) & 1]); SBAR(); RL2(n, 0); D((n) * 2 + 1); } while (0)
#define VW(i) do { if (ISW) { g2_w<ISW ? t + G2_WL : 0, i>(T.W); SBAR(); } } while (0)
#define VA(i) do { if (NA == 2) { g2_a<(i) < 2 ? GN : GN1, (i) & 1>(T); SBAR(); } else if (NA && ((i) & 1)) { g2_a<GN, (i) / 2>(T); SBAR(); } } while (0)
    f32x4& dg = T.dg[(t >> 2) & 1][t & 3]; f32x4& du = T.du[(t >> 2) & 1][t & 3];
#define D(i) do { if (t < 8 && (i) < 13) { asm volatile("" : "+v"(dg)); \
        if ((i) == 0) dg = dg * -1.4426950408889634f; \
        else if ((i) < 5) dg[((i) - 1) & 3] = __builtin_amdgcn_exp2f(dg[((i) - 1) & 3]); \
        else if ((i) == 5) dg = dg + 1.0f; \
        else if ((i) < 10) dg[((i) - 6) & 3] = __builtin_amdgcn_rcpf(dg[((i) - 6) & 3]); \
        else if ((i) == 10) dg = dg * du; \
        else { T.hq[(t & 1) * 2 + (((i) - 11) & 1)] = pk_bf16(dg[(((i) - 11) & 1) * 2], dg[(((i) - 11) & 1) * 2 + 1]); asm volatile("" : "+v"(T.hq[(t & 1) * 2 + (((i) - 11) & 1)])); } \
        if ((i) < 11) asm volatile("" : "+v"(dg)); \
        SBAR(); } } while (0)
    if (KG == 1) { VW(0); VW(1); VA(0); VA(1); VA(2); VA(3); P2(0); P2(1); P2(2); P2(3); P2(4); P2(5); P2(6); P2(7); }
    else { P2(0); P2(1); P2(2); P2(3); VW(0); P2(4); VW(1); P2(5); VA(0); VA(1); P2(6); VA(2); VA(3); P2(7); }
    RL2(7, 1);
#undef RL2
#undef VW
#undef VA
#undef D
#undef P2
    if (t < 8 && (t & 1)) {
        auto r0 = __builtin_amdgcn_permlane16_swap(T.hq[0], T.hq[2], false, false);
        auto r1 = __builtin_amdgcn_permlane16_swap(T.hq[1], T.hq[3], false, false);
        const u32x4 w = {r0[0], r1[0], r0[1], r1[1]};
        asm volatile("global_store_dwordx4 %0, %1, %2 offset:%3 sc1\n\ts_nop 1" :: "v"(T.hl), "v"(w), "s"(T.dd[(t >> 2) & 1]), "i"((t & 2) * 1024) : "memory");
        SBAR();
    }
    if (t == G2_PA || t == G2_PY || t == G2_PZ) {
        asm volatile("global_load_dwordx2 %0, %1, %2 offset:%3 sc1" : "=v"(T.hf) : "v"(t == G2_PZ ? T.foz : T.foa), "s"(T.flags), "i"(t == G2_PY ? 128 : 0) : "memory"); SBAR();
    }
    if (t == G2_F - 1) {
        MFMA_DRAIN(); SBAR();
#pragma unroll
        for (int j = 0; j < 4; ++j) *(LAS f32x4*)(T.xw + j * 1024) = T.acc[1][4 + j];
        SBAR();
    }
    if (t == G2_F) {
#pragma unroll
        for (int j = 0; j < 4; ++j) {
            const u32x4 w = never_ones(__builtin_bit_cast(u32x4, T.acc[0][4 + j] + ho[j]));
            asm volatile("global_store_dwordx4 %0, %1, off offset:%2 sc1\n\ts_nop 1" :: "v"(T.snd), "v"(w), "i"(j * 1024) : "memory");
        }
        SBAR();
    }
    if (t == G2_XST) { asm volatile("global_load_dwordx4 %0, %2, off\n\tglobal_load_dwordx4 %1, %2, off offset:64" : "=&v"(T.xres[0]), "=&v"(T.xres[1]) : "v"(T.xp) : "memory"); SBAR(); }
    if (t == G2_RCV) {
        asm volatile("global_load_dwordx4 %0, %4, off sc1\n\tglobal_load_dwordx4 %1, %4, off offset:1024 sc1\n\tglobal_load_dwordx4 %2, %4, off offset:2048 sc1\n\tglobal_load_dwordx4 %3, %4, off offset:3072 sc1"
                     : "=&v"(T.rc[0]), "=&v"(T.rc[1]), "=&v"(T.rc[2]), "=&v"(T.rc[3]) : "v"(T.rcv) : "memory");
        SBAR();
    }
}
template <int KG, int t, int E> DEVI void g2_run(G2St& T) { g2_stage<KG, t>(T); if constexpr (t + 1 < E) g2_run<KG, t + 1, E>(T); }

DEVI unsigned bsel(unsigned m, unsigned a, unsigned b) { return (m & a) | (~m & b); }
typedef short s16x4 __attribute__((ext_vector_type(4)));
#define MFMA16K16(a, b, c) __builtin_amdgcn_mfma_f32_16x16x16bf16_1k(__builtin_bit_cast(s16x4, a), __builtin_bit_cast(s16x4, b), (c), 0, 0, 0)
template <bool MAX> DEVI float rows_reduce(const float (&v)[4]) {
    auto a = __builtin_amdgcn_permlane16_swap(__float_as_uint(v[0]), __float_as_uint(v[1]), false, false);
    auto b = __builtin_amdgcn_permlane16_swap(__float_as_uint(v[2]), __float_as_uint(v[3]), false, false);
    const float t = MAX ? fmaxf(__uint_as_float(a[0]), __uint_as_float(a[1])) : __uint_as_float(a[0]) + __uint_as_float(a[1]);
    const float u = MAX ? fmaxf(__uint_as_float(b[0]), __uint_as_float(b[1])) : __uint_as_float(b[0]) + __uint_as_float(b[1]);
    auto c = __builtin_amdgcn_permlane32_swap(__float_as_uint(t), __float_as_uint(u), false, false);
    return MAX ? fmaxf(__uint_as_float(c[0]), __uint_as_float(c[1])) : __uint_as_float(c[0]) + __uint_as_float(c[1]);
}
DEVI void rows_spread(float w, float (&v)[4]) {
    auto c = __builtin_amdgcn_permlane32_swap(__float_as_uint(w), __float_as_uint(w), false, false);
    auto a = __builtin_amdgcn_permlane16_swap(c[0], c[0], false, false);
    auto b = __builtin_amdgcn_permlane16_swap(c[1], c[1], false, false);
    v[0] = __uint_as_float(a[0]); v[1] = __uint_as_float(a[1]); v[2] = __uint_as_float(b[0]); v[3] = __uint_as_float(b[1]);
}

template <int GRP>
DEVI void g1_body(G1Ld& L, G2St& T, LAS char* lds, char* ws, int rt, int ct, int wm, int kh STAMP_ARGS) {
    G1Ptr P;
    {
        const int lane = fresh_lane(), g = lane >> 4, q = (lane & 15) >> 2, p = lane & 3;
        const unsigned ldsb = (unsigned)(size_t)lds;
        P.pA = ldsb + wm * 4096 + lane * 16;
        P.pH = ldsb + lane * 16;
        const int brow = (16 * (g >> 1) + 4 * (g & 1) + q) * 224 + p * 8;
        P.pB01 = ldsb + G1_BOFF + brow;
        P.pB23 = P.pB01 + 2 * G1_BST;
        P.pBh01 = P.pB01 + (2 + wm) * 32;
        P.pBh23 = P.pB23 + (2 + wm) * 32;
    }
    f32x4 acc[4][10], acch = {0.f, 0.f, 0.f, 0.f};
#pragma unroll
    for (int m = 0; m < 4; ++m)
#pragma unroll
        for (int n = 0; n < 10; ++n) acc[m][n] = (f32x4){0.f, 0.f, 0.f, 0.f};

    g1_issue<0>(L); g1_issue<1>(L);
#pragma unroll
    for (int m = 0; m < 4; ++m)
        asm volatile("" : "+v"(acc[m][0]), "+v"(acc[m][1]), "+v"(acc[m][2]), "+v"(acc[m][3]), "+v"(acc[m][4]), "+v"(acc[m][5]), "+v"(acc[m][6]), "+v"(acc[m][7]), "+v"(acc[m][8]), "+v"(acc[m][9]), "+v"(acch));
    asm volatile("s_waitcnt vmcnt(5)\n\ts_barrier" ::: "memory", FRAG_REGS);
    g1_ldb<GRP, 0, 0, 0>(P); g1_ldb<GRP, 0, 0, 1>(P); g1_ldb<GRP, 0, 1, 0>(P); g1_ldb<GRP, 0, 1, 1>(P); g1_ldb<GRP, 0, 2, 0>(P); g1_ldb<GRP, 0, 2, 1>(P);
    g1_ldb<GRP, 0, 3, 0>(P); g1_ldb<GRP, 0, 3, 1>(P); g1_ldb<GRP, 0, 4, 0>(P); g1_ldb<GRP, 0, 4, 1>(P);
    if (GRP == 0) { g1_ldah<0>(P); g1_ldbh<0, 0>(P); g1_ldbh<0, 1>(P); }
    g1_lda<0, 0>(P); g1_lda<0, 1>(P);
    g1_ldb<GRP, 0, 5, 0>(P); g1_ldb<GRP, 0, 5, 1>(P); g1_ldb<GRP, 0, 6, 0>(P); g1_ldb<GRP, 0, 6, 1>(P); g1_ldb<GRP, 0, 7, 0>(P); g1_ldb<GRP, 0, 7, 1>(P);
    g1_lda<0, 2>(P); g1_lda<0, 3>(P);
    g1_issue<2>(L);
    if (GRP == 1) g1_issue<3>(L);
    STAMP_SEG(0);
#pragma unroll 1
    for (int it = 0; it < 8; ++it) {
        g1_stage<GRP, 0>(acc, acch, L, it == 7, P);
        if (GRP == 0) g1_tail<0>(L, it == 7);
        g1_stage<GRP, 1>(acc, acch, L, it == 7, P);
        g1_stage<GRP, 2>(acc, acch, L, it == 7, P);
        g1_stage<GRP, 3>(acc, acch, L, it == 7, P);
    }
    asm volatile("s_waitcnt vmcnt(0) lgkmcnt(0)\n\ts_nop 15" ::: "memory", FRAG_REGS);
    SBAR();
    STAMP_SEG(0);

    const int lane = fresh_lane(), g = lane >> 4, i16 = lane & 15;
    char* ho = ws + WS_HO;
    const int mb0 = 16 * rt + 4 * wm;
    const unsigned hl = (unsigned)((g & 1) * 1024 + (g >> 1) * 256 + i16 * 16);
    G2W& W = T.W;
    {
        const int wid = GRP * 4 + wm, r = lane >> 3, c = (lane & 7) ^ ((r & 2) | ((wid & 1) << 2));
        W.vw0 = (unsigned)((8 * wid + r) * 2048 + c * 16 + kh * 128);
        W.vw1 = W.vw0 ^ 128;
        W.vw2 = W.vw0 + 448 * 2048;
    }
    char* hs = ho + (size_t)(64 * (ct >> 4) + (ct & 15)) * SLAB_B + (size_t)mb0 * 1024;
#define GW(i) do { if ((i) < 2 * G2_WL) { g2_w<(i) / 2, (i) % 2>(W); SBAR(); } } while (0)
#define GLU1(n, m) glu_store(acc[m][n], acc[m][5 + (n)], acc[(m) + 1][n], acc[(m) + 1][5 + (n)], hs + (size_t)(16 * (1 + (n) / 2)) * SLAB_B + ((n) & 1) * 512 + (m) * 1024 + hl)
    if (GRP == 1) {
        SBAR(); BAR(); MEMFENCE(); SBAR();
        GW(0); GW(1); GW(2); GW(3); GLU1(0, 0); GW(4); GW(5); GLU1(0, 2); GW(6); GW(7); GLU1(1, 0); GW(8); GW(9); GLU1(1, 2); GW(10); GW(11); GLU1(2, 0); GW(12); GW(13); GLU1(2, 2); GW(14); GW(15);
        asm volatile("s_waitcnt vmcnt(%0)" :: "i"(2 * G2_WL + 4) : "memory");
#pragma unroll
        for (int m = 0; m < 4; ++m) { T.dg[0][m] = acc[m][3]; T.dg[1][m] = acc[m][4]; T.du[0][m] = acc[m][8]; T.du[1][m] = acc[m][9]; }
        T.dd[0] = hs + (size_t)32 * SLAB_B + 512; T.dd[1] = hs + (size_t)48 * SLAB_B;
    } else {
        u32x4 Qf[4], Kf[4]; u32x2 Vh[4][2];
#pragma unroll
        for (int m = 0; m < 4; ++m) {
            Qf[m] = (u32x4){pk_bf16(acc[m][0][0], acc[m][0][1]), pk_bf16(acc[m][0][2], acc[m][0][3]), pk_bf16(acc[m][1][0], acc[m][1][1]), pk_bf16(acc[m][1][2], acc[m][1][3])};
            Kf[m] = (u32x4){pk_bf16(acc[m][2][0], acc[m][2][1]), pk_bf16(acc[m][2][2], acc[m][2][3]), pk_bf16(acc[m][3][0], acc[m][3][1]), pk_bf16(acc[m][3][2], acc[m][3][3])};
#pragma unroll
            for (int b = 0; b < 2; ++b) Vh[m][b] = (u32x2){pk_bf16(acc[m][4 + b][0], acc[m][4 + b][1]), pk_bf16(acc[m][4 + b][2], acc[m][4 + b][3])};
        }
        LAS char* xs = lds + G1_XOFF;
        if (wm < 3) {
            *(LAS u32x4*)(xs + (wm + 1) * 2048 + lane * 16) = Kf[3];
            *(LAS u32x4*)(xs + (wm + 1) * 2048 + 1024 + lane * 16) = (u32x4){Vh[3][0][0], Vh[3][0][1], Vh[3][1][0], Vh[3][1][1]};
        }
        {
            const unsigned h0 = pk_bf16(acch[0], acch[1]), h1 = pk_bf16(acch[2], acch[3]);
            if (wm < 2) {
                *(LAS u32x2*)(xs + lane * 16 + wm * 8) = (u32x2){h0, h1};
            } else {
                LAS char* t = xs + 1024 + ((i16 >> 2) * 16 + 4 * g) * 16 + (wm - 2) * 8 + (i16 & 3) * 2;
                *(LAS u16*)(t) = (u16)h0; *(LAS u16*)(t + 16) = (u16)(h0 >> 16); *(LAS u16*)(t + 32) = (u16)h1; *(LAS u16*)(t + 48) = (u16)(h1 >> 16);
            }
        }
        asm volatile("s_waitcnt lgkmcnt(0)" ::: "memory");
        SBAR(); BAR(); MEMFENCE(); SBAR();
        GW(0); GW(1);
        const u32x4 Kp = *(LAS const u32x4*)(xs + wm * 2048 + lane * 16);
        const u32x4 Vp = *(LAS const u32x4*)(xs + wm * 2048 + 1024 + lane * 16);
        const int dc = i16 - 4 * g - (i16 & 1);
        unsigned modd = (i16 & 1) ? ~0u : 0u, mc0 = dc >= 0 ? ~0u : 0u, mc1 = dc >= 2 ? ~0u : 0u;
        asm volatile("" : "+v"(modd), "+v"(mc0), "+v"(mc1));
        const unsigned half = (modd & 0xffff0000u) | (~modd & 0x0000ffffu);
        const unsigned pp0 = half & ~mc0, pp1 = half & ~mc1, pc0 = half & mc0, pc1 = half & mc1;
        const float sc = 0.17677669529663687f * 1.4426950408889634f;
        const f32x4 z = {0.f, 0.f, 0.f, 0.f};
        float e0[4], e1[4], red[4];
#pragma unroll
        for (int m = 0; m < 4; ++m) {
            u32x4 s0 = __builtin_bit_cast(u32x4, MFMA16(as_frag(m ? Kf[m ? m - 1 : 0] : Kp), as_frag(Qf[m]), z));
            const u32x4 s1 = __builtin_bit_cast(u32x4, MFMA16(as_frag(Kf[m]), as_frag(Qf[m]), z));
            if (m == 0 && rt == 0 && wm == 0) { const unsigned c = __float_as_uint(-1e9f / 0.17677669529663687f); s0 = (u32x4){c, c, c, c}; }
            e0[m] = __uint_as_float(bsel(modd, bsel(mc0, s1[1], s0[1]), bsel(mc0, s1[0], s0[0])));
            e1[m] = __uint_as_float(bsel(modd, bsel(mc1, s1[3], s0[3]), bsel(mc1, s1[2], s0[2])));
            red[m] = fmaxf(e0[m], e1[m]);
        }
        {
            rows_spread(rows_reduce<true>(red), red);
        }
#pragma unroll
        for (int m = 0; m < 4; ++m) {
            e0[m] = __builtin_amdgcn_exp2f((e0[m] - red[m]) * sc); e1[m] = __builtin_amdgcn_exp2f((e1[m] - red[m]) * sc);
            red[m] = e0[m] + e1[m];
        }
        {
            float w = __builtin_amdgcn_rcpf(rows_reduce<false>(red));
            rows_spread(w, red);
        }
        char* od = hs + (((g & 1) << 1) | (g >> 1)) * 256 + i16 * 16;
#pragma unroll
        for (int m = 0; m < 4; ++m) {
            const float p0 = e0[m] * red[m], p1 = e1[m] * red[m];
            const unsigned w0 = pk_bf16(p0, p0), w1 = pk_bf16(p1, p1);
            const u32x2 Pp = {w0 & pp0, w1 & pp1}, Pc = {w0 & pc0, w1 & pc1};
            const u32x2 Vp0 = m ? Vh[m ? m - 1 : 0][0] : (u32x2){Vp[0], Vp[1]}, Vp1 = m ? Vh[m ? m - 1 : 0][1] : (u32x2){Vp[2], Vp[3]};
            const f32x4 o0 = MFMA16K16(Vh[m][0], Pc, MFMA16K16(Vp0, Pp, z));
            const f32x4 o1 = MFMA16K16(Vh[m][1], Pc, MFMA16K16(Vp1, Pp, z));
            unsigned x0 = pk_bf16(o0[0], o0[1]), x1 = pk_bf16(o0[2], o0[3]), y0 = pk_bf16(o1[0], o1[1]), y1 = pk_bf16(o1[2], o1[3]);
            auto r0 = __builtin_amdgcn_permlane16_swap(x0, y0, false, false);
            auto r1 = __builtin_amdgcn_permlane16_swap(x1, y1, false, false);
            st16(od + m * 1024, never_ones((u32x4){r0[0], r1[0], r0[1], r1[1]}));
        }
        SBAR();
        GW(2); GW(3); GW(4); GW(5); GW(6); GW(7); GW(8); GW(9); GW(10); GW(11); GW(12); GW(13); GW(14); GW(15);
        asm volatile("s_waitcnt vmcnt(%0)" :: "i"(2 * G2_WL + 2) : "memory");
#pragma unroll
        for (int m = 0; m < 4; ++m) { T.dg[0][m] = acc[m][6]; T.dg[1][m] = acc[m][7]; T.du[0][m] = acc[m][8]; T.du[1][m] = acc[m][9]; }
        T.dd[0] = hs + (size_t)48 * SLAB_B + 512; T.dd[1] = ho + (size_t)(128 + (ct >> 1)) * SLAB_B + (ct & 1) * 512 + (size_t)mb0 * 1024;
    }
#undef GW
#undef GLU1
    STAMP_SEG(2);
}

template <int KG>
DEVI void g2_body(G2St& T, LAS char* lds, char* ws, const u16* x, u16* out, int rt, int ct, int wm STAMP_ARGS2) {
    constexpr int kg = KG;
    const int bid = blockIdx.x, idx = bid >> 3, wid = KG * 4 + wm;
    const int kh = (bid >> 2) & 1, side = kh ^ 1, rt2 = 2 * rt + ((bid >> 1) & 1), ct2 = 4 * (bid & 1) + (idx & 3);
    const char* flq = ws + WS_FL + (rt * 4 + (bid & 2)) * 512;
    const int lane2 = fresh_lane(), g = lane2 >> 4, i16 = lane2 & 15, q = i16 >> 2, p = lane2 & 3;
    {
        const char* a0 = ws + WS_HO + (size_t)kg * SLAB_B + (size_t)(8 * rt2 + 2 * wm) * 1024;
        T.pa = a0 + (size_t)(64 * side) * SLAB_B;
        T.pad = a0 + (size_t)128 * SLAB_B;
        T.va0 = lane2 * 16 + kg * 1024; T.va1 = T.va0 ^ 1024;
        T.flagA = ws + WS_FL + (kg ^ 1) * 16384 + (rt * 4 + wm) * 512 + ct * 4;
        T.flagZ = ws + WS_FL + (rt * 4 + wm) * 512 + 256 + kg * 128 + ct * 4;
        T.hl = (unsigned)((g & 1) * 1024 + (g >> 1) * 256 + i16 * 16);
        T.poller = wid == 0;
        T.flags = T.poller ? flq : ws + WS_XB + (size_t)bid * 32768;
        T.foa = (unsigned)((lane2 >> 5) * 512 + side * 64 + (lane2 & 7) * 8);
        T.foz = (unsigned)((lane2 >> 5) * 512 + 256 + (lane2 & 31) * 8);
    }
#pragma unroll
    for (int n = 0; n < 4; ++n) {
        T.pB[0][n] = lds + kg * 4096 + (8 * g + q) * 128 + (((2 * n + (p >> 1)) ^ ((q & 2) | ((g & 1) << 2))) * 16) + (p & 1) * 8;
#pragma unroll
        for (int k = 1; k < (G2_NS + 3) / 4; ++k) { T.pB[k][n] = T.pB[0][n] + k * 4 * G2_ST; asm volatile("" : "+v"(T.pB[k][n])); }
        asm volatile("" : "+v"(T.pB[0][n]));
    }
    MEMFENCE(); SBAR(); BAR(); MEMFENCE(); SBAR();
    g2_a<0, 0>(T); g2_a<0, 1>(T); g2_a<1, 0>(T); g2_a<1, 1>(T);
    SBAR();
#pragma unroll
    for (int n = 0; n < 8; ++n) { T.B[n][0] = g2_ldb<0>(T, n, 0); T.B[n][1] = g2_ldb<0>(T, n, 1); }
    SBAR();
    STAMP_SEG(3);
    g2_run<KG, 0, 8>(T);
    STAMP_SEG(4);
    g2_run<KG, 8, 24>(T);
    STAMP_SEG(1);
    const int orow = 128 * rt2 + 32 * wm + 16 * kg + i16, ocol = 128 * ct2 + 64 * kh + (g & 1) * 16 + (g >> 1) * 8;
    T.xw = lds + G2_XOFF + wid * 4096 + lane2 * 16;
    T.xr = lds + G2_XOFF + (wid ^ 4) * 4096 + lane2 * 16;
    T.snd = ws + WS_XB + (size_t)bid * 32768 + wid * 4096 + lane2 * 16;
    T.rcv = ws + WS_XB + (size_t)(bid ^ 4) * 32768 + wid * 4096 + lane2 * 16;
    T.xp = x + (size_t)orow * D_ + ocol;
    g2_run<KG, 24, 36>(T);
    STAMP_SEG(5);

    MFMA_DRAIN(); SBAR();
#pragma unroll
    for (int j = 0; j < 4; ++j) *(LAS f32x4*)(T.xw + j * 1024) = T.acc[1][j];
    WAIT_LGKM0();
    MEMFENCE(); SBAR(); BAR(); MEMFENCE(); SBAR();
#pragma unroll
    for (int j = 0; j < 4; ++j) T.acc[0][j] += *(LAS const f32x4*)(T.xr + j * 1024);
    asm volatile("s_waitcnt vmcnt(0)" : "+v"(T.xres[0]), "+v"(T.xres[1]), "+v"(T.rc[0]), "+v"(T.rc[1]), "+v"(T.rc[2]), "+v"(T.rc[3]) :: "memory");
    for (unsigned spin = 0;; ++spin) {
        const unsigned mx = umax4((u32x4){umax4(T.rc[0]), umax4(T.rc[1]), umax4(T.rc[2]), umax4(T.rc[3])});
        if (!__any(mx == 0xffffffffu) || spin > SPIN_CAP) break;
        __builtin_amdgcn_s_sleep(4);
        asm volatile("global_load_dwordx4 %0, %4, off sc1\n\tglobal_load_dwordx4 %1, %4, off offset:1024 sc1\n\tglobal_load_dwordx4 %2, %4, off offset:2048 sc1\n\tglobal_load_dwordx4 %3, %4, off offset:3072 sc1\n\ts_waitcnt vmcnt(0)"
                     : "=&v"(T.rc[0]), "=&v"(T.rc[1]), "=&v"(T.rc[2]), "=&v"(T.rc[3]) : "v"(T.rcv) : "memory");
    }
    u16* orp = out + (size_t)orow * D_ + ocol;
#pragma unroll
    for (int j = 0; j < 4; j += 2) {
        const f32x4 s0 = T.acc[0][j] + __builtin_bit_cast(f32x4, T.rc[j]), s1 = T.acc[0][j + 1] + __builtin_bit_cast(f32x4, T.rc[j + 1]);
        float v[8];
#pragma unroll
        for (int e = 0; e < 4; ++e) { auto r = __builtin_amdgcn_permlane16_swap(__float_as_uint(s0[e]), __float_as_uint(s1[e]), false, false); v[e] = __uint_as_float(r[0]); v[4 + e] = __uint_as_float(r[1]); }
        const u32x4 xv = T.xres[j >> 1];
        u32x4 o;
#pragma unroll
        for (int e = 0; e < 4; ++e) o[e] = pk_bf16(v[2 * e] + bf_lo(xv[e]), v[2 * e + 1] + bf_hi(xv[e]));
        *(GAS u32x4*)(size_t)(orp + 16 * j) = o;
    }
    STAMP_SEG(6);
    STAMP_OUT(ws, 0, wid, lane2);
    STAMP_OUT(ws, 1, wid, lane2);
}

__global__ __launch_bounds__(512, 2) void k_main(const char* __restrict__ wqkv, const char* __restrict__ wg, const char* __restrict__ wu, const char* __restrict__ wo, const char* __restrict__ wd, char* __restrict__ ws,
                                                const u16* __restrict__ x, u16* __restrict__ out) {
    extern __shared__ __attribute__((aligned(16))) char smem[];
    TL_ENTRY();
    asm volatile("" : "+s"(wqkv), "+s"(wg), "+s"(wu), "+s"(wo), "+s"(wd), "+s"(ws), "+s"(x), "+s"(out));
    LAS char* lds = (LAS char*)smem;
    const int tid = threadIdx.x, lane = tid & 63;
    const int wid = __builtin_amdgcn_readfirstlane(tid >> 6);
    const int bid = blockIdx.x, xcd = bid & 7, idx = bid >> 3;
    const int ct = 4 * xcd + (idx & 3), rt = idx >> 2;
    const unsigned ldsb = (unsigned)(size_t)lds;
    G1Ld L;
    {
        const int c = wid * 64 + lane, rho = c / 14, cc = c - rho * 14;
        const int k = (rho & ~12) | ((rho & 4) << 1) | ((rho & 8) >> 1);
        const int c2 = cc < 12 ? cc : 11;
        const bool last = wid == 7;
        L.pa = ws + WS_XN + (size_t)(16 * rt + 2 * wid) * 1024;
        L.stepa = (unsigned)SLAB_B;
        L.va = lane * 16;
        L.la = ldsb + (1 + 2 * wid) * 1024;
        L.p2 = last ? ws + WS_XN + (size_t)(rt == 0 ? 0 : 16 * rt - 1) * 1024 : wqkv + ct * 64;
        L.step2 = last ? (unsigned)SLAB_B : 32u * 3072u * 2u;
        L.v2 = last ? (unsigned)((rt == 0 ? (lane & 48) : lane) * 16) : (unsigned)(k * 6144 + (c2 >> 2) * 2048 + (c2 & 3) * 16);
        L.l2 = last ? ldsb : ldsb + G1_BOFF + wid * 1024;
        L.s2 = last ? G1_AST : G1_BST;
        L.pg = wg + ct * 224;
        L.pu = wu + ct * 224;
        L.stepg = 32u * 3584u * 2u;
        L.vg = last ? 0u : (unsigned)(k * 7168 + cc * 16);
        L.lg = last ? ldsb + G1_BOFF + 192 : ldsb + G1_BOFF + G1_REG + wid * 1024;
        L.lu = last ? ldsb + G1_BOFF + 192 : ldsb + G1_BOFF + 2 * G1_REG + wid * 1024;
        L.ma = ~0ull; L.m2 = ~0ull; L.mg = last ? 1ull : ~0ull;
    }
    L.warm = wid < 4 ? wo + bid * 8192 + wid * 2048 : wd + bid * 28672 + (wid & 3) * 7168;
    L.lw = ldsb + G1_XOFF + (wid & 3) * 1024; L.ldum = ldsb + G1_BOFF + 192;
    L.pf = ws + WS_XB + (size_t)bid * 32768 + (wid & 3) * 8192;
    const int kh = (bid >> 2) & 1, side = kh ^ 1, rt2 = 2 * rt + ((bid >> 1) & 1), ct2 = 4 * (bid & 1) + (idx & 3);
    const int kg = wid >> 2, wm = wid & 3;
    G2St T;
    T.W.pwo = wo + (size_t)(side * 512) * 2048 + ct2 * 256;
    T.W.pwh = wd + (size_t)(side * 1792 + kg * 80) * 2048 + ct2 * 256;
    T.W.pwd = wd + (size_t)(96 + (wid >> 1) * 96) * 2048 + ct2 * 256;
    T.W.ldsw = ldsb + wid * 1024;
    STAMP_DECL STAMP_START();
    if (wid < 4) { g1_body<0>(L, T, lds, ws, rt, ct, wm, kh STAMP_PASS); g2_body<0>(T, lds, ws, x, out, rt, ct, wm STAMP_PASS2); }
    else { g1_body<1>(L, T, lds, ws, rt, ct, wm, kh STAMP_PASS); g2_body<1>(T, lds, ws, x, out, rt, ct, wm STAMP_PASS2); }
    TL_EXIT(ws, 1);
    TL_EXIT(ws, 2);

}

extern "C" void kernel_launch(void* const* d_in, const int* in_sizes, int n_in,
                              void* d_out, int out_size, void* d_ws, size_t ws_size,
                              hipStream_t stream) {
    const u16* x = (const u16*)d_in[0];
    const char* wqkv = (const char*)d_in[1];
    const char* wo = (const char*)d_in[2];
    const char* wg = (const char*)d_in[4];
    const char* wu = (const char*)d_in[5];
    const char* wd = (const char*)d_in[6];
    char* ws = (char*)d_ws;
    (void)hipFuncSetAttribute((const void*)k_main, hipFuncAttributeMaxDynamicSharedMemorySize, G1_LDS);
    k_ln<<<256, 512, 0, stream>>>(x, ws);
    k_main<<<256, 512, G1_LDS, stream>>>(wqkv, wg, wu, wo, wd, ws, x, (u16*)d_out);
}
```

```cpp
#include <hip/hip_runtime.h>
#include <hip/hip_bf16.h>
#include <stdint.h>

typedef __bf16 bf16x8 __attribute__((ext_vector_type(8)));
typedef __bf16 bf16x4 __attribute__((ext_vector_type(4)));
typedef __bf16 bf16x2 __attribute__((ext_vector_type(2)));
typedef float f32x4 __attribute__((ext_vector_type(4)));
typedef float f32x2 __attribute__((ext_vector_type(2)));
typedef unsigned u32x4 __attribute__((ext_vector_type(4)));
typedef unsigned u32x2 __attribute__((ext_vector_type(2)));
typedef unsigned short u16;
#define LAS __attribute__((address_space(3)))
#define GAS __attribute__((address_space(1)))
#define DEVI __device__ __forceinline__

constexpr int S_ = 2048, D_ = 1024, F_ = 3584;
constexpr int MB_ = S_ / 16;
constexpr size_t SLAB_B = (size_t)MB_ * 1024;
constexpr size_t WS_XN = 0;
constexpr size_t WS_HO = 32 * SLAB_B;
constexpr size_t WS_XB = WS_HO + 144 * SLAB_B;
constexpr size_t WS_FL = WS_XB + 256 * 32768;
constexpr size_t WS_LF = WS_FL + 32768;
constexpr size_t WS_EP = WS_LF + 65536;

DEVI unsigned pk_bf16(float lo, float hi) {
    bf16x2 v = {(__bf16)lo, (__bf16)hi};
    return __builtin_bit_cast(unsigned, v);
}
DEVI float bf_lo(unsigned u) { return __uint_as_float(u << 16); }
DEVI float bf_hi(unsigned u) { return __uint_as_float(u & 0xffff0000u); }
DEVI float rbf(float f) { return (float)(__bf16)f; }
DEVI bf16x8 as_frag(u32x4 v) { return __builtin_bit_cast(bf16x8, v); }
#define MFMA16(a, b, c) __builtin_amdgcn_mfma_f32_16x16x32_bf16((a), (b), (c), 0, 0, 0)
#define SBAR() __builtin_amdgcn_sched_barrier(0)
#define MFMA_ACC(c, a, b) asm volatile("v_mfma_f32_16x16x32_bf16 %0, %1, %2, %0" : "+v"(c) : "v"(a), "v"(b))
#define MFMA_NEW(c, a, b) asm volatile("v_mfma_f32_16x16x32_bf16 %0, %1, %2, 0" : "=&v"(c) : "v"(a), "v"(b))
#define MFMA_DRAIN() asm volatile("s_nop 15" ::: "memory")
#define BAR() __builtin_amdgcn_s_barrier()

#define TL_ENTRY()
#define TL_EXIT(ws, kern)
#define STAMP_DECL
#define STAMP_ARGS
#define STAMP_PASS
#define STAMP_ARGS2
#define STAMP_PASS2
#define STAMP_SEG(i)
#define STAMP_START()
#define STAMP_SEGI(i)
#define STAMP_OUT(ws, kern, wid, lane)

#define P0S(i)
DEVI int fresh_lane() { int l; asm volatile("v_mbcnt_lo_u32_b32 %0, -1, 0\n\tv_mbcnt_hi_u32_b32 %0, -1, %0" : "=v"(l)); return l; }

DEVI bf16x8 tr_frag(LAS const char* p, int off0, int off1) {
    bf16x4 lo = __builtin_amdgcn_ds_read_tr16_b64_v4bf16((LAS bf16x4*)(p + off0));
    bf16x4 hi = __builtin_amdgcn_ds_read_tr16_b64_v4bf16((LAS bf16x4*)(p + off1));
    return __builtin_shufflevector(lo, hi, 0, 1, 2, 3, 4, 5, 6, 7);
}

DEVI void glds16(const char* sbase, unsigned voff, unsigned ldsdst) {
    unsigned keep;
    asm volatile("s_mov_b32 %0, m0\n\ts_mov_b32 m0, %3\n\ts_nop 0\n\tglobal_load_lds_dwordx4 %1, %2\n\ts_mov_b32 m0, %0"
                 : "=&s"(keep) : "v"(voff), "s"(sbase), "s"(ldsdst) : "memory");
}

constexpr int G1_AST = 17 * 1024;
constexpr int G1_BST = 21 * 1024;
constexpr int G1_REG = 7168;
constexpr int G1_BOFF = 4 * G1_AST;
constexpr int G1_XOFF = G1_BOFF + 4 * G1_BST;
constexpr int G1_LDS = G1_XOFF + 8192;

typedef unsigned long long u64;
#define FRAG_REGS "v192", "v193", "v194", "v195", "v196", "v197", "v198", "v199", "v200", "v201", "v202", "v203", "v204", "v205", "v206", "v207", "v208", "v209", "v210", "v211", "v212", "v213", "v214", "v215", "v216", "v217", "v218", "v219", "v220", "v221", "v222", "v223", "v224", "v225", "v226", "v227", "v228", "v229", "v230", "v231", "v232", "v233", "v234", "v235", "v236", "v237", "v238", "v239", "v240", "v241", "v242", "v243", "v244", "v245", "v246", "v247", "v248", "v249", "v250", "v251", "v252", "v253", "v254", "v255"
constexpr int FA = 192, FB = 208, FAH = 248, FBH = 252;
struct G1Ld {
    const char *pa, *p2, *pg, *pu;
    unsigned stepa, step2, stepg;
    unsigned va, v2, vg;
    unsigned la, l2, s2, lg, lu;
    const char* warm; unsigned lw, ldum;
    char* pf;
    u64 ma, m2, mg;
};

#define WAIT_LGKM0() __builtin_amdgcn_s_waitcnt(0xC07F)
#define MEMFENCE() asm volatile("" ::: "memory")
#define OPAQUE_S(x) asm volatile("" : "+s"(x))

template <int LOFF, int KIND>
DEVI void g1_piece(const char* base, unsigned voff, unsigned lds, u64 msk) {
    if (KIND == 0) asm volatile("s_add_u32 m0, %2, %4\n\ts_mov_b64 exec, %3\n\tglobal_load_lds_dwordx4 %0, %1\n\ts_mov_b64 exec, -1" :: "v"(voff), "s"(base), "s"(lds), "s"(msk), "i"(LOFF) : "memory", "scc");
    if (KIND == 1) asm volatile("s_add_u32 m0, %2, %4\n\ts_mov_b64 exec, %3\n\tglobal_load_lds_dwordx4 %0, %1 sc1\n\ts_mov_b64 exec, -1" :: "v"(voff), "s"(base), "s"(lds), "s"(msk), "i"(LOFF) : "memory", "scc");
    if (KIND == 2) asm volatile("s_add_u32 m0, %2, %4\n\ts_mov_b64 exec, %3\n\tglobal_load_lds_dwordx4 %0, %1 sc1\n\tglobal_load_lds_dwordx4 %0, %1 offset:1024 sc1\n\ts_mov_b64 exec, -1"
                                :: "v"(voff), "s"(base), "s"(lds), "s"(msk), "i"(LOFF) : "memory", "scc");
}
template <int SLOT> DEVI void g1_pa(G1Ld& L) { g1_piece<SLOT * G1_AST, 2>(L.pa, L.va, L.la, L.ma); L.pa += L.stepa; OPAQUE_S(L.pa); }
template <int SLOT> DEVI void g1_p2(G1Ld& L) { g1_piece<0, 1>(L.p2, L.v2, L.l2 + SLOT * L.s2, L.m2); L.p2 += L.step2; OPAQUE_S(L.p2); }
template <int SLOT> DEVI void g1_pg(G1Ld& L) { g1_piece<SLOT * G1_BST, 0>(L.pg, L.vg, L.lg, L.mg); L.pg += L.stepg; OPAQUE_S(L.pg); }
template <int SLOT> DEVI void g1_pu(G1Ld& L) { g1_piece<SLOT * G1_BST, 0>(L.pu, L.vg, L.lu, L.mg); L.pu += L.stepg; OPAQUE_S(L.pu); }
template <int SLOT> DEVI void g1_issue(G1Ld& L) { g1_pa<SLOT>(L); g1_p2<SLOT>(L); g1_pg<SLOT>(L); g1_pu<SLOT>(L); }
template <int GRP> DEVI void g1_tail(G1Ld& L, bool tail) {
    L.p2 = tail ? L.p2 - L.step2 : L.p2; L.pg = tail ? L.pg - L.stepg : L.pg; L.pu = tail ? L.pu - L.stepg : L.pu;
    L.step2 = tail ? 0u : L.step2; L.stepg = tail ? 0u : L.stepg;
    L.m2 = tail ? 1ull : L.m2; L.mg = tail ? 1ull : L.mg;
    if (GRP == 0) L.pa = tail ? L.warm : L.pa;
    else { L.pa = tail ? L.pa - L.stepa : L.pa; L.ma = tail ? 1ull : L.ma; L.warm = tail ? L.warm - 1024 : L.warm; L.lw = tail ? L.ldum : L.lw; }
    L.stepa = tail ? 0u : L.stepa;
}

struct G1Ptr { unsigned pA, pH, pB01, pB23, pBh01, pBh23; };

template <int REG, int OFF> DEVI void ds_tr(unsigned base) { asm volatile("ds_read_b64_tr_b16 v[%c1:%c2], %0 offset:%c3" :: "v"(base), "i"(REG), "i"(REG + 1), "i"(OFF) : "memory"); }
template <int REG, int OFF> DEVI void ds_128(unsigned base) { asm volatile("ds_read_b128 v[%c1:%c2], %0 offset:%c3" :: "v"(base), "i"(REG), "i"(REG + 3), "i"(OFF) : "memory"); }
template <int RA_, int RB_> DEVI void mfma_fr(f32x4& c) { asm volatile("v_mfma_f32_16x16x32_bf16 %0, v[%c1:%c2], v[%c3:%c4], %0" : "+v"(c) : "i"(RA_), "i"(RA_ + 3), "i"(RB_), "i"(RB_ + 3)); }
template <int GRP, int SL, int n, int h> DEVI void g1_ldb(const G1Ptr& P) {
    constexpr int off = (SL & 1) * G1_BST + (GRP == 0 ? (n < 6 ? n * 32 : (n < 8 ? G1_REG + (n - 1) * 32 : 2 * G1_REG + (n - 3) * 32))
                                                      : (n < 5 ? G1_REG + n * 32 : 2 * G1_REG + (n - 5) * 32));
    ds_tr<FB + 4 * n + 2 * h, off + h * 8 * 224>((SL < 2) ? P.pB01 : P.pB23);
}
template <int SL, int m> DEVI void g1_lda(const G1Ptr& P) { ds_128<FA + 4 * m, SL * G1_AST + (1 + m) * 1024>(P.pA); }
template <int SL> DEVI void g1_ldah(const G1Ptr& P) { ds_128<FAH, SL * G1_AST>(P.pH); }
template <int SL, int h> DEVI void g1_ldbh(const G1Ptr& P) { ds_tr<FBH + 2 * h, (SL & 1) * G1_BST + h * 8 * 224>((SL < 2) ? P.pBh01 : P.pBh23); }

template <int GRP, int ST>
DEVI void g1_stage(f32x4 (&acc)[4][10], f32x4& acch, G1Ld& L, bool tail, const G1Ptr& P) {
    constexpr int NX = (ST + 1) & 3, IS = (ST + 3) & 3;
#define MM(m, n) do { if (GRP == 0 && ((n) == 4 || (n) == 5)) mfma_fr<FA + 4 * (m), FB + 4 * (n)>(acc[m][n]); else mfma_fr<FB + 4 * (n), FA + 4 * (m)>(acc[m][n]); } while (0)
#define RB(n, h) g1_ldb<GRP, NX, n, h>(P)
#define RC(n, h) g1_ldb<GRP, ST, n, h>(P)
#define RA(m) g1_lda<NX, m>(P)
#define G0(x) do { if (GRP == 0) { x; } } while (0)
    asm volatile("s_waitcnt lgkmcnt(9)" ::: "memory");
    MM(0, 0); RC(8, 0);
    asm volatile("s_waitcnt lgkmcnt(9)" ::: "memory");
    MM(1, 0); RC(8, 1);
    MM(1, 1); RC(9, 0); MM(0, 1); RC(9, 1);
    MM(0, 2); G0(g1_pa<IS>(L)); MM(1, 2);
    MM(1, 3); G0(g1_p2<IS>(L)); MM(0, 3); G0(g1_pg<IS>(L));
    MM(0, 4); G0(g1_pu<IS>(L)); MM(1, 4);
    G0((mfma_fr<FBH, FAH>(acch)));
    asm volatile("s_waitcnt vmcnt(10) lgkmcnt(0)\n\ts_barrier" ::: "memory", FRAG_REGS);
    if (GRP == 1) {
        if (ST == 0) g1_tail<1>(L, tail);
        g1_issue<ST>(L);
        if (ST == 1) { g1_piece<0, 0>(L.warm, L.va, L.lw, L.ma); L.warm += 1024; OPAQUE_S(L.warm); }
        if (ST == 2) {
            const u32x4 ones = {0xffffffffu, 0xffffffffu, 0xffffffffu, 0xffffffffu};
            asm volatile("global_store_dwordx4 %0, %1, %2 sc1\n\ts_nop 1" :: "v"(L.va), "v"(ones), "s"(L.pf) : "memory", FRAG_REGS);
            L.pf += 1024; OPAQUE_S(L.pf);
        }
    }
    MM(2, 0); MM(3, 0); RB(0, 0);
    MM(3, 1); RB(0, 1); MM(2, 1); RB(1, 0);
    MM(2, 2); RB(1, 1); MM(3, 2); RB(2, 0);
    MM(3, 3); RB(2, 1); MM(2, 3); RB(3, 0);
    MM(2, 4); RB(3, 1); MM(3, 4); RB(4, 0);
    MM(0, 5); RB(4, 1); MM(1, 5); G0(g1_ldah<NX>(P));
    MM(1, 6); G0((g1_ldbh<NX, 0>(P))); MM(0, 6); G0((g1_ldbh<NX, 1>(P)));
    MM(0, 7); MM(1, 7);
    MM(1, 8); MM(0, 8);
    MM(0, 9); RA(0); MM(1, 9); RA(1);
    MM(2, 5); MM(3, 5); RB(5, 0);
    MM(3, 6); RB(5, 1); MM(2, 6); RB(6, 0);
    MM(2, 7); RB(6, 1); MM(3, 7); RB(7, 0);
    MM(3, 8); RB(7, 1); MM(2, 8);
    MM(2, 9); RA(2); MM(3, 9); RA(3);
#undef MM
#undef RB
#undef RC
#undef RA
#undef G0
}

DEVI void st16(char* dst, u32x4 v) {
    asm volatile("global_store_dwordx4 %0, %1, off sc1\n\ts_nop 1" :: "v"(dst), "v"(v) : "memory");
}

DEVI unsigned glu2(float g0, float g1, float u0, float u1) {
    const f32x2 t = (f32x2){g0, g1} * -1.4426950408889634f;
    const f32x2 d = (f32x2){__builtin_amdgcn_exp2f(t[0]), __builtin_amdgcn_exp2f(t[1])} + 1.0f;
    const f32x2 h = (f32x2){u0, u1} * (f32x2){__builtin_amdgcn_rcpf(d[0]), __builtin_amdgcn_rcpf(d[1])};
    return pk_bf16(h[0], h[1]);
}

DEVI void glu_store(const f32x4& g0, const f32x4& u0, const f32x4& g1, const f32x4& u1, char* dst) {
    unsigned a0 = glu2(g0[0], g0[1], u0[0], u0[1]), a1 = glu2(g0[2], g0[3], u0[2], u0[3]);
    unsigned b0 = glu2(g1[0], g1[1], u1[0], u1[1]), b1 = glu2(g1[2], g1[3], u1[2], u1[3]);
    auto r0 = __builtin_amdgcn_permlane16_swap(a0, b0, false, false);
    auto r1 = __builtin_amdgcn_permlane16_swap(a1, b1, false, false);
    st16(dst, (u32x4){r0[0], r1[0], r0[1], r1[1]});
    SBAR();
}

#define W_EARLY 1
#define LN_SLEEP 2
#define POLL_SLEEP 8
#define G2_RCV 34
#define G2_PLACE 1
#define G2_NS 8
constexpr int G2_ST = 16384;
constexpr int G2_WL = G2_NS - 1;
constexpr int G2_XOFF = G2_NS <= 8 ? G2_NS * G2_ST : 0;
constexpr int G2_F = 32, G2_XST = 33;
constexpr int G2_FY = 8, G2_FZ = 12;
constexpr int G2_PA = 1, G2_PY = 9, G2_PZ = 15;
constexpr unsigned SPIN_CAP = 1u << 17;
static_assert(G2_RCV > G2_F && G2_RCV <= 35 && G2_NS >= 6 && G2_NS <= 9, "");
constexpr int g2_grp(int t) { return t < G2_F ? t : G2_F + 2 * (t - G2_F); }
constexpr int g2_afirst(int s) { return s < 2 ? 2 + 2 * s : s + 4 <= 35 ? g2_grp(s + 4) : 40; }
constexpr int g2_alast(int s) { return g2_afirst(s + 1); }
constexpr int g2_tops(int s) { return s == 0 || s == G2_FY || s == G2_FZ ? 1 : 0; }
constexpr int g2_wops(int s) { return s + G2_WL <= 35 ? 2 : 0; }
constexpr int g2_xops(int s) { return (s < 8 && (s & 1) ? 1 : 0) + (s == G2_PA || s == G2_PY || s == G2_PZ ? 1 : 0) + (s == G2_F ? 4 : 0) + (s == G2_XST ? 2 : 0) + (s == G2_RCV ? 4 : 0); }
constexpr int g2_wait(int t) {
    const int need = g2_grp(t + 1) - 1;
    int n = 4, last = need < 2 ? 2 * need + 2 : 0;
    for (int s = 0; s < t; ++s) {
        n += g2_tops(s) + g2_wops(s);
        if (s + G2_WL == t + 1 && n > last) last = n;
        for (int g = g2_afirst(s); g < g2_alast(s); ++g) { n += 2; if (g == need && n > last) last = n; }
        n += g2_xops(s);
        if ((t == 4 && s == G2_PA) || (t == 12 && s == G2_PY) || (t == 20 && s == G2_PZ) || (t == G2_FY && s == 3) || (t == G2_FZ && s == 7)) last = n > last ? n : last;
    }
    return n - last;
}

struct G2W {
    const char *pwo, *pwh, *pwd;
    unsigned vw0, vw1, vw2, ldsw;
};
struct G2St {
    G2W W;
    const char *pa, *pad;
    unsigned va0, va1;
    LAS const char* pB[(G2_NS + 3) / 4][4];
    LAS char* xw; LAS const char* xr;
    char* snd; const char* rcv; const u16* xp;
    f32x4 dg[2][4], du[2][4]; char* dd[2]; unsigned hl, hq[4];
    char *flagA, *flagZ; const char* flags; unsigned foa, foz, tag; u32x2 hf; bool poller;
    u32x4 A[40][2];
    bf16x4 B[8][2];
    f32x4 acc[2][8];
    u32x4 xres[2], rc[4];
};
template <int LOFF> DEVI void g2_glds(const char* base, unsigned voff, unsigned lds) {
    asm volatile("s_add_u32 m0, %2, %3\n\ts_nop 0\n\tglobal_load_lds_dwordx4 %0, %1" :: "v"(voff), "s"(base), "s"(lds), "i"(LOFF) : "memory", "scc");
}
template <int ST, int I> DEVI void g2_w(const G2W& W) {
    constexpr int SLOT = ST % G2_NS;
    const char* src = ST < 8 ? W.pwo + ST * 64 * 2048 : ST < G2_F ? W.pwh + (((ST - 8) & 7) * 224 + ((ST - 8) >> 3) * 32) * 2048 : W.pwd + (ST - G2_F) * 896 * 2048;
    g2_glds<SLOT * G2_ST + I * 8192>(src, I == 0 ? W.vw0 : ST < G2_F ? W.vw1 : W.vw2, W.ldsw);
}
template <int G> DEVI const char* g2_asrc(const G2St& T) {
    return G < G2_F ? T.pa + (size_t)G * 2 * SLAB_B : T.pad + (size_t)(G - G2_F) * 2 * SLAB_B; }
template <int G, int I> DEVI void g2_a(G2St& T) {
    asm volatile("global_load_dwordx4 %0, %1, %2 sc1" : "=v"(T.A[G][I]) : "v"(I ? T.va1 : T.va0), "s"(g2_asrc<G>(T)) : "memory");
}
template <int ST> DEVI bf16x4 g2_ldb(G2St& T, int n, int h) {
    constexpr int SLOT = ST % G2_NS;
    return __builtin_amdgcn_ds_read_tr16_b64_v4bf16((LAS bf16x4*)(T.pB[SLOT / 4][n & 3] + (SLOT % 4) * G2_ST + (n >> 2) * 8192 + h * 512));
}
DEVI bf16x8 cat(bf16x4 lo, bf16x4 hi) { return __builtin_shufflevector(lo, hi, 0, 1, 2, 3, 4, 5, 6, 7); }
template <int OFF> DEVI void flag_set(char* p, unsigned tag) { const unsigned zero = 0u; asm volatile("s_mov_b64 exec, 1\n\tglobal_store_dword %0, %1, %2 offset:%3 sc1\n\ts_mov_b64 exec, -1" :: "v"(zero), "v"(tag), "s"(p), "i"(OFF) : "memory"); }
template <int SLEEP> DEVI void flags_wait(u32x2 f, const char* base, unsigned voff, unsigned tag) {
#pragma unroll 1
    for (unsigned spin = 0; __any(f[0] != tag || f[1] != tag) && spin < SPIN_CAP; ++spin) {
        if (spin) __builtin_amdgcn_s_sleep(SLEEP);
        asm volatile("global_load_dwordx2 %0, %1, %2 sc1\n\ts_waitcnt vmcnt(0)" : "=&v"(f) : "v"(voff), "s"(base) : "memory");
    }
}
DEVI unsigned umax4(u32x4 v) { const unsigned a = v[0] > v[1] ? v[0] : v[1], b = v[2] > v[3] ? v[2] : v[3]; return a > b ? a : b; }
DEVI u32x4 never_ones(u32x4 w) {
#pragma unroll
    for (int e = 0; e < 4; ++e) w[e] = w[e] < 0xfffffffeu ? w[e] : 0xfffffffeu;
    return w;
}

template <int KG, int t>
DEVI void g2_stage(G2St& T) {
    constexpr bool TB = t >= G2_F, ISW = g2_wops(t) != 0, RL = t < 35, CHK = t == 4 || t == 12 || t == 20;
    constexpr int G0 = g2_grp(t), G1 = TB ? G0 + 1 : G0, NA = g2_alast(t) - g2_afirst(t), GN = NA ? g2_afirst(t) : 0, GN1 = NA == 2 ? GN + 1 : GN;
    u32x4 (&Aa)[2] = T.A[G0]; u32x4 (&Ab)[2] = T.A[G1];
    if (TB) asm volatile("s_waitcnt vmcnt(%4)" : "+v"(Aa[0]), "+v"(Aa[1]), "+v"(Ab[0]), "+v"(Ab[1]) : "i"(g2_wait(t)) : "memory");
    else if (CHK) asm volatile("s_waitcnt vmcnt(%3)" : "+v"(Aa[0]), "+v"(Aa[1]), "+v"(T.hf) : "i"(g2_wait(t)) : "memory");
    else asm volatile("s_waitcnt vmcnt(%2)" : "+v"(Aa[0]), "+v"(Aa[1]) : "i"(g2_wait(t)) : "memory");
    if (t == 0) flag_set<0>(T.flagA, T.tag);
    if (t == G2_FY) flag_set<128>(T.flagA, T.tag);
    if (t == G2_FZ) flag_set<0>(T.flagZ, T.tag);
    if (t < 8) {
#pragma unroll 1
        for (unsigned spin = 0; spin < SPIN_CAP; ++spin) {
            const unsigned a = umax4(Aa[0]), b = umax4(Aa[1]);
            if (!__any((a > b ? a : b) == 0xffffffffu)) break;
            __builtin_amdgcn_s_sleep(POLL_SLEEP);
            asm volatile("global_load_dwordx4 %0, %2, %4 sc1\n\tglobal_load_dwordx4 %1, %3, %4 sc1\n\ts_waitcnt vmcnt(0)" : "=&v"(Aa[0]), "=&v"(Aa[1]) : "v"(T.va0), "v"(T.va1), "s"(g2_asrc<G0>(T)) : "memory");
        }
    }
    if (KG == 0 && CHK && T.poller) flags_wait<POLL_SLEEP>(T.hf, t == 12 ? T.flags + 128 : T.flags, t == 20 ? T.foz : T.foa, T.tag);
    if (t == G2_F) WAIT_LGKM0();
    { MEMFENCE(); SBAR(); BAR(); MEMFENCE(); SBAR(); }
    f32x4 ho[4];
    if (t == G2_F) {
#pragma unroll
        for (int j = 0; j < 4; ++j) ho[j] = *(LAS const f32x4*)(T.xr + j * 1024);
        SBAR();
    }
#define AB2DS true
#define RL2(n, h) do { if (AB2DS && RL && (n) >= 0) { T.B[(n) >= 0 ? (n) : 0][h] = g2_ldb<t + 1>(T, (n) >= 0 ? (n) : 0, h); SBAR(); } } while (0)
#define P2(n) do { constexpr int c_ = TB ? ((n) & 3) : (n); u32x4 (&A_)[2] = (n) < 4 ? Aa : Ab; const bf16x8 b_ = cat(T.B[n][0], T.B[n][1]); \
                   if (t == 0) MFMA_NEW(T.acc[(n) & 1][c_], b_, A_[(n) & 1]); else MFMA_ACC(T.acc[(n) & 1][c_], b_, A_[(n) & 1]); SBAR(); RL2((n) - 1, 1); D((n) * 2); \
                   if (t == 0) MFMA_NEW(T.acc[~(n) & 1][c_], b_, A_[~(n) & 1]); else MFMA_ACC(T.acc[~(n) & 1][c_], b_, A_[~(n) & 1]); SBAR(); RL2(n, 0); D((n) * 2 + 1); } while (0)
#define VW(i) do { if (ISW) { g2_w<ISW ? t + G2_WL : 0, i>(T.W); SBAR(); } } while (0)
#define VA(i) do { if (NA == 2) { g2_a<(i) < 2 ? GN : GN1, (i) & 1>(T); SBAR(); } else if (NA && ((i) & 1)) { g2_a<GN, (i) / 2>(T); SBAR(); } } while (0)
    f32x4& dg = T.dg[(t >> 2) & 1][t & 3]; f32x4& du = T.du[(t >> 2) & 1][t & 3];
#define D(i) do { if (t < 8 && (i) < 13) { asm volatile("" : "+v"(dg)); \
        if ((i) == 0) dg = dg * -1.4426950408889634f; \
        else if ((i) < 5) dg[((i) - 1) & 3] = __builtin_amdgcn_exp2f(dg[((i) - 1) & 3]); \
        else if ((i) == 5) dg = dg + 1.0f; \
        else if ((i) < 10) dg[((i) - 6) & 3] = __builtin_amdgcn_rcpf(dg[((i) - 6) & 3]); \
        else if ((i) == 10) dg = dg * du; \
        else { T.hq[(t & 1) * 2 + (((i) - 11) & 1)] = pk_bf16(dg[(((i) - 11) & 1) * 2], dg[(((i) - 11) & 1) * 2 + 1]); asm volatile("" : "+v"(T.hq[(t & 1) * 2 + (((i) - 11) & 1)])); } \
        if ((i) < 11) asm volatile("" : "+v"(dg)); \
        SBAR(); } } while (0)
    if (KG == 1) { VW(0); VW(1); VA(0); VA(1); VA(2); VA(3); P2(0); P2(1); P2(2); P2(3); P2(4); P2(5); P2(6); P2(7); }
    else { P2(0); P2(1); P2(2); P2(3); VW(0); P2(4); VW(1); P2(5); VA(0); VA(1); P2(6); VA(2); VA(3); P2(7); }
    RL2(7, 1);
#undef RL2
#undef VW
#undef VA
#undef D
#undef P2
    if (t < 8 && (t & 1)) {
        auto r0 = __builtin_amdgcn_permlane16_swap(T.hq[0], T.hq[2], false, false);
        auto r1 = __builtin_amdgcn_permlane16_swap(T.hq[1], T.hq[3], false, false);
        const u32x4 w = {r0[0], r1[0], r0[1], r1[1]};
        asm volatile("global_store_dwordx4 %0, %1, %2 offset:%3 sc1\n\ts_nop 1" :: "v"(T.hl), "v"(w), "s"(T.dd[(t >> 2) & 1]), "i"((t & 2) * 1024) : "memory");
        SBAR();
    }
    if (t == G2_PA || t == G2_PY || t == G2_PZ) {
        asm volatile("global_load_dwordx2 %0, %1, %2 offset:%3 sc1" : "=v"(T.hf) : "v"(t == G2_PZ ? T.foz : T.foa), "s"(T.flags), "i"(t == G2_PY ? 128 : 0) : "memory"); SBAR();
    }
    if (t == G2_F - 1) {
        MFMA_DRAIN(); SBAR();
#pragma unroll
        for (int j = 0; j < 4; ++j) *(LAS f32x4*)(T.xw + j * 1024) = T.acc[1][4 + j];
        SBAR();
    }
    if (t == G2_F) {
#pragma unroll
        for (int j = 0; j < 4; ++j) {
            const u32x4 w = never_ones(__builtin_bit_cast(u32x4, T.acc[0][4 + j] + ho[j]));
            asm volatile("global_store_dwordx4 %0, %1, off offset:%2 sc1\n\ts_nop 1" :: "v"(T.snd), "v"(w), "i"(j * 1024) : "memory");
        }
        SBAR();
    }
    if (t == G2_XST) { asm volatile("global_load_dwordx4 %0, %2, off\n\tglobal_load_dwordx4 %1, %2, off offset:64" : "=&v"(T.xres[0]), "=&v"(T.xres[1]) : "v"(T.xp) : "memory"); SBAR(); }
    if (t == G2_RCV) {
        asm volatile("global_load_dwordx4 %0, %4, off sc1\n\tglobal_load_dwordx4 %1, %4, off offset:1024 sc1\n\tglobal_load_dwordx4 %2, %4, off offset:2048 sc1\n\tglobal_load_dwordx4 %3, %4, off offset:3072 sc1"
                     : "=&v"(T.rc[0]), "=&v"(T.rc[1]), "=&v"(T.rc[2]), "=&v"(T.rc[3]) : "v"(T.rcv) : "memory");
        SBAR();
    }
}
template <int KG, int t, int E> DEVI void g2_run(G2St& T) { g2_stage<KG, t>(T); if constexpr (t + 1 < E) g2_run<KG, t + 1, E>(T); }

DEVI unsigned bsel(unsigned m, unsigned a, unsigned b) { return (m & a) | (~m & b); }
typedef short s16x4 __attribute__((ext_vector_type(4)));
#define MFMA16K16(a, b, c) __builtin_amdgcn_mfma_f32_16x16x16bf16_1k(__builtin_bit_cast(s16x4, a), __builtin_bit_cast(s16x4, b), (c), 0, 0, 0)
template <bool MAX> DEVI float rows_reduce(const float (&v)[4]) {
    auto a = __builtin_amdgcn_permlane16_swap(__float_as_uint(v[0]), __float_as_uint(v[1]), false, false);
    auto b = __builtin_amdgcn_permlane16_swap(__float_as_uint(v[2]), __float_as_uint(v[3]), false, false);
    const float t = MAX ? fmaxf(__uint_as_float(a[0]), __uint_as_float(a[1])) : __uint_as_float(a[0]) + __uint_as_float(a[1]);
    const float u = MAX ? fmaxf(__uint_as_float(b[0]), __uint_as_float(b[1])) : __uint_as_float(b[0]) + __uint_as_float(b[1]);
    auto c = __builtin_amdgcn_permlane32_swap(__float_as_uint(t), __float_as_uint(u), false, false);
    return MAX ? fmaxf(__uint_as_float(c[0]), __uint_as_float(c[1])) : __uint_as_float(c[0]) + __uint_as_float(c[1]);
}
DEVI void rows_spread(float w, float (&v)[4]) {
    auto c = __builtin_amdgcn_permlane32_swap(__float_as_uint(w), __float_as_uint(w), false, false);
    auto a = __builtin_amdgcn_permlane16_swap(c[0], c[0], false, false);
    auto b = __builtin_amdgcn_permlane16_swap(c[1], c[1], false, false);
    v[0] = __uint_as_float(a[0]); v[1] = __uint_as_float(a[1]); v[2] = __uint_as_float(b[0]); v[3] = __uint_as_float(b[1]);
}

template <int GRP>
DEVI void g1_body(G1Ld& L, G2St& T, LAS char* lds, char* ws, int rt, int ct, int wm, int kh STAMP_ARGS) {
    G1Ptr P;
    {
        const int lane = fresh_lane(), g = lane >> 4, q = (lane & 15) >> 2, p = lane & 3;
        const unsigned ldsb = (unsigned)(size_t)lds;
        P.pA = ldsb + wm * 4096 + lane * 16;
        P.pH = ldsb + lane * 16;
        const int brow = (16 * (g >> 1) + 4 * (g & 1) + q) * 224 + p * 8;
        P.pB01 = ldsb + G1_BOFF + brow;
        P.pB23 = P.pB01 + 2 * G1_BST;
        P.pBh01 = P.pB01 + (2 + wm) * 32;
        P.pBh23 = P.pB23 + (2 + wm) * 32;
    }
    f32x4 acc[4][10], acch = {0.f, 0.f, 0.f, 0.f};
#pragma unroll
    for (int m = 0; m < 4; ++m)
#pragma unroll
        for (int n = 0; n < 10; ++n) acc[m][n] = (f32x4){0.f, 0.f, 0.f, 0.f};

    if (GRP == 1 && wm == 3) g1_p2<0>(L);
    g1_pa<0>(L);
    if (GRP == 1 && wm == 3) g1_p2<1>(L);
    g1_pa<1>(L);
#pragma unroll
    for (int m = 0; m < 4; ++m)
        asm volatile("" : "+v"(acc[m][0]), "+v"(acc[m][1]), "+v"(acc[m][2]), "+v"(acc[m][3]), "+v"(acc[m][4]), "+v"(acc[m][5]), "+v"(acc[m][6]), "+v"(acc[m][7]), "+v"(acc[m][8]), "+v"(acc[m][9]), "+v"(acch));
    asm volatile("s_waitcnt vmcnt(2)\n\ts_barrier" ::: "memory", FRAG_REGS);
    g1_ldb<GRP, 0, 0, 0>(P); g1_ldb<GRP, 0, 0, 1>(P); g1_ldb<GRP, 0, 1, 0>(P); g1_ldb<GRP, 0, 1, 1>(P); g1_ldb<GRP, 0, 2, 0>(P); g1_ldb<GRP, 0, 2, 1>(P);
    g1_ldb<GRP, 0, 3, 0>(P); g1_ldb<GRP, 0, 3, 1>(P); g1_ldb<GRP, 0, 4, 0>(P); g1_ldb<GRP, 0, 4, 1>(P);
    if (GRP == 0) { g1_ldah<0>(P); g1_ldbh<0, 0>(P); g1_ldbh<0, 1>(P); }
    g1_lda<0, 0>(P); g1_lda<0, 1>(P);
    g1_ldb<GRP, 0, 5, 0>(P); g1_ldb<GRP, 0, 5, 1>(P); g1_ldb<GRP, 0, 6, 0>(P); g1_ldb<GRP, 0, 6, 1>(P); g1_ldb<GRP, 0, 7, 0>(P); g1_ldb<GRP, 0, 7, 1>(P);
    g1_lda<0, 2>(P); g1_lda<0, 3>(P);
    g1_issue<2>(L);
    if (GRP == 1) g1_issue<3>(L);
    STAMP_SEG(0);
#pragma unroll 1
    for (int it = 0; it < 8; ++it) {
        g1_stage<GRP, 0>(acc, acch, L, it == 7, P);
        if (GRP == 0) g1_tail<0>(L, it == 7);
        g1_stage<GRP, 1>(acc, acch, L, it == 7, P);
        g1_stage<GRP, 2>(acc, acch, L, it == 7, P);
        g1_stage<GRP, 3>(acc, acch, L, it == 7, P);
    }
    asm volatile("s_waitcnt vmcnt(0) lgkmcnt(0)\n\ts_nop 15" ::: "memory", FRAG_REGS);
    SBAR();
    STAMP_SEG(0);

    const int lane = fresh_lane(), g = lane >> 4, i16 = lane & 15;
    char* ho = ws + WS_HO;
    const int mb0 = 16 * rt + 4 * wm;
    const unsigned hl = (unsigned)((g & 1) * 1024 + (g >> 1) * 256 + i16 * 16);
    G2W& W = T.W;
    {
        const int wid = GRP * 4 + wm, r = lane >> 3, c = (lane & 7) ^ ((r & 2) | ((wid & 1) << 2));
        W.vw0 = (unsigned)((8 * wid + r) * 2048 + c * 16 + kh * 128);
        W.vw1 = W.vw0 ^ 128;
        W.vw2 = W.vw0 + 448 * 2048;
    }
    char* hs = ho + (size_t)(64 * (ct >> 4) + (ct & 15)) * SLAB_B + (size_t)mb0 * 1024;
#define GW(i) do { if ((i) < 2 * G2_WL) { g2_w<(i) / 2, (i) % 2>(W); SBAR(); } } while (0)
#define GLU1(n, m) glu_store(acc[m][n], acc[m][5 + (n)], acc[(m) + 1][n], acc[(m) + 1][5 + (n)], hs + (size_t)(16 * (1 + (n) / 2)) * SLAB_B + ((n) & 1) * 512 + (m) * 1024 + hl)
    if (GRP == 1) {
        SBAR(); BAR(); MEMFENCE(); SBAR();
        GW(0); GW(1); GW(2); GW(3); GLU1(0, 0); GW(4); GW(5); GLU1(0, 2); GW(6); GW(7); GLU1(1, 0); GW(8); GW(9); GLU1(1, 2); GW(10); GW(11); GLU1(2, 0); GW(12); GW(13); GLU1(2, 2); GW(14); GW(15);
        asm volatile("s_waitcnt vmcnt(%0)" :: "i"(2 * G2_WL + 4) : "memory");
#pragma unroll
        for (int m = 0; m < 4; ++m) { T.dg[0][m] = acc[m][3]; T.dg[1][m] = acc[m][4]; T.du[0][m] = acc[m][8]; T.du[1][m] = acc[m][9]; }
        T.dd[0] = hs + (size_t)32 * SLAB_B + 512; T.dd[1] = hs + (size_t)48 * SLAB_B;
    } else {
        u32x4 Qf[4], Kf[4]; u32x2 Vh[4][2];
#pragma unroll
        for (int m = 0; m < 4; ++m) {
            Qf[m] = (u32x4){pk_bf16(acc[m][0][0], acc[m][0][1]), pk_bf16(acc[m][0][2], acc[m][0][3]), pk_bf16(acc[m][1][0], acc[m][1][1]), pk_bf16(acc[m][1][2], acc[m][1][3])};
            Kf[m] = (u32x4){pk_bf16(acc[m][2][0], acc[m][2][1]), pk_bf16(acc[m][2][2], acc[m][2][3]), pk_bf16(acc[m][3][0], acc[m][3][1]), pk_bf16(acc[m][3][2], acc[m][3][3])};
#pragma unroll
            for (int b = 0; b < 2; ++b) Vh[m][b] = (u32x2){pk_bf16(acc[m][4 + b][0], acc[m][4 + b][1]), pk_bf16(acc[m][4 + b][2], acc[m][4 + b][3])};
        }
        LAS char* xs = lds + G1_XOFF;
        if (wm < 3) {
            *(LAS u32x4*)(xs + (wm + 1) * 2048 + lane * 16) = Kf[3];
            *(LAS u32x4*)(xs + (wm + 1) * 2048 + 1024 + lane * 16) = (u32x4){Vh[3][0][0], Vh[3][0][1], Vh[3][1][0], Vh[3][1][1]};
        }
        {
            const unsigned h0 = pk_bf16(acch[0], acch[1]), h1 = pk_bf16(acch[2], acch[3]);
            if (wm < 2) {
                *(LAS u32x2*)(xs + lane * 16 + wm * 8) = (u32x2){h0, h1};
            } else {
                LAS char* t = xs + 1024 + ((i16 >> 2) * 16 + 4 * g) * 16 + (wm - 2) * 8 + (i16 & 3) * 2;
                *(LAS u16*)(t) = (u16)h0; *(LAS u16*)(t + 16) = (u16)(h0 >> 16); *(LAS u16*)(t + 32) = (u16)h1; *(LAS u16*)(t + 48) = (u16)(h1 >> 16);
            }
        }
        asm volatile("s_waitcnt lgkmcnt(0)" ::: "memory");
        SBAR(); BAR(); MEMFENCE(); SBAR();
        GW(0); GW(1);
        const u32x4 Kp = *(LAS const u32x4*)(xs + wm * 2048 + lane * 16);
        const u32x4 Vp = *(LAS const u32x4*)(xs + wm * 2048 + 1024 + lane * 16);
        const int dc = i16 - 4 * g - (i16 & 1);
        unsigned modd = (i16 & 1) ? ~0u : 0u, mc0 = dc >= 0 ? ~0u : 0u, mc1 = dc >= 2 ? ~0u : 0u;
        asm volatile("" : "+v"(modd), "+v"(mc0), "+v"(mc1));
        const unsigned half = (modd & 0xffff0000u) | (~modd & 0x0000ffffu);
        const unsigned pp0 = half & ~mc0, pp1 = half & ~mc1, pc0 = half & mc0, pc1 = half & mc1;
        const float sc = 0.17677669529663687f * 1.4426950408889634f;
        const f32x4 z = {0.f, 0.f, 0.f, 0.f};
        float e0[4], e1[4], red[4];
#pragma unroll
        for (int m = 0; m < 4; ++m) {
            u32x4 s0 = __builtin_bit_cast(u32x4, MFMA16(as_frag(m ? Kf[m ? m - 1 : 0] : Kp), as_frag(Qf[m]), z));
            const u32x4 s1 = __builtin_bit_cast(u32x4, MFMA16(as_frag(Kf[m]), as_frag(Qf[m]), z));
            if (m == 0 && rt == 0 && wm == 0) { const unsigned c = __float_as_uint(-1e9f / 0.17677669529663687f); s0 = (u32x4){c, c, c, c}; }
            e0[m] = __uint_as_float(bsel(modd, bsel(mc0, s1[1], s0[1]), bsel(mc0, s1[0], s0[0])));
            e1[m] = __uint_as_float(bsel(modd, bsel(mc1, s1[3], s0[3]), bsel(mc1, s1[2], s0[2])));
            red[m] = fmaxf(e0[m], e1[m]);
        }
        {
            rows_spread(rows_reduce<true>(red), red);
        }
#pragma unroll
        for (int m = 0; m < 4; ++m) {
            e0[m] = __builtin_amdgcn_exp2f((e0[m] - red[m]) * sc); e1[m] = __builtin_amdgcn_exp2f((e1[m] - red[m]) * sc);
            red[m] = e0[m] + e1[m];
        }
        {
            float w = __builtin_amdgcn_rcpf(rows_reduce<false>(red));
            rows_spread(w, red);
        }
        char* od = hs + (((g & 1) << 1) | (g >> 1)) * 256 + i16 * 16;
#pragma unroll
        for (int m = 0; m < 4; ++m) {
            const float p0 = e0[m] * red[m], p1 = e1[m] * red[m];
            const unsigned w0 = pk_bf16(p0, p0), w1 = pk_bf16(p1, p1);
            const u32x2 Pp = {w0 & pp0, w1 & pp1}, Pc = {w0 & pc0, w1 & pc1};
            const u32x2 Vp0 = m ? Vh[m ? m - 1 : 0][0] : (u32x2){Vp[0], Vp[1]}, Vp1 = m ? Vh[m ? m - 1 : 0][1] : (u32x2){Vp[2], Vp[3]};
            const f32x4 o0 = MFMA16K16(Vh[m][0], Pc, MFMA16K16(Vp0, Pp, z));
            const f32x4 o1 = MFMA16K16(Vh[m][1], Pc, MFMA16K16(Vp1, Pp, z));
            unsigned x0 = pk_bf16(o0[0], o0[1]), x1 = pk_bf16(o0[2], o0[3]), y0 = pk_bf16(o1[0], o1[1]), y1 = pk_bf16(o1[2], o1[3]);
            auto r0 = __builtin_amdgcn_permlane16_swap(x0, y0, false, false);
            auto r1 = __builtin_amdgcn_permlane16_swap(x1, y1, false, false);
            st16(od + m * 1024, never_ones((u32x4){r0[0], r1[0], r0[1], r1[1]}));
        }
        SBAR();
        GW(2); GW(3); GW(4); GW(5); GW(6); GW(7); GW(8); GW(9); GW(10); GW(11); GW(12); GW(13); GW(14); GW(15);
        asm volatile("s_waitcnt vmcnt(%0)" :: "i"(2 * G2_WL + 2) : "memory");
#pragma unroll
        for (int m = 0; m < 4; ++m) { T.dg[0][m] = acc[m][6]; T.dg[1][m] = acc[m][7]; T.du[0][m] = acc[m][8]; T.du[1][m] = acc[m][9]; }
        T.dd[0] = hs + (size_t)48 * SLAB_B + 512; T.dd[1] = ho + (size_t)(128 + (ct >> 1)) * SLAB_B + (ct & 1) * 512 + (size_t)mb0 * 1024;
        if (blockIdx.x == 0 && wm == 1) {
            flags_wait<POLL_SLEEP>((u32x2){~T.tag, 0u}, ws + WS_LF, (unsigned)(g * 8192 + i16 * 8), T.tag);
            flags_wait<POLL_SLEEP>((u32x2){~T.tag, 0u}, ws + WS_LF + 32768, (unsigned)(g * 8192 + i16 * 8), T.tag);
            flag_set<0>(ws + WS_EP, T.tag);
        }
    }
#undef GW
#undef GLU1
    STAMP_SEG(2);
}

DEVI float sum_x8(float v) {
    v += __uint_as_float(__builtin_amdgcn_update_dpp(0u, __float_as_uint(v), 0x128  , 0xf, 0xf, false));
    { auto t = __builtin_amdgcn_permlane16_swap(__float_as_uint(v), __float_as_uint(v), false, false); v = __uint_as_float(t[0]) + __uint_as_float(t[1]); }
    { auto t = __builtin_amdgcn_permlane32_swap(__float_as_uint(v), __float_as_uint(v), false, false); v = __uint_as_float(t[0]) + __uint_as_float(t[1]); }
    return v;
}

template <int KG>
DEVI void g2_body(G2St& T, LAS char* lds, char* ws, const u16* x, u16* out, int rt, int ct, int wm STAMP_ARGS2) {
    constexpr int kg = KG;
    const int bid = blockIdx.x, idx = bid >> 3, wid = KG * 4 + wm;
    const int kh = (bid >> 2) & 1, side = kh ^ 1, rt2 = 2 * rt + ((bid >> 1) & 1), ct2 = 4 * (bid & 1) + (idx & 3);
    const char* flq = ws + WS_FL + (rt * 4 + (bid & 2)) * 512;
    const int lane2 = fresh_lane(), g = lane2 >> 4, i16 = lane2 & 15, q = i16 >> 2, p = lane2 & 3;
    {
        const char* a0 = ws + WS_HO + (size_t)kg * SLAB_B + (size_t)(8 * rt2 + 2 * wm) * 1024;
        T.pa = a0 + (size_t)(64 * side) * SLAB_B;
        T.pad = a0 + (size_t)128 * SLAB_B;
        T.va0 = lane2 * 16 + kg * 1024; T.va1 = T.va0 ^ 1024;
        T.flagA = ws + WS_FL + (kg ^ 1) * 16384 + (rt * 4 + wm) * 512 + ct * 4;
        T.flagZ = ws + WS_FL + (rt * 4 + wm) * 512 + 256 + kg * 128 + ct * 4;
        T.hl = (unsigned)((g & 1) * 1024 + (g >> 1) * 256 + i16 * 16);
        T.poller = wid == 0;
        T.flags = T.poller ? flq : ws + WS_XB + (size_t)bid * 32768;
        T.foa = (unsigned)((lane2 >> 5) * 512 + side * 64 + (lane2 & 7) * 8);
        T.foz = (unsigned)((lane2 >> 5) * 512 + 256 + (lane2 & 31) * 8);
    }
#pragma unroll
    for (int n = 0; n < 4; ++n) {
        T.pB[0][n] = lds + kg * 4096 + (8 * g + q) * 128 + (((2 * n + (p >> 1)) ^ ((q & 2) | ((g & 1) << 2))) * 16) + (p & 1) * 8;
#pragma unroll
        for (int k = 1; k < (G2_NS + 3) / 4; ++k) { T.pB[k][n] = T.pB[0][n] + k * 4 * G2_ST; asm volatile("" : "+v"(T.pB[k][n])); }
        asm volatile("" : "+v"(T.pB[0][n]));
    }
    MEMFENCE(); SBAR(); BAR(); MEMFENCE(); SBAR();
    g2_a<0, 0>(T); g2_a<0, 1>(T); g2_a<1, 0>(T); g2_a<1, 1>(T);
    SBAR();
#pragma unroll
    for (int n = 0; n < 8; ++n) { T.B[n][0] = g2_ldb<0>(T, n, 0); T.B[n][1] = g2_ldb<0>(T, n, 1); }
    SBAR();
    STAMP_SEG(3);
    g2_run<KG, 0, 8>(T);
    STAMP_SEG(4);
    g2_run<KG, 8, 24>(T);
    const int orow = 128 * rt2 + 32 * wm + 16 * kg + i16, ocol = 128 * ct2 + 64 * kh + (g & 1) * 16 + (g >> 1) * 8;
    T.xw = lds + G2_XOFF + wid * 4096 + lane2 * 16;
    T.xr = lds + G2_XOFF + (wid ^ 4) * 4096 + lane2 * 16;
    T.snd = ws + WS_XB + (size_t)bid * 32768 + wid * 4096 + lane2 * 16;
    T.rcv = ws + WS_XB + (size_t)(bid ^ 4) * 32768 + wid * 4096 + lane2 * 16;
    T.xp = x + (size_t)orow * D_ + ocol;
    g2_run<KG, 24, 36>(T);
    STAMP_SEG(5);

    MFMA_DRAIN(); SBAR();
#pragma unroll
    for (int j = 0; j < 4; ++j) *(LAS f32x4*)(T.xw + j * 1024) = T.acc[1][j];
    WAIT_LGKM0();
    MEMFENCE(); SBAR(); BAR(); MEMFENCE(); SBAR();
#pragma unroll
    for (int j = 0; j < 4; ++j) T.acc[0][j] += *(LAS const f32x4*)(T.xr + j * 1024);
    asm volatile("s_waitcnt vmcnt(0)" : "+v"(T.xres[0]), "+v"(T.xres[1]), "+v"(T.rc[0]), "+v"(T.rc[1]), "+v"(T.rc[2]), "+v"(T.rc[3]) :: "memory");
    for (unsigned spin = 0;; ++spin) {
        const unsigned mx = umax4((u32x4){umax4(T.rc[0]), umax4(T.rc[1]), umax4(T.rc[2]), umax4(T.rc[3])});
        if (!__any(mx == 0xffffffffu) || spin > SPIN_CAP) break;
        __builtin_amdgcn_s_sleep(4);
        asm volatile("global_load_dwordx4 %0, %4, off sc1\n\tglobal_load_dwordx4 %1, %4, off offset:1024 sc1\n\tglobal_load_dwordx4 %2, %4, off offset:2048 sc1\n\tglobal_load_dwordx4 %3, %4, off offset:3072 sc1\n\ts_waitcnt vmcnt(0)"
                     : "=&v"(T.rc[0]), "=&v"(T.rc[1]), "=&v"(T.rc[2]), "=&v"(T.rc[3]) : "v"(T.rcv) : "memory");
    }
    u16* orp = out + (size_t)orow * D_ + ocol;
#pragma unroll
    for (int j = 0; j < 4; j += 2) {
        const f32x4 s0 = T.acc[0][j] + __builtin_bit_cast(f32x4, T.rc[j]), s1 = T.acc[0][j + 1] + __builtin_bit_cast(f32x4, T.rc[j + 1]);
        float v[8];
#pragma unroll
        for (int e = 0; e < 4; ++e) { auto r = __builtin_amdgcn_permlane16_swap(__float_as_uint(s0[e]), __float_as_uint(s1[e]), false, false); v[e] = __uint_as_float(r[0]); v[4 + e] = __uint_as_float(r[1]); }
        const u32x4 xv = T.xres[j >> 1];
        u32x4 o;
#pragma unroll
        for (int e = 0; e < 4; ++e) o[e] = pk_bf16(v[2 * e] + bf_lo(xv[e]), v[2 * e + 1] + bf_hi(xv[e]));
        *(GAS u32x4*)(size_t)(orp + 16 * j) = o;
    }
    STAMP_SEG(6);
    STAMP_OUT(ws, 0, wid, lane2);
    STAMP_OUT(ws, 1, wid, lane2);
}

__global__ __launch_bounds__(512, 2) void k_main(const char* __restrict__ wqkv, const char* __restrict__ wg, const char* __restrict__ wu, const char* __restrict__ wo, const char* __restrict__ wd, char* __restrict__ ws,
                                                const u16* __restrict__ x, u16* __restrict__ out) {
    extern __shared__ __attribute__((aligned(16))) char smem[];
    TL_ENTRY();
    asm volatile("" : "+s"(wqkv), "+s"(wg), "+s"(wu), "+s"(wo), "+s"(wd), "+s"(ws), "+s"(x), "+s"(out));
    LAS char* lds = (LAS char*)smem;
    const int tid = threadIdx.x, lane = tid & 63;
    const int wid = __builtin_amdgcn_readfirstlane(tid >> 6);
    const int bid = blockIdx.x, xcd = bid & 7, idx = bid >> 3;
    const int ct = 4 * xcd + (idx & 3), rt = idx >> 2;
    const unsigned ldsb = (unsigned)(size_t)lds;
    const int r8 = lane & 7, c8 = lane >> 3, row = 256 * rt + 8 * ct + r8;
    u32x4 xv[2];
    unsigned ep;
    asm volatile("global_load_dwordx4 %0, %3, %4\n\tglobal_load_dwordx4 %1, %3, %4 offset:128\n\ts_load_dword %2, %5, 0x0"
                 : "=&v"(xv[0]), "=&v"(xv[1]), "=&s"(ep) : "v"((unsigned)(row * 2048 + (16 * wid + c8) * 16)), "s"(x), "s"(ws + WS_EP) : "memory");
    G1Ld L;
    {
        const int c = wid * 64 + lane, rho = c / 14, cc = c - rho * 14;
        const int k = (rho & ~12) | ((rho & 4) << 1) | ((rho & 8) >> 1);
        const int c2 = cc < 12 ? cc : 11;
        const bool last = wid == 7;
        L.pa = ws + WS_XN + (size_t)(16 * rt + 2 * wid) * 1024;
        L.stepa = (unsigned)SLAB_B;
        L.va = lane * 16;
        L.la = ldsb + (1 + 2 * wid) * 1024;
        L.p2 = last ? ws + WS_XN + (size_t)(rt == 0 ? 0 : 16 * rt - 1) * 1024 : wqkv + ct * 64;
        L.step2 = last ? (unsigned)SLAB_B : 32u * 3072u * 2u;
        L.v2 = last ? (unsigned)((rt == 0 ? (lane & 48) : lane) * 16) : (unsigned)(k * 6144 + (c2 >> 2) * 2048 + (c2 & 3) * 16);
        L.l2 = last ? ldsb : ldsb + G1_BOFF + wid * 1024;
        L.s2 = last ? G1_AST : G1_BST;
        L.pg = wg + ct * 224;
        L.pu = wu + ct * 224;
        L.stepg = 32u * 3584u * 2u;
        L.vg = last ? 0u : (unsigned)(k * 7168 + cc * 16);
        L.lg = last ? ldsb + G1_BOFF + 192 : ldsb + G1_BOFF + G1_REG + wid * 1024;
        L.lu = last ? ldsb + G1_BOFF + 192 : ldsb + G1_BOFF + 2 * G1_REG + wid * 1024;
        L.ma = ~0ull; L.m2 = ~0ull; L.mg = last ? 1ull : ~0ull;
    }
    L.warm = wid < 4 ? wo + bid * 8192 + wid * 2048 : wd + bid * 28672 + (wid & 3) * 7168;
    L.lw = ldsb + G1_XOFF + (wid & 3) * 1024; L.ldum = ldsb + G1_BOFF + 192;
    L.pf = ws + WS_XB + (size_t)bid * 32768 + (wid & 3) * 8192;
    const int kh = (bid >> 2) & 1, side = kh ^ 1, rt2 = 2 * rt + ((bid >> 1) & 1), ct2 = 4 * (bid & 1) + (idx & 3);
    const int kg = wid >> 2, wm = wid & 3;
    G2St T;
    T.W.pwo = wo + (size_t)(side * 512) * 2048 + ct2 * 256;
    T.W.pwh = wd + (size_t)(side * 1792 + kg * 80) * 2048 + ct2 * 256;
    T.W.pwd = wd + (size_t)(96 + (wid >> 1) * 96) * 2048 + ct2 * 256;
    T.W.ldsw = ldsb + wid * 1024;
    STAMP_DECL STAMP_START();
    if (wid < 7) { g1_p2<0>(L); g1_pg<0>(L); g1_pu<0>(L); asm volatile("s_waitcnt vmcnt(3)" ::: "memory"); }
    else asm volatile("s_waitcnt vmcnt(0)" ::: "memory");
    asm volatile("s_waitcnt lgkmcnt(0)" : "+v"(xv[0]), "+v"(xv[1]), "+s"(ep) :: "memory");
    T.tag = ep * 1664525u + 1013904223u;
    P0S(2);
    {
        LAS float* red = (LAS float*)(lds + G1_XOFF);
        f32x2 f[8];
#pragma unroll
        for (int j = 0; j < 8; ++j) f[j] = (f32x2){bf_lo(xv[j >> 2][j & 3]), bf_hi(xv[j >> 2][j & 3])};
        {
            const u32x4 ones = {0xffffffffu, 0xffffffffu, 0xffffffffu, 0xffffffffu};
            asm volatile("global_store_dwordx4 %0, %1, %2 sc1\n\tglobal_store_dwordx4 %0, %1, %2 offset:1024 sc1\n\ts_nop 1"
                         :: "v"(L.va), "v"(ones), "s"(ws + WS_HO + (size_t)(64 * (ct >> 4) + (ct & 15)) * SLAB_B + (size_t)(16 * rt + 2 * wid) * 1024) : "memory");
        }
        const f32x2 s2 = ((f[0] + f[1]) + (f[2] + f[3])) + ((f[4] + f[5]) + (f[6] + f[7]));
        const float mw = sum_x8(s2[0] + s2[1]) * (1.0f / 128);
        f32x2 q2 = {0.f, 0.f};
#pragma unroll
        for (int j = 0; j < 8; ++j) { const f32x2 d = f[j] - mw; q2 = __builtin_elementwise_fma(d, d, q2); }
        const float qw = sum_x8(q2[0] + q2[1]);
        if (c8 == 0) *(LAS f32x2*)(red + r8 * 16 + wid * 2) = (f32x2){mw, qw};
        WAIT_LGKM0(); MEMFENCE(); BAR(); MEMFENCE();
        f32x4 st[4];
#pragma unroll
        for (int k = 0; k < 4; ++k) st[k] = *(LAS const f32x4*)(red + r8 * 16 + k * 4);
        const float mean = (((st[0][0] + st[0][2]) + (st[1][0] + st[1][2])) + ((st[2][0] + st[2][2]) + (st[3][0] + st[3][2]))) * 0.125f;
        float qt = 0.f, dt = 0.f;
#pragma unroll
        for (int k = 0; k < 4; ++k) { const float d0 = st[k][0] - mean, d1 = st[k][2] - mean; qt += st[k][1] + st[k][3]; dt = fmaf(d0, d0, fmaf(d1, d1, dt)); }
        const float mb = rbf(mean);
        const float vb = rbf(fmaf(dt, 128.f, qt) * (1.0f / D_));
        const float den = rbf(__builtin_amdgcn_sqrtf(rbf(vb + 1e-5f)));
        const float r0 = __builtin_amdgcn_rcpf(den), rc = fmaf(fmaf(-den, r0, 1.0f), r0, r0);
#pragma unroll
        for (int i = 0; i < 2; ++i) {
            u32x4 o;
#pragma unroll
            for (int j = 0; j < 4; ++j) {
                const f32x2 c = f[i * 4 + j] - mb;
                const unsigned d = pk_bf16(c[0], c[1]);
                const f32x2 y = (f32x2){bf_lo(d), bf_hi(d)} * rc;
                o[j] = pk_bf16(y[0], y[1]);
            }
            const int ch = 16 * wid + 8 * i + c8;
            asm volatile("global_store_dwordx4 %0, %1, %2 sc1\n\ts_nop 1" :: "v"((unsigned)((ch >> 2) * (int)SLAB_B + (row >> 4) * 1024 + ((ch & 3) * 16 + (row & 15)) * 16)), "v"(o), "s"(ws + WS_XN) : "memory");
        }
        P0S(3);
        asm volatile("s_waitcnt vmcnt(0)\n\ts_barrier" ::: "memory");
        P0S(4);
        if (wid < 7) {
            if (!W_EARLY) { g1_p2<0>(L); g1_pg<0>(L); g1_pu<0>(L); }
            g1_p2<1>(L); g1_pg<1>(L); g1_pu<1>(L);
        } else {
            const bool next = ct >= 30 && rt < 7;
            asm volatile("s_mov_b64 exec, %3\n\tglobal_store_dword %0, %1, %2 sc1\n\ts_mov_b64 exec, -1"
                         :: "v"((unsigned)((rt * 32 + lane) * 256 + (lane < 32 ? ct : ct + 2) * 4)), "v"(T.tag), "s"(ws + WS_LF), "s"(next ? ~0ull : 0xffffffffull) : "memory");
            flags_wait<LN_SLEEP>((u32x2){~T.tag, 0u}, ws + WS_LF + (rt * 32 + ct) * 256, (unsigned)(lane < 16 || rt == 0 ? (lane & 15) * 8 : 128), T.tag);
        }
        MEMFENCE(); BAR(); MEMFENCE();
    }
    STAMP_SEG(1);
    if (wid < 4) { g1_body<0>(L, T, lds, ws, rt, ct, wm, kh STAMP_PASS); g2_body<0>(T, lds, ws, x, out, rt, ct, wm STAMP_PASS2); }
    else { g1_body<1>(L, T, lds, ws, rt, ct, wm, kh STAMP_PASS); g2_body<1>(T, lds, ws, x, out, rt, ct, wm STAMP_PASS2); }
    TL_EXIT(ws, 0);
    TL_EXIT(ws, 1);
    TL_EXIT(ws, 2);

}

extern "C" void kernel_launch(void* const* d_in, const int* in_sizes, int n_in,
                              void* d_out, int out_size, void* d_ws, size_t ws_size,
                              hipStream_t stream) {
    const u16* x = (const u16*)d_in[0];
    const char* wqkv = (const char*)d_in[1];
    const char* wo = (const char*)d_in[2];
    const char* wg = (const char*)d_in[4];
    const char* wu = (const char*)d_in[5];
    const char* wd = (const char*)d_in[6];
    char* ws = (char*)d_ws;
    (void)hipFuncSetAttribute((const void*)k_main, hipFuncAttributeMaxDynamicSharedMemorySize, G1_LDS);
    k_main<<<256, 512, G1_LDS, stream>>>(wqkv, wg, wu, wo, wd, ws, x, (u16*)d_out);
}
```

```cpp
#define STAMPS
#define AB_NOINNER
#define PROBE_I -1
#define PROBE_K 1
#define PROBE_KERN 0
#define PROBE_SEG 0
#define PROBE_DIV 10
#define PROBE_WAVE 4
#include <hip/hip_runtime.h>
#include <hip/hip_bf16.h>
#include <stdint.h>

typedef __bf16 bf16x8 __attribute__((ext_vector_type(8)));
typedef __bf16 bf16x4 __attribute__((ext_vector_type(4)));
typedef __bf16 bf16x2 __attribute__((ext_vector_type(2)));
typedef float f32x4 __attribute__((ext_vector_type(4)));
typedef float f32x2 __attribute__((ext_vector_type(2)));
typedef unsigned u32x4 __attribute__((ext_vector_type(4)));
typedef unsigned u32x2 __attribute__((ext_vector_type(2)));
typedef unsigned short u16;
#define LAS __attribute__((address_space(3)))
#define GAS __attribute__((address_space(1)))
#define DEVI __device__ __forceinline__

constexpr int S_ = 2048, D_ = 1024, F_ = 3584;
constexpr int MB_ = S_ / 16;
constexpr size_t SLAB_B = (size_t)MB_ * 1024;
constexpr size_t WS_XN = 0;
constexpr size_t WS_HO = 32 * SLAB_B;
constexpr size_t WS_XB = WS_HO + 144 * SLAB_B;
constexpr size_t WS_FL = WS_XB + 256 * 32768;
constexpr size_t WS_LF = WS_FL + 32768;
constexpr size_t WS_EP = WS_LF + 65536;

DEVI unsigned pk_bf16(float lo, float hi) {
    bf16x2 v = {(__bf16)lo, (__bf16)hi};
    return __builtin_bit_cast(unsigned, v);
}
DEVI float bf_lo(unsigned u) { return __uint_as_float(u << 16); }
DEVI float bf_hi(unsigned u) { return __uint_as_float(u & 0xffff0000u); }
DEVI float rbf(float f) { return (float)(__bf16)f; }
DEVI bf16x8 as_frag(u32x4 v) { return __builtin_bit_cast(bf16x8, v); }
#define MFMA16(a, b, c) __builtin_amdgcn_mfma_f32_16x16x32_bf16((a), (b), (c), 0, 0, 0)
#define SBAR() __builtin_amdgcn_sched_barrier(0)
#define MFMA_ACC(c, a, b) asm volatile("v_mfma_f32_16x16x32_bf16 %0, %1, %2, %0" : "+v"(c) : "v"(a), "v"(b))
#define MFMA_NEW(c, a, b) asm volatile("v_mfma_f32_16x16x32_bf16 %0, %1, %2, 0" : "=&v"(c) : "v"(a), "v"(b))
#define MFMA_DRAIN() asm volatile("s_nop 15" ::: "memory")
#define BAR() __builtin_amdgcn_s_barrier()

#define STAMP(var) do { SBAR(); asm volatile("s_memtime %0\n\ts_waitcnt lgkmcnt(0)" : "=s"(var) :: "memory"); SBAR(); } while (0)
#define STAMP_DECL unsigned long long st_a, st_b, st_r0, st_r1; unsigned st_sum[8] = {0, 0, 0, 0, 0, 0, 0, 0}; asm volatile("s_memrealtime %0\n\ts_waitcnt lgkmcnt(0)" : "=s"(st_r0) :: "memory");
#define STAMP_ARGS , unsigned long long& st_a, unsigned long long& st_b, unsigned (&st_sum)[8]
#define STAMP_PASS , st_a, st_b, st_sum
#define STAMP_ARGS2 STAMP_ARGS, unsigned long long& st_r0, unsigned long long& st_r1
#define STAMP_PASS2 STAMP_PASS, st_r0, st_r1
#define STAMP_SEG(i) do { STAMP(st_b); st_sum[i] += (unsigned)(st_b - st_a); st_a = st_b; } while (0)
#define STAMP_START() STAMP(st_a)
#define STAMP_SEGI(i)
#define STAMP_OUT(ws, kern, wid, lane) do { asm volatile("s_memrealtime %0\n\ts_waitcnt lgkmcnt(0)" : "=s"(st_r1) :: "memory"); st_sum[7] = (unsigned)(st_r1 - st_r0); if (lane == 0) { unsigned* o_ = (unsigned*)((char*)(ws) + (128u << 20) + (kern) * (1u << 20)) + (blockIdx.x * 8 + (wid)) * 8; for (int i_ = 0; i_ < 8; ++i_) o_[i_] = st_sum[i_]; } } while (0)
#define TL_ENTRY() unsigned long long tl_0; asm volatile("s_memrealtime %0\n\ts_waitcnt lgkmcnt(0)" : "=s"(tl_0) :: "memory");
#define TL_EXIT(ws, kern) do { unsigned long long tl_1; asm volatile("s_waitcnt vmcnt(0)\n\ts_memrealtime %0\n\ts_waitcnt lgkmcnt(0)" : "=s"(tl_1) :: "memory"); \
    if (fresh_lane() == 0) { unsigned long long* o_ = (unsigned long long*)((char*)(ws) + (140u << 20) + (kern) * (1u << 20)) + (blockIdx.x * 8 + wid) * 2; o_[0] = tl_0; o_[1] = tl_1; } } while (0)

#define P0S(i)
DEVI int fresh_lane() { int l; asm volatile("v_mbcnt_lo_u32_b32 %0, -1, 0\n\tv_mbcnt_hi_u32_b32 %0, -1, %0" : "=v"(l)); return l; }

DEVI bf16x8 tr_frag(LAS const char* p, int off0, int off1) {
    bf16x4 lo = __builtin_amdgcn_ds_read_tr16_b64_v4bf16((LAS bf16x4*)(p + off0));
    bf16x4 hi = __builtin_amdgcn_ds_read_tr16_b64_v4bf16((LAS bf16x4*)(p + off1));
    return __builtin_shufflevector(lo, hi, 0, 1, 2, 3, 4, 5, 6, 7);
}

DEVI void glds16(const char* sbase, unsigned voff, unsigned ldsdst) {
    unsigned keep;
    asm volatile("s_mov_b32 %0, m0\n\ts_mov_b32 m0, %3\n\ts_nop 0\n\tglobal_load_lds_dwordx4 %1, %2\n\ts_mov_b32 m0, %0"
                 : "=&s"(keep) : "v"(voff), "s"(sbase), "s"(ldsdst) : "memory");
}

constexpr int G1_AST = 17 * 1024;
constexpr int G1_BST = 21 * 1024;
constexpr int G1_REG = 7168;
constexpr int G1_BOFF = 4 * G1_AST;
constexpr int G1_XOFF = G1_BOFF + 4 * G1_BST;
constexpr int G1_LDS = G1_XOFF + 8192;

typedef unsigned long long u64;
#define FRAG_REGS "v192", "v193", "v194", "v195", "v196", "v197", "v198", "v199", "v200", "v201", "v202", "v203", "v204", "v205", "v206", "v207", "v208", "v209", "v210", "v211", "v212", "v213", "v214", "v215", "v216", "v217", "v218", "v219", "v220", "v221", "v222", "v223", "v224", "v225", "v226", "v227", "v228", "v229", "v230", "v231", "v232", "v233", "v234", "v235", "v236", "v237", "v238", "v239", "v240", "v241", "v242", "v243", "v244", "v245", "v246", "v247", "v248", "v249", "v250", "v251", "v252", "v253", "v254", "v255"
constexpr int FA = 192, FB = 208, FAH = 248, FBH = 252;
struct G1Ld {
    const char *pa, *p2, *pg, *pu;
    unsigned stepa, step2, stepg;
    unsigned va, v2, vg;
    unsigned la, l2, s2, lg, lu;
    const char* warm; unsigned lw, ldum;
    char* pf;
    u64 ma, m2, mg;
};

#define WAIT_LGKM0() __builtin_amdgcn_s_waitcnt(0xC07F)
#define MEMFENCE() asm volatile("" ::: "memory")
#define OPAQUE_S(x) asm volatile("" : "+s"(x))

template <int LOFF, int KIND>
DEVI void g1_piece(const char* base, unsigned voff, unsigned lds, u64 msk) {
    if (KIND == 0) asm volatile("s_add_u32 m0, %2, %4\n\ts_mov_b64 exec, %3\n\tglobal_load_lds_dwordx4 %0, %1\n\ts_mov_b64 exec, -1" :: "v"(voff), "s"(base), "s"(lds), "s"(msk), "i"(LOFF) : "memory", "scc");
    if (KIND == 1) asm volatile("s_add_u32 m0, %2, %4\n\ts_mov_b64 exec, %3\n\tglobal_load_lds_dwordx4 %0, %1 sc1\n\ts_mov_b64 exec, -1" :: "v"(voff), "s"(base), "s"(lds), "s"(msk), "i"(LOFF) : "memory", "scc");
    if (KIND == 2) asm volatile("s_add_u32 m0, %2, %4\n\ts_mov_b64 exec, %3\n\tglobal_load_lds_dwordx4 %0, %1 sc1\n\tglobal_load_lds_dwordx4 %0, %1 offset:1024 sc1\n\ts_mov_b64 exec, -1"
                                :: "v"(voff), "s"(base), "s"(lds), "s"(msk), "i"(LOFF) : "memory", "scc");
}
template <int SLOT> DEVI void g1_pa(G1Ld& L) { g1_piece<SLOT * G1_AST, 2>(L.pa, L.va, L.la, L.ma); L.pa += L.stepa; OPAQUE_S(L.pa); }
template <int SLOT> DEVI void g1_p2(G1Ld& L) { g1_piece<0, 1>(L.p2, L.v2, L.l2 + SLOT * L.s2, L.m2); L.p2 += L.step2; OPAQUE_S(L.p2); }
template <int SLOT> DEVI void g1_pg(G1Ld& L) { g1_piece<SLOT * G1_BST, 0>(L.pg, L.vg, L.lg, L.mg); L.pg += L.stepg; OPAQUE_S(L.pg); }
template <int SLOT> DEVI void g1_pu(G1Ld& L) { g1_piece<SLOT * G1_BST, 0>(L.pu, L.vg, L.lu, L.mg); L.pu += L.stepg; OPAQUE_S(L.pu); }
template <int SLOT> DEVI void g1_issue(G1Ld& L) { g1_pa<SLOT>(L); g1_p2<SLOT>(L); g1_pg<SLOT>(L); g1_pu<SLOT>(L); }
template <int GRP> DEVI void g1_tail(G1Ld& L, bool tail) {
    L.p2 = tail ? L.p2 - L.step2 : L.p2; L.pg = tail ? L.pg - L.stepg : L.pg; L.pu = tail ? L.pu - L.stepg : L.pu;
    L.step2 = tail ? 0u : L.step2; L.stepg = tail ? 0u : L.stepg;
    L.m2 = tail ? 1ull : L.m2; L.mg = tail ? 1ull : L.mg;
    if (GRP == 0) L.pa = tail ? L.warm : L.pa;
    else { L.pa = tail ? L.pa - L.stepa : L.pa; L.ma = tail ? 1ull : L.ma; L.warm = tail ? L.warm - 1024 : L.warm; L.lw = tail ? L.ldum : L.lw; }
    L.stepa = tail ? 0u : L.stepa;
}

struct G1Ptr { unsigned pA, pH, pB01, pB23, pBh01, pBh23; };

template <int REG, int OFF> DEVI void ds_tr(unsigned base) { asm volatile("ds_read_b64_tr_b16 v[%c1:%c2], %0 offset:%c3" :: "v"(base), "i"(REG), "i"(REG + 1), "i"(OFF) : "memory"); }
template <int REG, int OFF> DEVI void ds_128(unsigned base) { asm volatile("ds_read_b128 v[%c1:%c2], %0 offset:%c3" :: "v"(base), "i"(REG), "i"(REG + 3), "i"(OFF) : "memory"); }
template <int RA_, int RB_> DEVI void mfma_fr(f32x4& c) { asm volatile("v_mfma_f32_16x16x32_bf16 %0, v[%c1:%c2], v[%c3:%c4], %0" : "+v"(c) : "i"(RA_), "i"(RA_ + 3), "i"(RB_), "i"(RB_ + 3)); }
template <int GRP, int SL, int n, int h> DEVI void g1_ldb(const G1Ptr& P) {
    constexpr int off = (SL & 1) * G1_BST + (GRP == 0 ? (n < 6 ? n * 32 : (n < 8 ? G1_REG + (n - 1) * 32 : 2 * G1_REG + (n - 3) * 32))
                                                      : (n < 5 ? G1_REG + n * 32 : 2 * G1_REG + (n - 5) * 32));
    ds_tr<FB + 4 * n + 2 * h, off + h * 8 * 224>((SL < 2) ? P.pB01 : P.pB23);
}
template <int SL, int m> DEVI void g1_lda(const G1Ptr& P) { ds_128<FA + 4 * m, SL * G1_AST + (1 + m) * 1024>(P.pA); }
template <int SL> DEVI void g1_ldah(const G1Ptr& P) { ds_128<FAH, SL * G1_AST>(P.pH); }
template <int SL, int h> DEVI void g1_ldbh(const G1Ptr& P) { ds_tr<FBH + 2 * h, (SL & 1) * G1_BST + h * 8 * 224>((SL < 2) ? P.pBh01 : P.pBh23); }

template <int GRP, int ST>
DEVI void g1_stage(f32x4 (&acc)[4][10], f32x4& acch, G1Ld& L, bool tail, const G1Ptr& P) {
    constexpr int NX = (ST + 1) & 3, IS = (ST + 3) & 3;
#define MM(m, n) do { if (GRP == 0 && ((n) == 4 || (n) == 5)) mfma_fr<FA + 4 * (m), FB + 4 * (n)>(acc[m][n]); else mfma_fr<FB + 4 * (n), FA + 4 * (m)>(acc[m][n]); } while (0)
#define RB(n, h) g1_ldb<GRP, NX, n, h>(P)
#define RC(n, h) g1_ldb<GRP, ST, n, h>(P)
#define RA(m) g1_lda<NX, m>(P)
#define G0(x) do { if (GRP == 0) { x; } } while (0)
    asm volatile("s_waitcnt lgkmcnt(9)" ::: "memory");
    MM(0, 0); RC(8, 0);
    asm volatile("s_waitcnt lgkmcnt(9)" ::: "memory");
    MM(1, 0); RC(8, 1);
    MM(1, 1); RC(9, 0); MM(0, 1); RC(9, 1);
    MM(0, 2); G0(g1_pa<IS>(L)); MM(1, 2);
    MM(1, 3); G0(g1_p2<IS>(L)); MM(0, 3); G0(g1_pg<IS>(L));
    MM(0, 4); G0(g1_pu<IS>(L)); MM(1, 4);
    G0((mfma_fr<FBH, FAH>(acch)));
    asm volatile("s_waitcnt vmcnt(10) lgkmcnt(0)\n\ts_barrier" ::: "memory", FRAG_REGS);
    if (GRP == 1) {
        if (ST == 0) g1_tail<1>(L, tail);
        g1_issue<ST>(L);
        if (ST == 1) { g1_piece<0, 0>(L.warm, L.va, L.lw, L.ma); L.warm += 1024; OPAQUE_S(L.warm); }
        if (ST == 2) {
            const u32x4 ones = {0xffffffffu, 0xffffffffu, 0xffffffffu, 0xffffffffu};
            asm volatile("global_store_dwordx4 %0, %1, %2 sc1\n\ts_nop 1" :: "v"(L.va), "v"(ones), "s"(L.pf) : "memory", FRAG_REGS);
            L.pf += 1024; OPAQUE_S(L.pf);
        }
    }
    MM(2, 0); MM(3, 0); RB(0, 0);
    MM(3, 1); RB(0, 1); MM(2, 1); RB(1, 0);
    MM(2, 2); RB(1, 1); MM(3, 2); RB(2, 0);
    MM(3, 3); RB(2, 1); MM(2, 3); RB(3, 0);
    MM(2, 4); RB(3, 1); MM(3, 4); RB(4, 0);
    MM(0, 5); RB(4, 1); MM(1, 5); G0(g1_ldah<NX>(P));
    MM(1, 6); G0((g1_ldbh<NX, 0>(P))); MM(0, 6); G0((g1_ldbh<NX, 1>(P)));
    MM(0, 7); MM(1, 7);
    MM(1, 8); MM(0, 8);
    MM(0, 9); RA(0); MM(1, 9); RA(1);
    MM(2, 5); MM(3, 5); RB(5, 0);
    MM(3, 6); RB(5, 1); MM(2, 6); RB(6, 0);
    MM(2, 7); RB(6, 1); MM(3, 7); RB(7, 0);
    MM(3, 8); RB(7, 1); MM(2, 8);
    MM(2, 9); RA(2); MM(3, 9); RA(3);
#undef MM
#undef RB
#undef RC
#undef RA
#undef G0
}

DEVI void st16(char* dst, u32x4 v) {
    asm volatile("global_store_dwordx4 %0, %1, off sc1\n\ts_nop 1" :: "v"(dst), "v"(v) : "memory");
}

DEVI unsigned glu2(float g0, float g1, float u0, float u1) {
    const f32x2 t = (f32x2){g0, g1} * -1.4426950408889634f;
    const f32x2 d = (f32x2){__builtin_amdgcn_exp2f(t[0]), __builtin_amdgcn_exp2f(t[1])} + 1.0f;
    const f32x2 h = (f32x2){u0, u1} * (f32x2){__builtin_amdgcn_rcpf(d[0]), __builtin_amdgcn_rcpf(d[1])};
    return pk_bf16(h[0], h[1]);
}

DEVI void glu_store(const f32x4& g0, const f32x4& u0, const f32x4& g1, const f32x4& u1, char* dst) {
    unsigned a0 = glu2(g0[0], g0[1], u0[0], u0[1]), a1 = glu2(g0[2], g0[3], u0[2], u0[3]);
    unsigned b0 = glu2(g1[0], g1[1], u1[0], u1[1]), b1 = glu2(g1[2], g1[3], u1[2], u1[3]);
    auto r0 = __builtin_amdgcn_permlane16_swap(a0, b0, false, false);
    auto r1 = __builtin_amdgcn_permlane16_swap(a1, b1, false, false);
    st16(dst, (u32x4){r0[0], r1[0], r0[1], r1[1]});
    SBAR();
}

#define W_EARLY 1
#define LN_SLEEP 2
#define POLL_SLEEP 8
#define G2_RCV 34
#define G2_PLACE 1
#define G2_NS 8
constexpr int G2_ST = 16384;
constexpr int G2_WL = G2_NS - 1;
constexpr int G2_XOFF = G2_NS <= 8 ? G2_NS * G2_ST : 0;
constexpr int G2_F = 32, G2_XST = 33;
constexpr int G2_FY = 8, G2_FZ = 12;
constexpr int G2_PA = 1, G2_PY = 9, G2_PZ = 15;
constexpr unsigned SPIN_CAP = 1u << 17;
static_assert(G2_RCV > G2_F && G2_RCV <= 35 && G2_NS >= 6 && G2_NS <= 9, "");
constexpr int g2_grp(int t) { return t < G2_F ? t : G2_F + 2 * (t - G2_F); }
constexpr int g2_afirst(int s) { return s < 2 ? 2 + 2 * s : s + 4 <= 35 ? g2_grp(s + 4) : 40; }
constexpr int g2_alast(int s) { return g2_afirst(s + 1); }
constexpr int g2_tops(int s) { return s == 0 || s == G2_FY || s == G2_FZ ? 1 : 0; }
constexpr int g2_wops(int s) { return s + G2_WL <= 35 ? 2 : 0; }
constexpr int g2_xops(int s) { return (s < 8 && (s & 1) ? 1 : 0) + (s == G2_PA || s == G2_PY || s == G2_PZ ? 1 : 0) + (s == G2_F ? 4 : 0) + (s == G2_XST ? 2 : 0) + (s == G2_RCV ? 4 : 0); }
constexpr int g2_wait(int t) {
    const int need = g2_grp(t + 1) - 1;
    int n = 4, last = need < 2 ? 2 * need + 2 : 0;
    for (int s = 0; s < t; ++s) {
        n += g2_tops(s) + g2_wops(s);
        if (s + G2_WL == t + 1 && n > last) last = n;
        for (int g = g2_afirst(s); g < g2_alast(s); ++g) { n += 2; if (g == need && n > last) last = n; }
        n += g2_xops(s);
        if ((t == 4 && s == G2_PA) || (t == 12 && s == G2_PY) || (t == 20 && s == G2_PZ) || (t == G2_FY && s == 3) || (t == G2_FZ && s == 7)) last = n > last ? n : last;
    }
    return n - last;
}

struct G2W {
    const char *pwo, *pwh, *pwd;
    unsigned vw0, vw1, vw2, ldsw;
};
struct G2St {
    G2W W;
    const char *pa, *pad;
    unsigned va0, va1;
    LAS const char* pB[(G2_NS + 3) / 4][4];
    LAS char* xw; LAS const char* xr;
    char* snd; const char* rcv; const u16* xp;
    f32x4 dg[2][4], du[2][4]; char* dd[2]; unsigned hl, hq[4];
    char *flagA, *flagZ; const char* flags; unsigned foa, foz, tag; u32x2 hf; bool poller;
    u32x4 A[40][2];
    bf16x4 B[8][2];
    f32x4 acc[2][8];
    u32x4 xres[2], rc[4];
};
template <int LOFF> DEVI void g2_glds(const char* base, unsigned voff, unsigned lds) {
    asm volatile("s_add_u32 m0, %2, %3\n\ts_nop 0\n\tglobal_load_lds_dwordx4 %0, %1" :: "v"(voff), "s"(base), "s"(lds), "i"(LOFF) : "memory", "scc");
}
template <int ST, int I> DEVI void g2_w(const G2W& W) {
    constexpr int SLOT = ST % G2_NS;
    const char* src = ST < 8 ? W.pwo + ST * 64 * 2048 : ST < G2_F ? W.pwh + (((ST - 8) & 7) * 224 + ((ST - 8) >> 3) * 32) * 2048 : W.pwd + (ST - G2_F) * 896 * 2048;
    g2_glds<SLOT * G2_ST + I * 8192>(src, I == 0 ? W.vw0 : ST < G2_F ? W.vw1 : W.vw2, W.ldsw);
}
template <int G> DEVI const char* g2_asrc(const G2St& T) {
    return G < G2_F ? T.pa + (size_t)G * 2 * SLAB_B : T.pad + (size_t)(G - G2_F) * 2 * SLAB_B; }
template <int G, int I> DEVI void g2_a(G2St& T) {
    asm volatile("global_load_dwordx4 %0, %1, %2 sc1" : "=v"(T.A[G][I]) : "v"(I ? T.va1 : T.va0), "s"(g2_asrc<G>(T)) : "memory");
}
template <int ST> DEVI bf16x4 g2_ldb(G2St& T, int n, int h) {
    constexpr int SLOT = ST % G2_NS;
    return __builtin_amdgcn_ds_read_tr16_b64_v4bf16((LAS bf16x4*)(T.pB[SLOT / 4][n & 3] + (SLOT % 4) * G2_ST + (n >> 2) * 8192 + h * 512));
}
DEVI bf16x8 cat(bf16x4 lo, bf16x4 hi) { return __builtin_shufflevector(lo, hi, 0, 1, 2, 3, 4, 5, 6, 7); }
template <int OFF> DEVI void flag_set(char* p, unsigned tag) { const unsigned zero = 0u; asm volatile("s_mov_b64 exec, 1\n\tglobal_store_dword %0, %1, %2 offset:%3 sc1\n\ts_mov_b64 exec, -1" :: "v"(zero), "v"(tag), "s"(p), "i"(OFF) : "memory"); }
template <int SLEEP> DEVI void flags_wait(u32x2 f, const char* base, unsigned voff, unsigned tag) {
#pragma unroll 1
    for (unsigned spin = 0; __any(f[0] != tag || f[1] != tag) && spin < SPIN_CAP; ++spin) {
        if (spin) __builtin_amdgcn_s_sleep(SLEEP);
        asm volatile("global_load_dwordx2 %0, %1, %2 sc1\n\ts_waitcnt vmcnt(0)" : "=&v"(f) : "v"(voff), "s"(base) : "memory");
    }
}
DEVI unsigned umax4(u32x4 v) { const unsigned a = v[0] > v[1] ? v[0] : v[1], b = v[2] > v[3] ? v[2] : v[3]; return a > b ? a : b; }
DEVI u32x4 never_ones(u32x4 w) {
#pragma unroll
    for (int e = 0; e < 4; ++e) w[e] = w[e] < 0xfffffffeu ? w[e] : 0xfffffffeu;
    return w;
}

template <int KG, int t>
DEVI void g2_stage(G2St& T) {
    constexpr bool TB = t >= G2_F, ISW = g2_wops(t) != 0, RL = t < 35, CHK = t == 4 || t == 12 || t == 20;
    constexpr int G0 = g2_grp(t), G1 = TB ? G0 + 1 : G0, NA = g2_alast(t) - g2_afirst(t), GN = NA ? g2_afirst(t) : 0, GN1 = NA == 2 ? GN + 1 : GN;
    u32x4 (&Aa)[2] = T.A[G0]; u32x4 (&Ab)[2] = T.A[G1];
    if (TB) asm volatile("s_waitcnt vmcnt(%4)" : "+v"(Aa[0]), "+v"(Aa[1]), "+v"(Ab[0]), "+v"(Ab[1]) : "i"(g2_wait(t)) : "memory");
    else if (CHK) asm volatile("s_waitcnt vmcnt(%3)" : "+v"(Aa[0]), "+v"(Aa[1]), "+v"(T.hf) : "i"(g2_wait(t)) : "memory");
    else asm volatile("s_waitcnt vmcnt(%2)" : "+v"(Aa[0]), "+v"(Aa[1]) : "i"(g2_wait(t)) : "memory");
    if (t == 0) flag_set<0>(T.flagA, T.tag);
    if (t == G2_FY) flag_set<128>(T.flagA, T.tag);
    if (t == G2_FZ) flag_set<0>(T.flagZ, T.tag);
    if (t < 8) {
#pragma unroll 1
        for (unsigned spin = 0; spin < SPIN_CAP; ++spin) {
            const unsigned a = umax4(Aa[0]), b = umax4(Aa[1]);
            if (!__any((a > b ? a : b) == 0xffffffffu)) break;
            __builtin_amdgcn_s_sleep(POLL_SLEEP);
            asm volatile("global_load_dwordx4 %0, %2, %4 sc1\n\tglobal_load_dwordx4 %1, %3, %4 sc1\n\ts_waitcnt vmcnt(0)" : "=&v"(Aa[0]), "=&v"(Aa[1]) : "v"(T.va0), "v"(T.va1), "s"(g2_asrc<G0>(T)) : "memory");
        }
    }
    if (KG == 0 && CHK && T.poller) flags_wait<POLL_SLEEP>(T.hf, t == 12 ? T.flags + 128 : T.flags, t == 20 ? T.foz : T.foa, T.tag);
    if (t == G2_F) WAIT_LGKM0();
    { MEMFENCE(); SBAR(); BAR(); MEMFENCE(); SBAR(); }
    f32x4 ho[4];
    if (t == G2_F) {
#pragma unroll
        for (int j = 0; j < 4; ++j) ho[j] = *(LAS const f32x4*)(T.xr + j * 1024);
        SBAR();
    }
#define AB2DS true
#define RL2(n, h) do { if (AB2DS && RL && (n) >= 0) { T.B[(n) >= 0 ? (n) : 0][h] = g2_ldb<t + 1>(T, (n) >= 0 ? (n) : 0, h); SBAR(); } } while (0)
#define P2(n) do { constexpr int c_ = TB ? ((n) & 3) : (n); u32x4 (&A_)[2] = (n) < 4 ? Aa : Ab; const bf16x8 b_ = cat(T.B[n][0], T.B[n][1]); \
                   if (t == 0) MFMA_NEW(T.acc[(n) & 1][c_], b_, A_[(n) & 1]); else MFMA_ACC(T.acc[(n) & 1][c_], b_, A_[(n) & 1]); SBAR(); RL2((n) - 1, 1); D((n) * 2); \
                   if (t == 0) MFMA_NEW(T.acc[~(n) & 1][c_], b_, A_[~(n) & 1]); else MFMA_ACC(T.acc[~(n) & 1][c_], b_, A_[~(n) & 1]); SBAR(); RL2(n, 0); D((n) * 2 + 1); } while (0)
#define VW(i) do { if (ISW) { g2_w<ISW ? t + G2_WL : 0, i>(T.W); SBAR(); } } while (0)
#define VA(i) do { if (NA == 2) { g2_a<(i) < 2 ? GN : GN1, (i) & 1>(T); SBAR(); } else if (NA && ((i) & 1)) { g2_a<GN, (i) / 2>(T); SBAR(); } } while (0)
    f32x4& dg = T.dg[(t >> 2) & 1][t & 3]; f32x4& du = T.du[(t >> 2) & 1][t & 3];
#define D(i) do { if (t < 8 && (i) < 13) { asm volatile("" : "+v"(dg)); \
        if ((i) == 0) dg = dg * -1.4426950408889634f; \
        else if ((i) < 5) dg[((i) - 1) & 3] = __builtin_amdgcn_exp2f(dg[((i) - 1) & 3]); \
        else if ((i) == 5) dg = dg + 1.0f; \
        else if ((i) < 10) dg[((i) - 6) & 3] = __builtin_amdgcn_rcpf(dg[((i) - 6) & 3]); \
        else if ((i) == 10) dg = dg * du; \
        else { T.hq[(t & 1) * 2 + (((i) - 11) & 1)] = pk_bf16(dg[(((i) - 11) & 1) * 2], dg[(((i) - 11) & 1) * 2 + 1]); asm volatile("" : "+v"(T.hq[(t & 1) * 2 + (((i) - 11) & 1)])); } \
        if ((i) < 11) asm volatile("" : "+v"(dg)); \
        SBAR(); } } while (0)
    if (KG == 1) { VW(0); VW(1); VA(0); VA(1); VA(2); VA(3); P2(0); P2(1); P2(2); P2(3); P2(4); P2(5); P2(6); P2(7); }
    else { P2(0); P2(1); P2(2); P2(3); VW(0); P2(4); VW(1); P2(5); VA(0); VA(1); P2(6); VA(2); VA(3); P2(7); }
    RL2(7, 1);
#undef RL2
#undef VW
#undef VA
#undef D
#undef P2
    if (t < 8 && (t & 1)) {
        auto r0 = __builtin_amdgcn_permlane16_swap(T.hq[0], T.hq[2], false, false);
        auto r1 = __builtin_amdgcn_permlane16_swap(T.hq[1], T.hq[3], false, false);
        const u32x4 w = {r0[0], r1[0], r0[1], r1[1]};
        asm volatile("global_store_dwordx4 %0, %1, %2 offset:%3 sc1\n\ts_nop 1" :: "v"(T.hl), "v"(w), "s"(T.dd[(t >> 2) & 1]), "i"((t & 2) * 1024) : "memory");
        SBAR();
    }
    if (t == G2_PA || t == G2_PY || t == G2_PZ) {
        asm volatile("global_load_dwordx2 %0, %1, %2 offset:%3 sc1" : "=v"(T.hf) : "v"(t == G2_PZ ? T.foz : T.foa), "s"(T.flags), "i"(t == G2_PY ? 128 : 0) : "memory"); SBAR();
    }
    if (t == G2_F - 1) {
        MFMA_DRAIN(); SBAR();
#pragma unroll
        for (int j = 0; j < 4; ++j) *(LAS f32x4*)(T.xw + j * 1024) = T.acc[1][4 + j];
        SBAR();
    }
    if (t == G2_F) {
#pragma unroll
        for (int j = 0; j < 4; ++j) {
            const u32x4 w = never_ones(__builtin_bit_cast(u32x4, T.acc[0][4 + j] + ho[j]));
            asm volatile("global_store_dwordx4 %0, %1, off offset:%2 sc1\n\ts_nop 1" :: "v"(T.snd), "v"(w), "i"(j * 1024) : "memory");
        }
        SBAR();
    }
    if (t == G2_XST) { asm volatile("global_load_dwordx4 %0, %2, off\n\tglobal_load_dwordx4 %1, %2, off offset:64" : "=&v"(T.xres[0]), "=&v"(T.xres[1]) : "v"(T.xp) : "memory"); SBAR(); }
    if (t == G2_RCV) {
        asm volatile("global_load_dwordx4 %0, %4, off sc1\n\tglobal_load_dwordx4 %1, %4, off offset:1024 sc1\n\tglobal_load_dwordx4 %2, %4, off offset:2048 sc1\n\tglobal_load_dwordx4 %3, %4, off offset:3072 sc1"
                     : "=&v"(T.rc[0]), "=&v"(T.rc[1]), "=&v"(T.rc[2]), "=&v"(T.rc[3]) : "v"(T.rcv) : "memory");
        SBAR();
    }
}
template <int KG, int t, int E> DEVI void g2_run(G2St& T) { g2_stage<KG, t>(T); if constexpr (t + 1 < E) g2_run<KG, t + 1, E>(T); }

DEVI unsigned bsel(unsigned m, unsigned a, unsigned b) { return (m & a) | (~m & b); }
typedef short s16x4 __attribute__((ext_vector_type(4)));
#define MFMA16K16(a, b, c) __builtin_amdgcn_mfma_f32_16x16x16bf16_1k(__builtin_bit_cast(s16x4, a), __builtin_bit_cast(s16x4, b), (c), 0, 0, 0)
template <bool MAX> DEVI float rows_reduce(const float (&v)[4]) {
    auto a = __builtin_amdgcn_permlane16_swap(__float_as_uint(v[0]), __float_as_uint(v[1]), false, false);
    auto b = __builtin_amdgcn_permlane16_swap(__float_as_uint(v[2]), __float_as_uint(v[3]), false, false);
    const float t = MAX ? fmaxf(__uint_as_float(a[0]), __uint_as_float(a[1])) : __uint_as_float(a[0]) + __uint_as_float(a[1]);
    const float u = MAX ? fmaxf(__uint_as_float(b[0]), __uint_as_float(b[1])) : __uint_as_float(b[0]) + __uint_as_float(b[1]);
    auto c = __builtin_amdgcn_permlane32_swap(__float_as_uint(t), __float_as_uint(u), false, false);
    return MAX ? fmaxf(__uint_as_float(c[0]), __uint_as_float(c[1])) : __uint_as_float(c[0]) + __uint_as_float(c[1]);
}
DEVI void rows_spread(float w, float (&v)[4]) {
    auto c = __builtin_amdgcn_permlane32_swap(__float_as_uint(w), __float_as_uint(w), false, false);
    auto a = __builtin_amdgcn_permlane16_swap(c[0], c[0], false, false);
    auto b = __builtin_amdgcn_permlane16_swap(c[1], c[1], false, false);
    v[0] = __uint_as_float(a[0]); v[1] = __uint_as_float(a[1]); v[2] = __uint_as_float(b[0]); v[3] = __uint_as_float(b[1]);
}

template <int GRP>
DEVI void g1_body(G1Ld& L, G2St& T, LAS char* lds, char* ws, int rt, int ct, int wm, int kh STAMP_ARGS) {
    G1Ptr P;
    {
        const int lane = fresh_lane(), g = lane >> 4, q = (lane & 15) >> 2, p = lane & 3;
        const unsigned ldsb = (unsigned)(size_t)lds;
        P.pA = ldsb + wm * 4096 + lane * 16;
        P.pH = ldsb + lane * 16;
        const int brow = (16 * (g >> 1) + 4 * (g & 1) + q) * 224 + p * 8;
        P.pB01 = ldsb + G1_BOFF + brow;
        P.pB23 = P.pB01 + 2 * G1_BST;
        P.pBh01 = P.pB01 + (2 + wm) * 32;
        P.pBh23 = P.pB23 + (2 + wm) * 32;
    }
    f32x4 acc[4][10], acch = {0.f, 0.f, 0.f, 0.f};
#pragma unroll
    for (int m = 0; m < 4; ++m)
#pragma unroll
        for (int n = 0; n < 10; ++n) acc[m][n] = (f32x4){0.f, 0.f, 0.f, 0.f};

    if (GRP == 1 && wm == 3) g1_p2<0>(L);
    g1_pa<0>(L);
    if (GRP == 1 && wm == 3) g1_p2<1>(L);
    g1_pa<1>(L);
#pragma unroll
    for (int m = 0; m < 4; ++m)
        asm volatile("" : "+v"(acc[m][0]), "+v"(acc[m][1]), "+v"(acc[m][2]), "+v"(acc[m][3]), "+v"(acc[m][4]), "+v"(acc[m][5]), "+v"(acc[m][6]), "+v"(acc[m][7]), "+v"(acc[m][8]), "+v"(acc[m][9]), "+v"(acch));
    asm volatile("s_waitcnt vmcnt(2)\n\ts_barrier" ::: "memory", FRAG_REGS);
    g1_ldb<GRP, 0, 0, 0>(P); g1_ldb<GRP, 0, 0, 1>(P); g1_ldb<GRP, 0, 1, 0>(P); g1_ldb<GRP, 0, 1, 1>(P); g1_ldb<GRP, 0, 2, 0>(P); g1_ldb<GRP, 0, 2, 1>(P);
    g1_ldb<GRP, 0, 3, 0>(P); g1_ldb<GRP, 0, 3, 1>(P); g1_ldb<GRP, 0, 4, 0>(P); g1_ldb<GRP, 0, 4, 1>(P);
    if (GRP == 0) { g1_ldah<0>(P); g1_ldbh<0, 0>(P); g1_ldbh<0, 1>(P); }
    g1_lda<0, 0>(P); g1_lda<0, 1>(P);
    g1_ldb<GRP, 0, 5, 0>(P); g1_ldb<GRP, 0, 5, 1>(P); g1_ldb<GRP, 0, 6, 0>(P); g1_ldb<GRP, 0, 6, 1>(P); g1_ldb<GRP, 0, 7, 0>(P); g1_ldb<GRP, 0, 7, 1>(P);
    g1_lda<0, 2>(P); g1_lda<0, 3>(P);
    g1_issue<2>(L);
    if (GRP == 1) g1_issue<3>(L);
    STAMP_SEG(0);
#pragma unroll 1
    for (int it = 0; it < 8; ++it) {
        g1_stage<GRP, 0>(acc, acch, L, it == 7, P);
        if (GRP == 0) g1_tail<0>(L, it == 7);
        g1_stage<GRP, 1>(acc, acch, L, it == 7, P);
        g1_stage<GRP, 2>(acc, acch, L, it == 7, P);
        g1_stage<GRP, 3>(acc, acch, L, it == 7, P);
    }
    asm volatile("s_waitcnt vmcnt(0) lgkmcnt(0)\n\ts_nop 15" ::: "memory", FRAG_REGS);
    SBAR();
    STAMP_SEG(0);

    const int lane = fresh_lane(), g = lane >> 4, i16 = lane & 15;
    char* ho = ws + WS_HO;
    const int mb0 = 16 * rt + 4 * wm;
    const unsigned hl = (unsigned)((g & 1) * 1024 + (g >> 1) * 256 + i16 * 16);
    G2W& W = T.W;
    {
        const int wid = GRP * 4 + wm, r = lane >> 3, c = (lane & 7) ^ ((r & 2) | ((wid & 1) << 2));
        W.vw0 = (unsigned)((8 * wid + r) * 2048 + c * 16 + kh * 128);
        W.vw1 = W.vw0 ^ 128;
        W.vw2 = W.vw0 + 448 * 2048;
    }
    char* hs = ho + (size_t)(64 * (ct >> 4) + (ct & 15)) * SLAB_B + (size_t)mb0 * 1024;
#define GW(i) do { if ((i) < 2 * G2_WL) { g2_w<(i) / 2, (i) % 2>(W); SBAR(); } } while (0)
#define GLU1(n, m) glu_store(acc[m][n], acc[m][5 + (n)], acc[(m) + 1][n], acc[(m) + 1][5 + (n)], hs + (size_t)(16 * (1 + (n) / 2)) * SLAB_B + ((n) & 1) * 512 + (m) * 1024 + hl)
    if (GRP == 1) {
        SBAR(); BAR(); MEMFENCE(); SBAR();
        GW(0); GW(1); GW(2); GW(3); GLU1(0, 0); GW(4); GW(5); GLU1(0, 2); GW(6); GW(7); GLU1(1, 0); GW(8); GW(9); GLU1(1, 2); GW(10); GW(11); GLU1(2, 0); GW(12); GW(13); GLU1(2, 2); GW(14); GW(15);
        asm volatile("s_waitcnt vmcnt(%0)" :: "i"(2 * G2_WL + 4) : "memory");
#pragma unroll
        for (int m = 0; m < 4; ++m) { T.dg[0][m] = acc[m][3]; T.dg[1][m] = acc[m][4]; T.du[0][m] = acc[m][8]; T.du[1][m] = acc[m][9]; }
        T.dd[0] = hs + (size_t)32 * SLAB_B + 512; T.dd[1] = hs + (size_t)48 * SLAB_B;
    } else {
        u32x4 Qf[4], Kf[4]; u32x2 Vh[4][2];
#pragma unroll
        for (int m = 0; m < 4; ++m) {
            Qf[m] = (u32x4){pk_bf16(acc[m][0][0], acc[m][0][1]), pk_bf16(acc[m][0][2], acc[m][0][3]), pk_bf16(acc[m][1][0], acc[m][1][1]), pk_bf16(acc[m][1][2], acc[m][1][3])};
            Kf[m] = (u32x4){pk_bf16(acc[m][2][0], acc[m][2][1]), pk_bf16(acc[m][2][2], acc[m][2][3]), pk_bf16(acc[m][3][0], acc[m][3][1]), pk_bf16(acc[m][3][2], acc[m][3][3])};
#pragma unroll
            for (int b = 0; b < 2; ++b) Vh[m][b] = (u32x2){pk_bf16(acc[m][4 + b][0], acc[m][4 + b][1]), pk_bf16(acc[m][4 + b][2], acc[m][4 + b][3])};
        }
        LAS char* xs = lds + G1_XOFF;
        if (wm < 3) {
            *(LAS u32x4*)(xs + (wm + 1) * 2048 + lane * 16) = Kf[3];
            *(LAS u32x4*)(xs + (wm + 1) * 2048 + 1024 + lane * 16) = (u32x4){Vh[3][0][0], Vh[3][0][1], Vh[3][1][0], Vh[3][1][1]};
        }
        {
            const unsigned h0 = pk_bf16(acch[0], acch[1]), h1 = pk_bf16(acch[2], acch[3]);
            if (wm < 2) {
                *(LAS u32x2*)(xs + lane * 16 + wm * 8) = (u32x2){h0, h1};
            } else {
                LAS char* t = xs + 1024 + ((i16 >> 2) * 16 + 4 * g) * 16 + (wm - 2) * 8 + (i16 & 3) * 2;
                *(LAS u16*)(t) = (u16)h0; *(LAS u16*)(t + 16) = (u16)(h0 >> 16); *(LAS u16*)(t + 32) = (u16)h1; *(LAS u16*)(t + 48) = (u16)(h1 >> 16);
            }
        }
        asm volatile("s_waitcnt lgkmcnt(0)" ::: "memory");
        SBAR(); BAR(); MEMFENCE(); SBAR();
        GW(0); GW(1);
        const u32x4 Kp = *(LAS const u32x4*)(xs + wm * 2048 + lane * 16);
        const u32x4 Vp = *(LAS const u32x4*)(xs + wm * 2048 + 1024 + lane * 16);
        const int dc = i16 - 4 * g - (i16 & 1);
        unsigned modd = (i16 & 1) ? ~0u : 0u, mc0 = dc >= 0 ? ~0u : 0u, mc1 = dc >= 2 ? ~0u : 0u;
        asm volatile("" : "+v"(modd), "+v"(mc0), "+v"(mc1));
        const unsigned half = (modd & 0xffff0000u) | (~modd & 0x0000ffffu);
        const unsigned pp0 = half & ~mc0, pp1 = half & ~mc1, pc0 = half & mc0, pc1 = half & mc1;
        const float sc = 0.17677669529663687f * 1.4426950408889634f;
        const f32x4 z = {0.f, 0.f, 0.f, 0.f};
        float e0[4], e1[4], red[4];
#pragma unroll
        for (int m = 0; m < 4; ++m) {
            u32x4 s0 = __builtin_bit_cast(u32x4, MFMA16(as_frag(m ? Kf[m ? m - 1 : 0] : Kp), as_frag(Qf[m]), z));
            const u32x4 s1 = __builtin_bit_cast(u32x4, MFMA16(as_frag(Kf[m]), as_frag(Qf[m]), z));
            if (m == 0 && rt == 0 && wm == 0) { const unsigned c = __float_as_uint(-1e9f / 0.17677669529663687f); s0 = (u32x4){c, c, c, c}; }
            e0[m] = __uint_as_float(bsel(modd, bsel(mc0, s1[1], s0[1]), bsel(mc0, s1[0], s0[0])));
            e1[m] = __uint_as_float(bsel(modd, bsel(mc1, s1[3], s0[3]), bsel(mc1, s1[2], s0[2])));
            red[m] = fmaxf(e0[m], e1[m]);
        }
        {
            rows_spread(rows_reduce<true>(red), red);
        }
#pragma unroll
        for (int m = 0; m < 4; ++m) {
            e0[m] = __builtin_amdgcn_exp2f((e0[m] - red[m]) * sc); e1[m] = __builtin_amdgcn_exp2f((e1[m] - red[m]) * sc);
            red[m] = e0[m] + e1[m];
        }
        {
            float w = __builtin_amdgcn_rcpf(rows_reduce<false>(red));
            rows_spread(w, red);
        }
        char* od = hs + (((g & 1) << 1) | (g >> 1)) * 256 + i16 * 16;
#pragma unroll
        for (int m = 0; m < 4; ++m) {
            const float p0 = e0[m] * red[m], p1 = e1[m] * red[m];
            const unsigned w0 = pk_bf16(p0, p0), w1 = pk_bf16(p1, p1);
            const u32x2 Pp = {w0 & pp0, w1 & pp1}, Pc = {w0 & pc0, w1 & pc1};
            const u32x2 Vp0 = m ? Vh[m ? m - 1 : 0][0] : (u32x2){Vp[0], Vp[1]}, Vp1 = m ? Vh[m ? m - 1 : 0][1] : (u32x2){Vp[2], Vp[3]};
            const f32x4 o0 = MFMA16K16(Vh[m][0], Pc, MFMA16K16(Vp0, Pp, z));
            const f32x4 o1 = MFMA16K16(Vh[m][1], Pc, MFMA16K16(Vp1, Pp, z));
            unsigned x0 = pk_bf16(o0[0], o0[1]), x1 = pk_bf16(o0[2], o0[3]), y0 = pk_bf16(o1[0], o1[1]), y1 = pk_bf16(o1[2], o1[3]);
            auto r0 = __builtin_amdgcn_permlane16_swap(x0, y0, false, false);
            auto r1 = __builtin_amdgcn_permlane16_swap(x1, y1, false, false);
            st16(od + m * 1024, never_ones((u32x4){r0[0], r1[0], r0[1], r1[1]}));
        }
        SBAR();
        GW(2); GW(3); GW(4); GW(5); GW(6); GW(7); GW(8); GW(9); GW(10); GW(11); GW(12); GW(13); GW(14); GW(15);
        asm volatile("s_waitcnt vmcnt(%0)" :: "i"(2 * G2_WL + 2) : "memory");
#pragma unroll
        for (int m = 0; m < 4; ++m) { T.dg[0][m] = acc[m][6]; T.dg[1][m] = acc[m][7]; T.du[0][m] = acc[m][8]; T.du[1][m] = acc[m][9]; }
        T.dd[0] = hs + (size_t)48 * SLAB_B + 512; T.dd[1] = ho + (size_t)(128 + (ct >> 1)) * SLAB_B + (ct & 1) * 512 + (size_t)mb0 * 1024;
        if (blockIdx.x == 0 && wm == 1) {
            flags_wait<POLL_SLEEP>((u32x2){~T.tag, 0u}, ws + WS_LF, (unsigned)(g * 8192 + i16 * 8), T.tag);
            flags_wait<POLL_SLEEP>((u32x2){~T.tag, 0u}, ws + WS_LF + 32768, (unsigned)(g * 8192 + i16 * 8), T.tag);
            flag_set<0>(ws + WS_EP, T.tag);
        }
    }
#undef GW
#undef GLU1
    STAMP_SEG(2);
}

DEVI float sum_x8(float v) {
    v += __uint_as_float(__builtin_amdgcn_update_dpp(0u, __float_as_uint(v), 0x128  , 0xf, 0xf, false));
    { auto t = __builtin_amdgcn_permlane16_swap(__float_as_uint(v), __float_as_uint(v), false, false); v = __uint_as_float(t[0]) + __uint_as_float(t[1]); }
    { auto t = __builtin_amdgcn_permlane32_swap(__float_as_uint(v), __float_as_uint(v), false, false); v = __uint_as_float(t[0]) + __uint_as_float(t[1]); }
    return v;
}

template <int KG>
DEVI void g2_body(G2St& T, LAS char* lds, char* ws, const u16* x, u16* out, int rt, int ct, int wm STAMP_ARGS2) {
    constexpr int kg = KG;
    const int bid = blockIdx.x, idx = bid >> 3, wid = KG * 4 + wm;
    const int kh = (bid >> 2) & 1, side = kh ^ 1, rt2 = 2 * rt + ((bid >> 1) & 1), ct2 = 4 * (bid & 1) + (idx & 3);
    const char* flq = ws + WS_FL + (rt * 4 + (bid & 2)) * 512;
    const int lane2 = fresh_lane(), g = lane2 >> 4, i16 = lane2 & 15, q = i16 >> 2, p = lane2 & 3;
    {
        const char* a0 = ws + WS_HO + (size_t)kg * SLAB_B + (size_t)(8 * rt2 + 2 * wm) * 1024;
        T.pa = a0 + (size_t)(64 * side) * SLAB_B;
        T.pad = a0 + (size_t)128 * SLAB_B;
        T.va0 = lane2 * 16 + kg * 1024; T.va1 = T.va0 ^ 1024;
        T.flagA = ws + WS_FL + (kg ^ 1) * 16384 + (rt * 4 + wm) * 512 + ct * 4;
        T.flagZ = ws + WS_FL + (rt * 4 + wm) * 512 + 256 + kg * 128 + ct * 4;
        T.hl = (unsigned)((g & 1) * 1024 + (g >> 1) * 256 + i16 * 16);
        T.poller = wid == 0;
        T.flags = T.poller ? flq : ws + WS_XB + (size_t)bid * 32768;
        T.foa = (unsigned)((lane2 >> 5) * 512 + side * 64 + (lane2 & 7) * 8);
        T.foz = (unsigned)((lane2 >> 5) * 512 + 256 + (lane2 & 31) * 8);
    }
#pragma unroll
    for (int n = 0; n < 4; ++n) {
        T.pB[0][n] = lds + kg * 4096 + (8 * g + q) * 128 + (((2 * n + (p >> 1)) ^ ((q & 2) | ((g & 1) << 2))) * 16) + (p & 1) * 8;
#pragma unroll
        for (int k = 1; k < (G2_NS + 3) / 4; ++k) { T.pB[k][n] = T.pB[0][n] + k * 4 * G2_ST; asm volatile("" : "+v"(T.pB[k][n])); }
        asm volatile("" : "+v"(T.pB[0][n]));
    }
    MEMFENCE(); SBAR(); BAR(); MEMFENCE(); SBAR();
    g2_a<0, 0>(T); g2_a<0, 1>(T); g2_a<1, 0>(T); g2_a<1, 1>(T);
    SBAR();
#pragma unroll
    for (int n = 0; n < 8; ++n) { T.B[n][0] = g2_ldb<0>(T, n, 0); T.B[n][1] = g2_ldb<0>(T, n, 1); }
    SBAR();
    STAMP_SEG(3);
    g2_run<KG, 0, 8>(T);
    STAMP_SEG(4);
    g2_run<KG, 8, 24>(T);
    const int orow = 128 * rt2 + 32 * wm + 16 * kg + i16, ocol = 128 * ct2 + 64 * kh + (g & 1) * 16 + (g >> 1) * 8;
    T.xw = lds + G2_XOFF + wid * 4096 + lane2 * 16;
    T.xr = lds + G2_XOFF + (wid ^ 4) * 4096 + lane2 * 16;
    T.snd = ws + WS_XB + (size_t)bid * 32768 + wid * 4096 + lane2 * 16;
    T.rcv = ws + WS_XB + (size_t)(bid ^ 4) * 32768 + wid * 4096 + lane2 * 16;
    T.xp = x + (size_t)orow * D_ + ocol;
    g2_run<KG, 24, 36>(T);
    STAMP_SEG(5);

    MFMA_DRAIN(); SBAR();
#pragma unroll
    for (int j = 0; j < 4; ++j) *(LAS f32x4*)(T.xw + j * 1024) = T.acc[1][j];
    WAIT_LGKM0();
    MEMFENCE(); SBAR(); BAR(); MEMFENCE(); SBAR();
#pragma unroll
    for (int j = 0; j < 4; ++j) T.acc[0][j] += *(LAS const f32x4*)(T.xr + j * 1024);
    asm volatile("s_waitcnt vmcnt(0)" : "+v"(T.xres[0]), "+v"(T.xres[1]), "+v"(T.rc[0]), "+v"(T.rc[1]), "+v"(T.rc[2]), "+v"(T.rc[3]) :: "memory");
    for (unsigned spin = 0;; ++spin) {
        const unsigned mx = umax4((u32x4){umax4(T.rc[0]), umax4(T.rc[1]), umax4(T.rc[2]), umax4(T.rc[3])});
        if (!__any(mx == 0xffffffffu) || spin > SPIN_CAP) break;
        __builtin_amdgcn_s_sleep(4);
        asm volatile("global_load_dwordx4 %0, %4, off sc1\n\tglobal_load_dwordx4 %1, %4, off offset:1024 sc1\n\tglobal_load_dwordx4 %2, %4, off offset:2048 sc1\n\tglobal_load_dwordx4 %3, %4, off offset:3072 sc1\n\ts_waitcnt vmcnt(0)"
                     : "=&v"(T.rc[0]), "=&v"(T.rc[1]), "=&v"(T.rc[2]), "=&v"(T.rc[3]) : "v"(T.rcv) : "memory");
    }
    u16* orp = out + (size_t)orow * D_ + ocol;
#pragma unroll
    for (int j = 0; j < 4; j += 2) {
        const f32x4 s0 = T.acc[0][j] + __builtin_bit_cast(f32x4, T.rc[j]), s1 = T.acc[0][j + 1] + __builtin_bit_cast(f32x4, T.rc[j + 1]);
        float v[8];
#pragma unroll
        for (int e = 0; e < 4; ++e) { auto r = __builtin_amdgcn_permlane16_swap(__float_as_uint(s0[e]), __float_as_uint(s1[e]), false, false); v[e] = __uint_as_float(r[0]); v[4 + e] = __uint_as_float(r[1]); }
        const u32x4 xv = T.xres[j >> 1];
        u32x4 o;
#pragma unroll
        for (int e = 0; e < 4; ++e) o[e] = pk_bf16(v[2 * e] + bf_lo(xv[e]), v[2 * e + 1] + bf_hi(xv[e]));
        *(GAS u32x4*)(size_t)(orp + 16 * j) = o;
    }
    asm volatile("s_waitcnt vmcnt(0)" ::: "memory");
    STAMP_SEG(6);
    STAMP_OUT(ws, 0, wid, lane2);
    STAMP_OUT(ws, 1, wid, lane2);
}

__global__ __launch_bounds__(512, 2) void k_main(const char* __restrict__ wqkv, const char* __restrict__ wg, const char* __restrict__ wu, const char* __restrict__ wo, const char* __restrict__ wd, char* __restrict__ ws,
                                                const u16* __restrict__ x, u16* __restrict__ out) {
    extern __shared__ __attribute__((aligned(16))) char smem[];
    TL_ENTRY();
    asm volatile("" : "+s"(wqkv), "+s"(wg), "+s"(wu), "+s"(wo), "+s"(wd), "+s"(ws), "+s"(x), "+s"(out));
    LAS char* lds = (LAS char*)smem;
    const int tid = threadIdx.x, lane = tid & 63;
    const int wid = __builtin_amdgcn_readfirstlane(tid >> 6);
    const int bid = blockIdx.x, xcd = bid & 7, idx = bid >> 3;
    const int ct = 4 * xcd + (idx & 3), rt = idx >> 2;
    const unsigned ldsb = (unsigned)(size_t)lds;
    const int r8 = lane & 7, c8 = lane >> 3, row = 256 * rt + 8 * ct + r8;
    u32x4 xv[2];
    unsigned ep;
    asm volatile("global_load_dwordx4 %0, %3, %4\n\tglobal_load_dwordx4 %1, %3, %4 offset:128\n\ts_load_dword %2, %5, 0x0"
                 : "=&v"(xv[0]), "=&v"(xv[1]), "=&s"(ep) : "v"((unsigned)(row * 2048 + (16 * wid + c8) * 16)), "s"(x), "s"(ws + WS_EP) : "memory");
    G1Ld L;
    {
        const int c = wid * 64 + lane, rho = c / 14, cc = c - rho * 14;
        const int k = (rho & ~12) | ((rho & 4) << 1) | ((rho & 8) >> 1);
        const int c2 = cc < 12 ? cc : 11;
        const bool last = wid == 7;
        L.pa = ws + WS_XN + (size_t)(16 * rt + 2 * wid) * 1024;
        L.stepa = (unsigned)SLAB_B;
        L.va = lane * 16;
        L.la = ldsb + (1 + 2 * wid) * 1024;
        L.p2 = last ? ws + WS_XN + (size_t)(rt == 0 ? 0 : 16 * rt - 1) * 1024 : wqkv + ct * 64;
        L.step2 = last ? (unsigned)SLAB_B : 32u * 3072u * 2u;
        L.v2 = last ? (unsigned)((rt == 0 ? (lane & 48) : lane) * 16) : (unsigned)(k * 6144 + (c2 >> 2) * 2048 + (c2 & 3) * 16);
        L.l2 = last ? ldsb : ldsb + G1_BOFF + wid * 1024;
        L.s2 = last ? G1_AST : G1_BST;
        L.pg = wg + ct * 224;
        L.pu = wu + ct * 224;
        L.stepg = 32u * 3584u * 2u;
        L.vg = last ? 0u : (unsigned)(k * 7168 + cc * 16);
        L.lg = last ? ldsb + G1_BOFF + 192 : ldsb + G1_BOFF + G1_REG + wid * 1024;
        L.lu = last ? ldsb + G1_BOFF + 192 : ldsb + G1_BOFF + 2 * G1_REG + wid * 1024;
        L.ma = ~0ull; L.m2 = ~0ull; L.mg = last ? 1ull : ~0ull;
    }
    L.warm = wid < 4 ? wo + bid * 8192 + wid * 2048 : wd + bid * 28672 + (wid & 3) * 7168;
    L.lw = ldsb + G1_XOFF + (wid & 3) * 1024; L.ldum = ldsb + G1_BOFF + 192;
    L.pf = ws + WS_XB + (size_t)bid * 32768 + (wid & 3) * 8192;
    const int kh = (bid >> 2) & 1, side = kh ^ 1, rt2 = 2 * rt + ((bid >> 1) & 1), ct2 = 4 * (bid & 1) + (idx & 3);
    const int kg = wid >> 2, wm = wid & 3;
    G2St T;
    T.W.pwo = wo + (size_t)(side * 512) * 2048 + ct2 * 256;
    T.W.pwh = wd + (size_t)(side * 1792 + kg * 80) * 2048 + ct2 * 256;
    T.W.pwd = wd + (size_t)(96 + (wid >> 1) * 96) * 2048 + ct2 * 256;
    T.W.ldsw = ldsb + wid * 1024;
    STAMP_DECL STAMP_START();
    if (wid < 7) { g1_p2<0>(L); g1_pg<0>(L); g1_pu<0>(L); asm volatile("s_waitcnt vmcnt(3)" ::: "memory"); }
    else asm volatile("s_waitcnt vmcnt(0)" ::: "memory");
    asm volatile("s_waitcnt lgkmcnt(0)" : "+v"(xv[0]), "+v"(xv[1]), "+s"(ep) :: "memory");
    T.tag = ep * 1664525u + 1013904223u;
    P0S(2);
    {
        LAS float* red = (LAS float*)(lds + G1_XOFF);
        f32x2 f[8];
#pragma unroll
        for (int j = 0; j < 8; ++j) f[j] = (f32x2){bf_lo(xv[j >> 2][j & 3]), bf_hi(xv[j >> 2][j & 3])};
        {
            const u32x4 ones = {0xffffffffu, 0xffffffffu, 0xffffffffu, 0xffffffffu};
            asm volatile("global_store_dwordx4 %0, %1, %2 sc1\n\tglobal_store_dwordx4 %0, %1, %2 offset:1024 sc1\n\ts_nop 1"
                         :: "v"(L.va), "v"(ones), "s"(ws + WS_HO + (size_t)(64 * (ct >> 4) + (ct & 15)) * SLAB_B + (size_t)(16 * rt + 2 * wid) * 1024) : "memory");
        }
        const f32x2 s2 = ((f[0] + f[1]) + (f[2] + f[3])) + ((f[4] + f[5]) + (f[6] + f[7]));
        const float mw = sum_x8(s2[0] + s2[1]) * (1.0f / 128);
        f32x2 q2 = {0.f, 0.f};
#pragma unroll
        for (int j = 0; j < 8; ++j) { const f32x2 d = f[j] - mw; q2 = __builtin_elementwise_fma(d, d, q2); }
        const float qw = sum_x8(q2[0] + q2[1]);
        if (c8 == 0) *(LAS f32x2*)(red + r8 * 16 + wid * 2) = (f32x2){mw, qw};
        WAIT_LGKM0(); MEMFENCE(); BAR(); MEMFENCE();
        f32x4 st[4];
#pragma unroll
        for (int k = 0; k < 4; ++k) st[k] = *(LAS const f32x4*)(red + r8 * 16 + k * 4);
        const float mean = (((st[0][0] + st[0][2]) + (st[1][0] + st[1][2])) + ((st[2][0] + st[2][2]) + (st[3][0] + st[3][2]))) * 0.125f;
        float qt = 0.f, dt = 0.f;
#pragma unroll
        for (int k = 0; k < 4; ++k) { const float d0 = st[k][0] - mean, d1 = st[k][2] - mean; qt += st[k][1] + st[k][3]; dt = fmaf(d0, d0, fmaf(d1, d1, dt)); }
        const float mb = rbf(mean);
        const float vb = rbf(fmaf(dt, 128.f, qt) * (1.0f / D_));
        const float den = rbf(__builtin_amdgcn_sqrtf(rbf(vb + 1e-5f)));
        const float r0 = __builtin_amdgcn_rcpf(den), rc = fmaf(fmaf(-den, r0, 1.0f), r0, r0);
#pragma unroll
        for (int i = 0; i < 2; ++i) {
            u32x4 o;
#pragma unroll
            for (int j = 0; j < 4; ++j) {
                const f32x2 c = f[i * 4 + j] - mb;
                const unsigned d = pk_bf16(c[0], c[1]);
                const f32x2 y = (f32x2){bf_lo(d), bf_hi(d)} * rc;
                o[j] = pk_bf16(y[0], y[1]);
            }
            const int ch = 16 * wid + 8 * i + c8;
            asm volatile("global_store_dwordx4 %0, %1, %2 sc1\n\ts_nop 1" :: "v"((unsigned)((ch >> 2) * (int)SLAB_B + (row >> 4) * 1024 + ((ch & 3) * 16 + (row & 15)) * 16)), "v"(o), "s"(ws + WS_XN) : "memory");
        }
        P0S(3);
        asm volatile("s_waitcnt vmcnt(0)\n\ts_barrier" ::: "memory");
        P0S(4);
        if (wid < 7) {
            if (!W_EARLY) { g1_p2<0>(L); g1_pg<0>(L); g1_pu<0>(L); }
            g1_p2<1>(L); g1_pg<1>(L); g1_pu<1>(L);
        } else {
            const bool next = ct >= 30 && rt < 7;
            asm volatile("s_mov_b64 exec, %3\n\tglobal_store_dword %0, %1, %2 sc1\n\ts_mov_b64 exec, -1"
                         :: "v"((unsigned)((rt * 32 + lane) * 256 + (lane < 32 ? ct : ct + 2) * 4)), "v"(T.tag), "s"(ws + WS_LF), "s"(next ? ~0ull : 0xffffffffull) : "memory");
            flags_wait<LN_SLEEP>((u32x2){~T.tag, 0u}, ws + WS_LF + (rt * 32 + ct) * 256, (unsigned)(lane < 16 || rt == 0 ? (lane & 15) * 8 : 128), T.tag);
        }
        MEMFENCE(); BAR(); MEMFENCE();
    }
    STAMP_SEG(1);
    if (wid < 4) { g1_body<0>(L, T, lds, ws, rt, ct, wm, kh STAMP_PASS); g2_body<0>(T, lds, ws, x, out, rt, ct, wm STAMP_PASS2); }
    else { g1_body<1>(L, T, lds, ws, rt, ct, wm, kh STAMP_PASS); g2_body<1>(T, lds, ws, x, out, rt, ct, wm STAMP_PASS2); }
    TL_EXIT(ws, 0);
    TL_EXIT(ws, 1);
    TL_EXIT(ws, 2);

}

__global__ void k_probe(char* ws) {
    const int t = threadIdx.x;
    unsigned long long v[6];
    for (int k = 0; k < 3; ++k) {
        const unsigned long long* p = (const unsigned long long*)(ws + (140u << 20) + k * (1u << 20));
        unsigned long long e = ~0ull, x = 0;
#pragma unroll
        for (int i = 0; i < 32; ++i) { const unsigned long long a = p[2 * (i * 64 + t)], b = p[2 * (i * 64 + t) + 1]; e = e < a ? e : a; x = x > b ? x : b; }
        for (int o = 32; o; o >>= 1) { const unsigned long long a = __shfl_xor(e, o), b = __shfl_xor(x, o); e = e < a ? e : a; x = x > b ? x : b; }
        v[2 * k] = e; v[2 * k + 1] = x;
    }
    unsigned long long s = 0;
    { const unsigned* q = (const unsigned*)(ws + (128u << 20) + PROBE_KERN * (1u << 20)); for (int i = 0; i < 4; ++i) s += q[((i * 64 + t) * 8 + PROBE_WAVE) * 8 + PROBE_SEG]; }
    for (int o = 32; o; o >>= 1) s += __shfl_xor(s, o);
    unsigned long long all = (v[0] ^ v[1] ^ v[2] ^ v[3] ^ v[4] ^ v[5] ^ s) == 0x123456789abcdefull ? 1 : 0;
    unsigned long long d = PROBE_I < 0 ? 0 : (PROBE_I < 5 ? v[PROBE_I < 5 && PROBE_I >= 0 ? PROBE_I + 1 : 0] - v[PROBE_I < 5 && PROBE_I >= 0 ? PROBE_I : 0] : (PROBE_I == 5 ? v[5] - v[0] : s / 256 / PROBE_DIV));
    d += all;
    if (d > 20000) d = 20000;
    const unsigned long long t0 = __builtin_amdgcn_s_memrealtime();
    while (__builtin_amdgcn_s_memrealtime() - t0 < d * PROBE_K) __builtin_amdgcn_s_sleep(1);
}

extern "C" void kernel_launch(void* const* d_in, const int* in_sizes, int n_in,
                              void* d_out, int out_size, void* d_ws, size_t ws_size,
                              hipStream_t stream) {
    const u16* x = (const u16*)d_in[0];
    const char* wqkv = (const char*)d_in[1];
    const char* wo = (const char*)d_in[2];
    const char* wg = (const char*)d_in[4];
    const char* wu = (const char*)d_in[5];
    const char* wd = (const char*)d_in[6];
    char* ws = (char*)d_ws;
    (void)hipFuncSetAttribute((const void*)k_main, hipFuncAttributeMaxDynamicSharedMemorySize, G1_LDS);
    k_main<<<256, 512, G1_LDS, stream>>>(wqkv, wg, wu, wo, wd, ws, x, (u16*)d_out);
    k_probe<<<1, 64, 0, stream>>>(ws);
}
```

```cpp
#include <hip/hip_runtime.h>
#include <hip/hip_bf16.h>
#include <stdint.h>

typedef __bf16 bf16x8 __attribute__((ext_vector_type(8)));
typedef __bf16 bf16x4 __attribute__((ext_vector_type(4)));
typedef __bf16 bf16x2 __attribute__((ext_vector_type(2)));
typedef float f32x4 __attribute__((ext_vector_type(4)));
typedef float f32x2 __attribute__((ext_vector_type(2)));
typedef unsigned u32x4 __attribute__((ext_vector_type(4)));
typedef unsigned u32x2 __attribute__((ext_vector_type(2)));
typedef unsigned short u16;
#define LAS __attribute__((address_space(3)))
#define GAS __attribute__((address_space(1)))
#define DEVI __device__ __forceinline__

constexpr int S_ = 2048, D_ = 1024, F_ = 3584;
constexpr int MB_ = S_ / 16;
constexpr size_t SLAB_B = (size_t)MB_ * 1024;
constexpr size_t WS_XN = 0;
constexpr size_t WS_HO = 32 * SLAB_B;
constexpr size_t WS_XB = WS_HO + 144 * SLAB_B;
constexpr size_t WS_FL = WS_XB + 256 * 32768;
constexpr size_t WS_LF = WS_FL + 32768;
constexpr size_t WS_EP = WS_LF + 65536;

DEVI unsigned pk_bf16(float lo, float hi) {
    bf16x2 v = {(__bf16)lo, (__bf16)hi};
    return __builtin_bit_cast(unsigned, v);
}
DEVI float bf_lo(unsigned u) { return __uint_as_float(u << 16); }
DEVI float bf_hi(unsigned u) { return __uint_as_float(u & 0xffff0000u); }
DEVI float rbf(float f) { return (float)(__bf16)f; }
DEVI bf16x8 as_frag(u32x4 v) { return __builtin_bit_cast(bf16x8, v); }
#define MFMA16(a, b, c) __builtin_amdgcn_mfma_f32_16x16x32_bf16((a), (b), (c), 0, 0, 0)
#define SBAR() __builtin_amdgcn_sched_barrier(0)
#define MFMA_ACC(c, a, b) asm volatile("v_mfma_f32_16x16x32_bf16 %0, %1, %2, %0" : "+v"(c) : "v"(a), "v"(b))
#define MFMA_NEW(c, a, b) asm volatile("v_mfma_f32_16x16x32_bf16 %0, %1, %2, 0" : "=&v"(c) : "v"(a), "v"(b))
#define MFMA_DRAIN() asm volatile("s_nop 15" ::: "memory")
#define BAR() __builtin_amdgcn_s_barrier()

#define TL_ENTRY()
#define TL_EXIT(ws, kern)
#define STAMP_DECL
#define STAMP_ARGS
#define STAMP_PASS
#define STAMP_ARGS2
#define STAMP_PASS2
#define STAMP_SEG(i)
#define STAMP_START()
#define STAMP_SEGI(i)
#define STAMP_OUT(ws, kern, wid, lane)

#define P0S(i)
DEVI int fresh_lane() { int l; asm volatile("v_mbcnt_lo_u32_b32 %0, -1, 0\n\tv_mbcnt_hi_u32_b32 %0, -1, %0" : "=v"(l)); return l; }

DEVI bf16x8 tr_frag(LAS const char* p, int off0, int off1) {
    bf16x4 lo = __builtin_amdgcn_ds_read_tr16_b64_v4bf16((LAS bf16x4*)(p + off0));
    bf16x4 hi = __builtin_amdgcn_ds_read_tr16_b64_v4bf16((LAS bf16x4*)(p + off1));
    return __builtin_shufflevector(lo, hi, 0, 1, 2, 3, 4, 5, 6, 7);
}

DEVI void glds16(const char* sbase, unsigned voff, unsigned ldsdst) {
    unsigned keep;
    asm volatile("s_mov_b32 %0, m0\n\ts_mov_b32 m0, %3\n\ts_nop 0\n\tglobal_load_lds_dwordx4 %1, %2\n\ts_mov_b32 m0, %0"
                 : "=&s"(keep) : "v"(voff), "s"(sbase), "s"(ldsdst) : "memory");
}

constexpr int G1_AST = 17 * 1024;
constexpr int G1_BST = 21 * 1024;
constexpr int G1_REG = 7168;
constexpr int G1_BOFF = 4 * G1_AST;
constexpr int G1_XOFF = G1_BOFF + 4 * G1_BST;
constexpr int G1_LDS = G1_XOFF + 8192;

typedef unsigned long long u64;
#define FRAG_REGS "v192", "v193", "v194", "v195", "v196", "v197", "v198", "v199", "v200", "v201", "v202", "v203", "v204", "v205", "v206", "v207", "v208", "v209", "v210", "v211", "v212", "v213", "v214", "v215", "v216", "v217", "v218", "v219", "v220", "v221", "v222", "v223", "v224", "v225", "v226", "v227", "v228", "v229", "v230", "v231", "v232", "v233", "v234", "v235", "v236", "v237", "v238", "v239", "v240", "v241", "v242", "v243", "v244", "v245", "v246", "v247", "v248", "v249", "v250", "v251", "v252", "v253", "v254", "v255"
constexpr int FA = 192, FB = 208, FAH = 248, FBH = 252;
struct G1Ld {
    const char *pa, *p2, *pg, *pu;
    unsigned stepa, step2, stepg;
    unsigned va, v2, vg;
    unsigned la, l2, s2, lg, lu;
    const char* warm; unsigned lw, ldum;
    const char* tb; unsigned ts;
    char* pf;
    u64 ma, m2, mg;
};

#define WAIT_LGKM0() __builtin_amdgcn_s_waitcnt(0xC07F)
#define MEMFENCE() asm volatile("" ::: "memory")
#define OPAQUE_S(x) asm volatile("" : "+s"(x))

template <int LOFF, int KIND>
DEVI void g1_piece(const char* base, unsigned voff, unsigned lds, u64 msk) {
    if (KIND == 0) asm volatile("s_add_u32 m0, %2, %4\n\ts_mov_b64 exec, %3\n\tglobal_load_lds_dwordx4 %0, %1\n\ts_mov_b64 exec, -1" :: "v"(voff), "s"(base), "s"(lds), "s"(msk), "i"(LOFF) : "memory", "scc");
    if (KIND == 1) asm volatile("s_add_u32 m0, %2, %4\n\ts_mov_b64 exec, %3\n\tglobal_load_lds_dwordx4 %0, %1 sc1\n\ts_mov_b64 exec, -1" :: "v"(voff), "s"(base), "s"(lds), "s"(msk), "i"(LOFF) : "memory", "scc");
    if (KIND == 2) asm volatile("s_add_u32 m0, %2, %4\n\ts_mov_b64 exec, %3\n\tglobal_load_lds_dwordx4 %0, %1 sc1\n\tglobal_load_lds_dwordx4 %0, %1 offset:1024 sc1\n\ts_mov_b64 exec, -1"
                                :: "v"(voff), "s"(base), "s"(lds), "s"(msk), "i"(LOFF) : "memory", "scc");
}
template <int SLOT> DEVI void g1_pa(G1Ld& L) { g1_piece<SLOT * G1_AST, 2>(L.pa, L.va, L.la, L.ma); L.pa += L.stepa; OPAQUE_S(L.pa); }
template <int SLOT> DEVI void g1_p2(G1Ld& L) { g1_piece<0, 1>(L.p2, L.v2, L.l2 + SLOT * L.s2, L.m2); L.p2 += L.step2; OPAQUE_S(L.p2); }
template <int SLOT> DEVI void g1_pg(G1Ld& L) { g1_piece<SLOT * G1_BST, 0>(L.pg, L.vg, L.lg, L.mg); L.pg += L.stepg; OPAQUE_S(L.pg); }
template <int SLOT> DEVI void g1_pu(G1Ld& L) { g1_piece<SLOT * G1_BST, 0>(L.pu, L.vg, L.lu, L.mg); L.pu += L.stepg; OPAQUE_S(L.pu); }
template <int SLOT> DEVI void g1_issue(G1Ld& L) { g1_pa<SLOT>(L); g1_p2<SLOT>(L); g1_pg<SLOT>(L); g1_pu<SLOT>(L); }
template <int GRP> DEVI void g1_tail(G1Ld& L, bool tail) {
    L.p2 = tail ? L.p2 - L.step2 : L.p2; L.pg = tail ? L.pg - L.stepg : L.pg; L.pu = tail ? L.pu - L.stepg : L.pu;
    L.step2 = tail ? 0u : L.step2; L.stepg = tail ? 0u : L.stepg;
    L.m2 = tail ? 1ull : L.m2; L.mg = tail ? 1ull : L.mg;
    if (GRP == 0) L.pa = tail ? L.warm : L.pa;
    else { L.pa = tail ? L.pa - L.stepa : L.pa; L.ma = tail ? 1ull : L.ma; L.warm = tail ? L.warm - 1024 : L.warm; L.lw = tail ? L.ldum : L.lw; }
    L.stepa = tail ? 0u : L.stepa;
}

struct G1Ptr { unsigned pA, pH, pB01, pB23, pBh01, pBh23; };

template <int REG, int OFF> DEVI void ds_tr(unsigned base) { asm volatile("ds_read_b64_tr_b16 v[%c1:%c2], %0 offset:%c3" :: "v"(base), "i"(REG), "i"(REG + 1), "i"(OFF) : "memory"); }
template <int REG, int OFF> DEVI void ds_128(unsigned base) { asm volatile("ds_read_b128 v[%c1:%c2], %0 offset:%c3" :: "v"(base), "i"(REG), "i"(REG + 3), "i"(OFF) : "memory"); }
template <int RA_, int RB_> DEVI void mfma_fr(f32x4& c) { asm volatile("v_mfma_f32_16x16x32_bf16 %0, v[%c1:%c2], v[%c3:%c4], %0" : "+v"(c) : "i"(RA_), "i"(RA_ + 3), "i"(RB_), "i"(RB_ + 3)); }
template <int GRP, int SL, int n, int h> DEVI void g1_ldb(const G1Ptr& P) {
    constexpr int off = (SL & 1) * G1_BST + (GRP == 0 ? (n < 6 ? n * 32 : (n < 8 ? G1_REG + (n - 1) * 32 : 2 * G1_REG + (n - 3) * 32))
                                                      : (n < 5 ? G1_REG + n * 32 : 2 * G1_REG + (n - 5) * 32));
    ds_tr<FB + 4 * n + 2 * h, off + h * 8 * 224>((SL < 2) ? P.pB01 : P.pB23);
}
template <int SL, int m> DEVI void g1_lda(const G1Ptr& P) { ds_128<FA + 4 * m, SL * G1_AST + (1 + m) * 1024>(P.pA); }
template <int SL> DEVI void g1_ldah(const G1Ptr& P) { ds_128<FAH, SL * G1_AST>(P.pH); }
template <int SL, int h> DEVI void g1_ldbh(const G1Ptr& P) { ds_tr<FBH + 2 * h, (SL & 1) * G1_BST + h * 8 * 224>((SL < 2) ? P.pBh01 : P.pBh23); }

template <int GRP, int ST>
DEVI void g1_stage(f32x4 (&acc)[4][10], f32x4& acch, G1Ld& L, bool tail, const G1Ptr& P) {
    constexpr int NX = (ST + 1) & 3, IS = (ST + 3) & 3;
#define MM(m, n) do { if (GRP == 0 && ((n) == 4 || (n) == 5)) mfma_fr<FA + 4 * (m), FB + 4 * (n)>(acc[m][n]); else mfma_fr<FB + 4 * (n), FA + 4 * (m)>(acc[m][n]); } while (0)
#define RB(n, h) g1_ldb<GRP, NX, n, h>(P)
#define RC(n, h) g1_ldb<GRP, ST, n, h>(P)
#define RA(m) g1_lda<NX, m>(P)
#define G0(x) do { if (GRP == 0) { x; } } while (0)
    asm volatile("s_waitcnt lgkmcnt(9)" ::: "memory");
    MM(0, 0); RC(8, 0);
    asm volatile("s_waitcnt lgkmcnt(9)" ::: "memory");
    MM(1, 0); RC(8, 1);
    MM(1, 1); RC(9, 0); MM(0, 1); RC(9, 1);
    MM(0, 2); G0(g1_pa<IS>(L)); MM(1, 2);
    MM(1, 3); G0(g1_p2<IS>(L)); MM(0, 3); G0(g1_pg<IS>(L));
    MM(0, 4); G0(g1_pu<IS>(L)); MM(1, 4);
    G0((mfma_fr<FBH, FAH>(acch)));
    asm volatile("s_waitcnt vmcnt(10) lgkmcnt(0)\n\ts_barrier" ::: "memory", FRAG_REGS);
    if (GRP == 1) {
        if (ST == 0) g1_tail<1>(L, tail);
        g1_issue<ST>(L);
        if (ST == 1) { g1_piece<0, 0>(L.warm, L.va, L.lw, L.ma); L.warm += 1024; OPAQUE_S(L.warm); }
        if (ST == 2) {
            const u32x4 ones = {0xffffffffu, 0xffffffffu, 0xffffffffu, 0xffffffffu};
            asm volatile("global_store_dwordx4 %0, %1, %2 sc1\n\ts_nop 1" :: "v"(L.va), "v"(ones), "s"(L.pf) : "memory", FRAG_REGS);
            L.pf += 1024; OPAQUE_S(L.pf);
        }
    }
    MM(2, 0); MM(3, 0); RB(0, 0);
    MM(3, 1); RB(0, 1); MM(2, 1); RB(1, 0);
    MM(2, 2); RB(1, 1); MM(3, 2); RB(2, 0);
    MM(3, 3); RB(2, 1); MM(2, 3); RB(3, 0);
    MM(2, 4); RB(3, 1); MM(3, 4); RB(4, 0);
    MM(0, 5); RB(4, 1); MM(1, 5); G0(g1_ldah<NX>(P));
    MM(1, 6); G0((g1_ldbh<NX, 0>(P))); MM(0, 6); G0((g1_ldbh<NX, 1>(P)));
    MM(0, 7); MM(1, 7);
    MM(1, 8); MM(0, 8);
    MM(0, 9); RA(0); MM(1, 9); RA(1);
    MM(2, 5); MM(3, 5); RB(5, 0);
    MM(3, 6); RB(5, 1); MM(2, 6); RB(6, 0);
    MM(2, 7); RB(6, 1); MM(3, 7); RB(7, 0);
    MM(3, 8); RB(7, 1); MM(2, 8);
    MM(2, 9); RA(2); MM(3, 9); RA(3);
#undef MM
#undef RB
#undef RC
#undef RA
#undef G0
}

DEVI void st16(char* dst, u32x4 v) {
    asm volatile("global_store_dwordx4 %0, %1, off sc1\n\ts_nop 1" :: "v"(dst), "v"(v) : "memory");
}

DEVI unsigned glu2(float g0, float g1, float u0, float u1) {
    const f32x2 t = (f32x2){g0, g1} * -1.4426950408889634f;
    const f32x2 d = (f32x2){__builtin_amdgcn_exp2f(t[0]), __builtin_amdgcn_exp2f(t[1])} + 1.0f;
    const f32x2 h = (f32x2){u0, u1} * (f32x2){__builtin_amdgcn_rcpf(d[0]), __builtin_amdgcn_rcpf(d[1])};
    return pk_bf16(h[0], h[1]);
}

DEVI void glu_store(const f32x4& g0, const f32x4& u0, const f32x4& g1, const f32x4& u1, char* dst) {
    unsigned a0 = glu2(g0[0], g0[1], u0[0], u0[1]), a1 = glu2(g0[2], g0[3], u0[2], u0[3]);
    unsigned b0 = glu2(g1[0], g1[1], u1[0], u1[1]), b1 = glu2(g1[2], g1[3], u1[2], u1[3]);
    auto r0 = __builtin_amdgcn_permlane16_swap(a0, b0, false, false);
    auto r1 = __builtin_amdgcn_permlane16_swap(a1, b1, false, false);
    st16(dst, (u32x4){r0[0], r1[0], r0[1], r1[1]});
    SBAR();
}

#define W_EARLY 1
#define LN_SLEEP 2
#define POLL_SLEEP 8
#define G2_RCV 34
#define G2_PLACE 1
#define G2_NS 8
constexpr int G2_ST = 16384;
constexpr int G2_WL = G2_NS - 1;
constexpr int G2_XOFF = G2_NS <= 8 ? G2_NS * G2_ST : 0;
constexpr int G2_F = 32, G2_XST = 33;
constexpr int G2_FY = 8, G2_FZ = 12;
constexpr int G2_PA = 1, G2_PY = 9, G2_PZ = 15;
constexpr unsigned SPIN_CAP = 1u << 17;
static_assert(G2_RCV > G2_F && G2_RCV <= 35 && G2_NS >= 6 && G2_NS <= 9, "");
constexpr int g2_grp(int t) { return t < G2_F ? t : G2_F + 2 * (t - G2_F); }
constexpr int g2_afirst(int s) { return s < 2 ? 2 + 2 * s : s + 4 <= 35 ? g2_grp(s + 4) : 40; }
constexpr int g2_alast(int s) { return g2_afirst(s + 1); }
constexpr int g2_tops(int s) { return s == 0 || s == G2_FY || s == G2_FZ ? 1 : 0; }
constexpr int g2_wops(int s) { return s + G2_WL <= 35 ? 2 : 0; }
constexpr int g2_xops(int s) { return (s < 8 && (s & 1) ? 1 : 0) + (s == G2_PA || s == G2_PY || s == G2_PZ ? 1 : 0) + (s == G2_F ? 4 : 0) + (s == G2_XST ? 2 : 0) + (s == G2_RCV ? 4 : 0); }
constexpr int g2_wait(int t) {
    const int need = g2_grp(t + 1) - 1;
    int n = 4, last = need < 2 ? 2 * need + 2 : 0;
    for (int s = 0; s < t; ++s) {
        n += g2_tops(s) + g2_wops(s);
        if (s + G2_WL == t + 1 && n > last) last = n;
        for (int g = g2_afirst(s); g < g2_alast(s); ++g) { n += 2; if (g == need && n > last) last = n; }
        n += g2_xops(s);
        if ((t == 4 && s == G2_PA) || (t == 12 && s == G2_PY) || (t == 20 && s == G2_PZ) || (t == G2_FY && s == 3) || (t == G2_FZ && s == 7)) last = n > last ? n : last;
    }
    return n - last;
}

struct G2W {
    const char *pwo, *pwh, *pwd;
    unsigned vw0, vw1, vw2, ldsw;
};
struct G2St {
    G2W W;
    const char *pa, *pad;
    unsigned va0, va1;
    LAS const char* pB[(G2_NS + 3) / 4][4];
    LAS char* xw; LAS const char* xr;
    char* snd; const char* rcv; const u16* xp;
    f32x4 dg[2][4], du[2][4]; char* dd[2]; unsigned hl, hq[4];
    char *flagA, *flagZ; const char* flags; unsigned foa, foz, tag; u32x2 hf; bool poller;
    u32x4 A[40][2];
    bf16x4 B[8][2];
    f32x4 acc[2][8];
    u32x4 xres[2], rc[4];
};
template <int LOFF> DEVI void g2_glds(const char* base, unsigned voff, unsigned lds) {
    asm volatile("s_add_u32 m0, %2, %3\n\ts_nop 0\n\tglobal_load_lds_dwordx4 %0, %1" :: "v"(voff), "s"(base), "s"(lds), "i"(LOFF) : "memory", "scc");
}
template <int ST, int I> DEVI void g2_w(const G2W& W) {
    constexpr int SLOT = ST % G2_NS;
    const char* src = ST < 8 ? W.pwo + ST * 64 * 2048 : ST < G2_F ? W.pwh + (((ST - 8) & 7) * 224 + ((ST - 8) >> 3) * 32) * 2048 : W.pwd + (ST - G2_F) * 896 * 2048;
    g2_glds<SLOT * G2_ST + I * 8192>(src, I == 0 ? W.vw0 : ST < G2_F ? W.vw1 : W.vw2, W.ldsw);
}
template <int G> DEVI const char* g2_asrc(const G2St& T) {
    return G < G2_F ? T.pa + (size_t)G * 2 * SLAB_B : T.pad + (size_t)(G - G2_F) * 2 * SLAB_B; }
template <int G, int I> DEVI void g2_a(G2St& T) {
    asm volatile("global_load_dwordx4 %0, %1, %2 sc1" : "=v"(T.A[G][I]) : "v"(I ? T.va1 : T.va0), "s"(g2_asrc<G>(T)) : "memory");
}
template <int ST> DEVI bf16x4 g2_ldb(G2St& T, int n, int h) {
    constexpr int SLOT = ST % G2_NS;
    return __builtin_amdgcn_ds_read_tr16_b64_v4bf16((LAS bf16x4*)(T.pB[SLOT / 4][n & 3] + (SLOT % 4) * G2_ST + (n >> 2) * 8192 + h * 512));
}
DEVI bf16x8 cat(bf16x4 lo, bf16x4 hi) { return __builtin_shufflevector(lo, hi, 0, 1, 2, 3, 4, 5, 6, 7); }
template <int OFF> DEVI void flag_set(char* p, unsigned tag) { const unsigned zero = 0u; asm volatile("s_mov_b64 exec, 1\n\tglobal_store_dword %0, %1, %2 offset:%3 sc1\n\ts_mov_b64 exec, -1" :: "v"(zero), "v"(tag), "s"(p), "i"(OFF) : "memory"); }
template <int SLEEP> DEVI void flags_wait(u32x2 f, const char* base, unsigned voff, unsigned tag) {
#pragma unroll 1
    for (unsigned spin = 0; __any(f[0] != tag || f[1] != tag) && spin < SPIN_CAP; ++spin) {
        if (spin) __builtin_amdgcn_s_sleep(SLEEP);
        asm volatile("global_load_dwordx2 %0, %1, %2 sc1\n\ts_waitcnt vmcnt(0)" : "=&v"(f) : "v"(voff), "s"(base) : "memory");
    }
}
DEVI unsigned umax4(u32x4 v) { const unsigned a = v[0] > v[1] ? v[0] : v[1], b = v[2] > v[3] ? v[2] : v[3]; return a > b ? a : b; }
DEVI u32x4 never_ones(u32x4 w) {
#pragma unroll
    for (int e = 0; e < 4; ++e) w[e] = w[e] < 0xfffffffeu ? w[e] : 0xfffffffeu;
    return w;
}

template <int KG, int t>
DEVI void g2_stage(G2St& T) {
    constexpr bool TB = t >= G2_F, ISW = g2_wops(t) != 0, RL = t < 35, CHK = t == 4 || t == 12 || t == 20;
    constexpr int G0 = g2_grp(t), G1 = TB ? G0 + 1 : G0, NA = g2_alast(t) - g2_afirst(t), GN = NA ? g2_afirst(t) : 0, GN1 = NA == 2 ? GN + 1 : GN;
    u32x4 (&Aa)[2] = T.A[G0]; u32x4 (&Ab)[2] = T.A[G1];
    if (TB) asm volatile("s_waitcnt vmcnt(%4)" : "+v"(Aa[0]), "+v"(Aa[1]), "+v"(Ab[0]), "+v"(Ab[1]) : "i"(g2_wait(t)) : "memory");
    else if (CHK) asm volatile("s_waitcnt vmcnt(%3)" : "+v"(Aa[0]), "+v"(Aa[1]), "+v"(T.hf) : "i"(g2_wait(t)) : "memory");
    else asm volatile("s_waitcnt vmcnt(%2)" : "+v"(Aa[0]), "+v"(Aa[1]) : "i"(g2_wait(t)) : "memory");
    if (t == 0) flag_set<0>(T.flagA, T.tag);
    if (t == G2_FY) flag_set<128>(T.flagA, T.tag);
    if (t == G2_FZ) flag_set<0>(T.flagZ, T.tag);
    if (t < 8) {
#pragma unroll 1
        for (unsigned spin = 0; spin < SPIN_CAP; ++spin) {
            const unsigned a = umax4(Aa[0]), b = umax4(Aa[1]);
            if (!__any((a > b ? a : b) == 0xffffffffu)) break;
            __builtin_amdgcn_s_sleep(POLL_SLEEP);
            asm volatile("global_load_dwordx4 %0, %2, %4 sc1\n\tglobal_load_dwordx4 %1, %3, %4 sc1\n\ts_waitcnt vmcnt(0)" : "=&v"(Aa[0]), "=&v"(Aa[1]) : "v"(T.va0), "v"(T.va1), "s"(g2_asrc<G0>(T)) : "memory");
        }
    }
    if (KG == 0 && CHK && T.poller) flags_wait<POLL_SLEEP>(T.hf, t == 12 ? T.flags + 128 : T.flags, t == 20 ? T.foz : T.foa, T.tag);
    if (t == G2_F) WAIT_LGKM0();
    { MEMFENCE(); SBAR(); BAR(); MEMFENCE(); SBAR(); }
    f32x4 ho[4];
    if (t == G2_F) {
#pragma unroll
        for (int j = 0; j < 4; ++j) ho[j] = *(LAS const f32x4*)(T.xr + j * 1024);
        SBAR();
    }
#define AB2DS true
#define RL2(n, h) do { if (AB2DS && RL && (n) >= 0) { T.B[(n) >= 0 ? (n) : 0][h] = g2_ldb<t + 1>(T, (n) >= 0 ? (n) : 0, h); SBAR(); } } while (0)
#define P2(n) do { constexpr int c_ = TB ? ((n) & 3) : (n); u32x4 (&A_)[2] = (n) < 4 ? Aa : Ab; const bf16x8 b_ = cat(T.B[n][0], T.B[n][1]); \
                   if (t == 0) MFMA_NEW(T.acc[(n) & 1][c_], b_, A_[(n) & 1]); else MFMA_ACC(T.acc[(n) & 1][c_], b_, A_[(n) & 1]); SBAR(); RL2((n) - 1, 1); D((n) * 2); \
                   if (t == 0) MFMA_NEW(T.acc[~(n) & 1][c_], b_, A_[~(n) & 1]); else MFMA_ACC(T.acc[~(n) & 1][c_], b_, A_[~(n) & 1]); SBAR(); RL2(n, 0); D((n) * 2 + 1); } while (0)
#define VW(i) do { if (ISW) { g2_w<ISW ? t + G2_WL : 0, i>(T.W); SBAR(); } } while (0)
#define VA(i) do { if (NA == 2) { g2_a<(i) < 2 ? GN : GN1, (i) & 1>(T); SBAR(); } else if (NA && ((i) & 1)) { g2_a<GN, (i) / 2>(T); SBAR(); } } while (0)
    f32x4& dg = T.dg[(t >> 2) & 1][t & 3]; f32x4& du = T.du[(t >> 2) & 1][t & 3];
#define D(i) do { if (t < 8 && (i) < 13) { asm volatile("" : "+v"(dg)); \
        if ((i) == 0) dg = dg * -1.4426950408889634f; \
        else if ((i) < 5) dg[((i) - 1) & 3] = __builtin_amdgcn_exp2f(dg[((i) - 1) & 3]); \
        else if ((i) == 5) dg = dg + 1.0f; \
        else if ((i) < 10) dg[((i) - 6) & 3] = __builtin_amdgcn_rcpf(dg[((i) - 6) & 3]); \
        else if ((i) == 10) dg = dg * du; \
        else { T.hq[(t & 1) * 2 + (((i) - 11) & 1)] = pk_bf16(dg[(((i) - 11) & 1) * 2], dg[(((i) - 11) & 1) * 2 + 1]); asm volatile("" : "+v"(T.hq[(t & 1) * 2 + (((i) - 11) & 1)])); } \
        if ((i) < 11) asm volatile("" : "+v"(dg)); \
        SBAR(); } } while (0)
    if (KG == 1) { VW(0); VW(1); VA(0); VA(1); VA(2); VA(3); P2(0); P2(1); P2(2); P2(3); P2(4); P2(5); P2(6); P2(7); }
    else { P2(0); P2(1); P2(2); P2(3); VW(0); P2(4); VW(1); P2(5); VA(0); VA(1); P2(6); VA(2); VA(3); P2(7); }
    RL2(7, 1);
#undef RL2
#undef VW
#undef VA
#undef D
#undef P2
    if (t < 8 && (t & 1)) {
        auto r0 = __builtin_amdgcn_permlane16_swap(T.hq[0], T.hq[2], false, false);
        auto r1 = __builtin_amdgcn_permlane16_swap(T.hq[1], T.hq[3], false, false);
        const u32x4 w = {r0[0], r1[0], r0[1], r1[1]};
        asm volatile("global_store_dwordx4 %0, %1, %2 offset:%3 sc1\n\ts_nop 1" :: "v"(T.hl), "v"(w), "s"(T.dd[(t >> 2) & 1]), "i"((t & 2) * 1024) : "memory");
        SBAR();
    }
    if (t == G2_PA || t == G2_PY || t == G2_PZ) {
        asm volatile("global_load_dwordx2 %0, %1, %2 offset:%3 sc1" : "=v"(T.hf) : "v"(t == G2_PZ ? T.foz : T.foa), "s"(T.flags), "i"(t == G2_PY ? 128 : 0) : "memory"); SBAR();
    }
    if (t == G2_F - 1) {
        MFMA_DRAIN(); SBAR();
#pragma unroll
        for (int j = 0; j < 4; ++j) *(LAS f32x4*)(T.xw + j * 1024) = T.acc[1][4 + j];
        SBAR();
    }
    if (t == G2_F) {
#pragma unroll
        for (int j = 0; j < 4; ++j) {
            const u32x4 w = never_ones(__builtin_bit_cast(u32x4, T.acc[0][4 + j] + ho[j]));
            asm volatile("global_store_dwordx4 %0, %1, off offset:%2 sc1\n\ts_nop 1" :: "v"(T.snd), "v"(w), "i"(j * 1024) : "memory");
        }
        SBAR();
    }
    if (t == G2_XST) { asm volatile("global_load_dwordx4 %0, %2, off\n\tglobal_load_dwordx4 %1, %2, off offset:64" : "=&v"(T.xres[0]), "=&v"(T.xres[1]) : "v"(T.xp) : "memory"); SBAR(); }
    if (t == G2_RCV) {
        asm volatile("global_load_dwordx4 %0, %4, off sc1\n\tglobal_load_dwordx4 %1, %4, off offset:1024 sc1\n\tglobal_load_dwordx4 %2, %4, off offset:2048 sc1\n\tglobal_load_dwordx4 %3, %4, off offset:3072 sc1"
                     : "=&v"(T.rc[0]), "=&v"(T.rc[1]), "=&v"(T.rc[2]), "=&v"(T.rc[3]) : "v"(T.rcv) : "memory");
        SBAR();
    }
}
template <int KG, int t, int E> DEVI void g2_run(G2St& T) { g2_stage<KG, t>(T); if constexpr (t + 1 < E) g2_run<KG, t + 1, E>(T); }

DEVI unsigned bsel(unsigned m, unsigned a, unsigned b) { return (m & a) | (~m & b); }
typedef short s16x4 __attribute__((ext_vector_type(4)));
#define MFMA16K16(a, b, c) __builtin_amdgcn_mfma_f32_16x16x16bf16_1k(__builtin_bit_cast(s16x4, a), __builtin_bit_cast(s16x4, b), (c), 0, 0, 0)
template <bool MAX> DEVI float rows_reduce(const float (&v)[4]) {
    auto a = __builtin_amdgcn_permlane16_swap(__float_as_uint(v[0]), __float_as_uint(v[1]), false, false);
    auto b = __builtin_amdgcn_permlane16_swap(__float_as_uint(v[2]), __float_as_uint(v[3]), false, false);
    const float t = MAX ? fmaxf(__uint_as_float(a[0]), __uint_as_float(a[1])) : __uint_as_float(a[0]) + __uint_as_float(a[1]);
    const float u = MAX ? fmaxf(__uint_as_float(b[0]), __uint_as_float(b[1])) : __uint_as_float(b[0]) + __uint_as_float(b[1]);
    auto c = __builtin_amdgcn_permlane32_swap(__float_as_uint(t), __float_as_uint(u), false, false);
    return MAX ? fmaxf(__uint_as_float(c[0]), __uint_as_float(c[1])) : __uint_as_float(c[0]) + __uint_as_float(c[1]);
}
DEVI void rows_spread(float w, float (&v)[4]) {
    auto c = __builtin_amdgcn_permlane32_swap(__float_as_uint(w), __float_as_uint(w), false, false);
    auto a = __builtin_amdgcn_permlane16_swap(c[0], c[0], false, false);
    auto b = __builtin_amdgcn_permlane16_swap(c[1], c[1], false, false);
    v[0] = __uint_as_float(a[0]); v[1] = __uint_as_float(a[1]); v[2] = __uint_as_float(b[0]); v[3] = __uint_as_float(b[1]);
}

template <int GRP>
DEVI void g1_body(G1Ld& L, G2St& T, LAS char* lds, char* ws, int rt, int ct, int wm, int kh STAMP_ARGS) {
    G1Ptr P;
    {
        const int lane = fresh_lane(), g = lane >> 4, q = (lane & 15) >> 2, p = lane & 3;
        const unsigned ldsb = (unsigned)(size_t)lds;
        P.pA = ldsb + wm * 4096 + lane * 16;
        P.pH = ldsb + lane * 16;
        const int brow = (16 * (g >> 1) + 4 * (g & 1) + q) * 224 + p * 8;
        P.pB01 = ldsb + G1_BOFF + brow;
        P.pB23 = P.pB01 + 2 * G1_BST;
        P.pBh01 = P.pB01 + (2 + wm) * 32;
        P.pBh23 = P.pB23 + (2 + wm) * 32;
    }
    f32x4 acc[4][10], acch = {0.f, 0.f, 0.f, 0.f};
#pragma unroll
    for (int m = 0; m < 4; ++m)
#pragma unroll
        for (int n = 0; n < 10; ++n) acc[m][n] = (f32x4){0.f, 0.f, 0.f, 0.f};

    if (GRP == 1 && wm == 3) g1_p2<0>(L);
    g1_pa<0>(L);
    if (GRP == 1 && wm == 3) g1_p2<1>(L);
    g1_pa<1>(L);
#pragma unroll
    for (int m = 0; m < 4; ++m)
        asm volatile("" : "+v"(acc[m][0]), "+v"(acc[m][1]), "+v"(acc[m][2]), "+v"(acc[m][3]), "+v"(acc[m][4]), "+v"(acc[m][5]), "+v"(acc[m][6]), "+v"(acc[m][7]), "+v"(acc[m][8]), "+v"(acc[m][9]), "+v"(acch));
    asm volatile("s_waitcnt vmcnt(2)\n\ts_barrier" ::: "memory", FRAG_REGS);
    g1_ldb<GRP, 0, 0, 0>(P); g1_ldb<GRP, 0, 0, 1>(P); g1_ldb<GRP, 0, 1, 0>(P); g1_ldb<GRP, 0, 1, 1>(P); g1_ldb<GRP, 0, 2, 0>(P); g1_ldb<GRP, 0, 2, 1>(P);
    g1_ldb<GRP, 0, 3, 0>(P); g1_ldb<GRP, 0, 3, 1>(P); g1_ldb<GRP, 0, 4, 0>(P); g1_ldb<GRP, 0, 4, 1>(P);
    if (GRP == 0) { g1_ldah<0>(P); g1_ldbh<0, 0>(P); g1_ldbh<0, 1>(P); }
    g1_lda<0, 0>(P); g1_lda<0, 1>(P);
    g1_ldb<GRP, 0, 5, 0>(P); g1_ldb<GRP, 0, 5, 1>(P); g1_ldb<GRP, 0, 6, 0>(P); g1_ldb<GRP, 0, 6, 1>(P); g1_ldb<GRP, 0, 7, 0>(P); g1_ldb<GRP, 0, 7, 1>(P);
    g1_lda<0, 2>(P); g1_lda<0, 3>(P);
    g1_issue<2>(L);
    if (GRP == 1) g1_issue<3>(L);
    STAMP_SEG(0);
#pragma unroll 1
    for (int it = 0; it < 8; ++it) {
        g1_stage<GRP, 0>(acc, acch, L, it == 7, P);
        if (GRP == 0) g1_tail<0>(L, it == 7);
        g1_stage<GRP, 1>(acc, acch, L, it == 7, P);
        g1_stage<GRP, 2>(acc, acch, L, it == 7, P);
        g1_stage<GRP, 3>(acc, acch, L, it == 7, P);
    }
    asm volatile("s_waitcnt vmcnt(0) lgkmcnt(0)\n\ts_nop 15" ::: "memory", FRAG_REGS);
    SBAR();
    STAMP_SEG(0);

    const int lane = fresh_lane(), g = lane >> 4, i16 = lane & 15;
    char* ho = ws + WS_HO;
    const int mb0 = 16 * rt + 4 * wm;
    const unsigned hl = (unsigned)((g & 1) * 1024 + (g >> 1) * 256 + i16 * 16);
    G2W& W = T.W;
    {
        const int wid = GRP * 4 + wm, r = lane >> 3, c = (lane & 7) ^ ((r & 2) | ((wid & 1) << 2));
        W.vw0 = (unsigned)((8 * wid + r) * 2048 + c * 16 + kh * 128);
        W.vw1 = W.vw0 ^ 128;
        W.vw2 = W.vw0 + 448 * 2048;
    }
    char* hs = ho + (size_t)(64 * (ct >> 4) + (ct & 15)) * SLAB_B + (size_t)mb0 * 1024;
#define GW(i) do { if ((i) < 2 * G2_WL) { g2_w<(i) / 2, (i) % 2>(W); SBAR(); } } while (0)
#define GLU1(n, m) glu_store(acc[m][n], acc[m][5 + (n)], acc[(m) + 1][n], acc[(m) + 1][5 + (n)], hs + (size_t)(16 * (1 + (n) / 2)) * SLAB_B + ((n) & 1) * 512 + (m) * 1024 + hl)
    if (GRP == 1) {
        SBAR(); BAR(); MEMFENCE(); SBAR();
        GW(0); GW(1); GW(2); GW(3); GLU1(0, 0); GW(4); GW(5); GLU1(0, 2); GW(6); GW(7); GLU1(1, 0); GW(8); GW(9); GLU1(1, 2); GW(10); GW(11); GLU1(2, 0); GW(12); GW(13); GLU1(2, 2); GW(14); GW(15);
        asm volatile("s_waitcnt vmcnt(%0)" :: "i"(2 * G2_WL + 4) : "memory");
#pragma unroll
        for (int m = 0; m < 4; ++m) { T.dg[0][m] = acc[m][3]; T.dg[1][m] = acc[m][4]; T.du[0][m] = acc[m][8]; T.du[1][m] = acc[m][9]; }
        T.dd[0] = hs + (size_t)32 * SLAB_B + 512; T.dd[1] = hs + (size_t)48 * SLAB_B;
    } else {
        u32x4 Qf[4], Kf[4]; u32x2 Vh[4][2];
#pragma unroll
        for (int m = 0; m < 4; ++m) {
            Qf[m] = (u32x4){pk_bf16(acc[m][0][0], acc[m][0][1]), pk_bf16(acc[m][0][2], acc[m][0][3]), pk_bf16(acc[m][1][0], acc[m][1][1]), pk_bf16(acc[m][1][2], acc[m][1][3])};
            Kf[m] = (u32x4){pk_bf16(acc[m][2][0], acc[m][2][1]), pk_bf16(acc[m][2][2], acc[m][2][3]), pk_bf16(acc[m][3][0], acc[m][3][1]), pk_bf16(acc[m][3][2], acc[m][3][3])};
#pragma unroll
            for (int b = 0; b < 2; ++b) Vh[m][b] = (u32x2){pk_bf16(acc[m][4 + b][0], acc[m][4 + b][1]), pk_bf16(acc[m][4 + b][2], acc[m][4 + b][3])};
        }
        LAS char* xs = lds + G1_XOFF;
        if (wm < 3) {
            *(LAS u32x4*)(xs + (wm + 1) * 2048 + lane * 16) = Kf[3];
            *(LAS u32x4*)(xs + (wm + 1) * 2048 + 1024 + lane * 16) = (u32x4){Vh[3][0][0], Vh[3][0][1], Vh[3][1][0], Vh[3][1][1]};
        }
        {
            const unsigned h0 = pk_bf16(acch[0], acch[1]), h1 = pk_bf16(acch[2], acch[3]);
            if (wm < 2) {
                *(LAS u32x2*)(xs + lane * 16 + wm * 8) = (u32x2){h0, h1};
            } else {
                LAS char* t = xs + 1024 + ((i16 >> 2) * 16 + 4 * g) * 16 + (wm - 2) * 8 + (i16 & 3) * 2;
                *(LAS u16*)(t) = (u16)h0; *(LAS u16*)(t + 16) = (u16)(h0 >> 16); *(LAS u16*)(t + 32) = (u16)h1; *(LAS u16*)(t + 48) = (u16)(h1 >> 16);
            }
        }
        asm volatile("s_waitcnt lgkmcnt(0)" ::: "memory");
        SBAR(); BAR(); MEMFENCE(); SBAR();
        GW(0); GW(1);
        const u32x4 Kp = *(LAS const u32x4*)(xs + wm * 2048 + lane * 16);
        const u32x4 Vp = *(LAS const u32x4*)(xs + wm * 2048 + 1024 + lane * 16);
        const int dc = i16 - 4 * g - (i16 & 1);
        unsigned modd = (i16 & 1) ? ~0u : 0u, mc0 = dc >= 0 ? ~0u : 0u, mc1 = dc >= 2 ? ~0u : 0u;
        asm volatile("" : "+v"(modd), "+v"(mc0), "+v"(mc1));
        const unsigned half = (modd & 0xffff0000u) | (~modd & 0x0000ffffu);
        const unsigned pp0 = half & ~mc0, pp1 = half & ~mc1, pc0 = half & mc0, pc1 = half & mc1;
        const float sc = 0.17677669529663687f * 1.4426950408889634f;
        const f32x4 z = {0.f, 0.f, 0.f, 0.f};
        float e0[4], e1[4], red[4];
#pragma unroll
        for (int m = 0; m < 4; ++m) {
            u32x4 s0 = __builtin_bit_cast(u32x4, MFMA16(as_frag(m ? Kf[m ? m - 1 : 0] : Kp), as_frag(Qf[m]), z));
            const u32x4 s1 = __builtin_bit_cast(u32x4, MFMA16(as_frag(Kf[m]), as_frag(Qf[m]), z));
            if (m == 0 && rt == 0 && wm == 0) { const unsigned c = __float_as_uint(-1e9f / 0.17677669529663687f); s0 = (u32x4){c, c, c, c}; }
            e0[m] = __uint_as_float(bsel(modd, bsel(mc0, s1[1], s0[1]), bsel(mc0, s1[0], s0[0])));
            e1[m] = __uint_as_float(bsel(modd, bsel(mc1, s1[3], s0[3]), bsel(mc1, s1[2], s0[2])));
            red[m] = fmaxf(e0[m], e1[m]);
        }
        {
            rows_spread(rows_reduce<true>(red), red);
        }
#pragma unroll
        for (int m = 0; m < 4; ++m) {
            e0[m] = __builtin_amdgcn_exp2f((e0[m] - red[m]) * sc); e1[m] = __builtin_amdgcn_exp2f((e1[m] - red[m]) * sc);
            red[m] = e0[m] + e1[m];
        }
        {
            float w = __builtin_amdgcn_rcpf(rows_reduce<false>(red));
            rows_spread(w, red);
        }
        char* od = hs + (((g & 1) << 1) | (g >> 1)) * 256 + i16 * 16;
#pragma unroll
        for (int m = 0; m < 4; ++m) {
            const float p0 = e0[m] * red[m], p1 = e1[m] * red[m];
            const unsigned w0 = pk_bf16(p0, p0), w1 = pk_bf16(p1, p1);
            const u32x2 Pp = {w0 & pp0, w1 & pp1}, Pc = {w0 & pc0, w1 & pc1};
            const u32x2 Vp0 = m ? Vh[m ? m - 1 : 0][0] : (u32x2){Vp[0], Vp[1]}, Vp1 = m ? Vh[m ? m - 1 : 0][1] : (u32x2){Vp[2], Vp[3]};
            const f32x4 o0 = MFMA16K16(Vh[m][0], Pc, MFMA16K16(Vp0, Pp, z));
            const f32x4 o1 = MFMA16K16(Vh[m][1], Pc, MFMA16K16(Vp1, Pp, z));
            unsigned x0 = pk_bf16(o0[0], o0[1]), x1 = pk_bf16(o0[2], o0[3]), y0 = pk_bf16(o1[0], o1[1]), y1 = pk_bf16(o1[2], o1[3]);
            auto r0 = __builtin_amdgcn_permlane16_swap(x0, y0, false, false);
            auto r1 = __builtin_amdgcn_permlane16_swap(x1, y1, false, false);
            st16(od + m * 1024, never_ones((u32x4){r0[0], r1[0], r0[1], r1[1]}));
        }
        SBAR();
        GW(2); GW(3); GW(4); GW(5); GW(6); GW(7); GW(8); GW(9); GW(10); GW(11); GW(12); GW(13); GW(14); GW(15);
        asm volatile("s_waitcnt vmcnt(%0)" :: "i"(2 * G2_WL + 2) : "memory");
#pragma unroll
        for (int m = 0; m < 4; ++m) { T.dg[0][m] = acc[m][6]; T.dg[1][m] = acc[m][7]; T.du[0][m] = acc[m][8]; T.du[1][m] = acc[m][9]; }
        T.dd[0] = hs + (size_t)48 * SLAB_B + 512; T.dd[1] = ho + (size_t)(128 + (ct >> 1)) * SLAB_B + (ct & 1) * 512 + (size_t)mb0 * 1024;
        if (blockIdx.x == 0 && wm == 1) {
            flags_wait<POLL_SLEEP>((u32x2){~T.tag, 0u}, ws + WS_LF, (unsigned)(g * 8192 + i16 * 8), T.tag);
            flags_wait<POLL_SLEEP>((u32x2){~T.tag, 0u}, ws + WS_LF + 32768, (unsigned)(g * 8192 + i16 * 8), T.tag);
            flag_set<0>(ws + WS_EP, T.tag);
        }
    }
#undef GW
#undef GLU1
    STAMP_SEG(2);
}

DEVI float sum_x8(float v) {
    v += __uint_as_float(__builtin_amdgcn_update_dpp(0u, __float_as_uint(v), 0x128  , 0xf, 0xf, false));
    { auto t = __builtin_amdgcn_permlane16_swap(__float_as_uint(v), __float_as_uint(v), false, false); v = __uint_as_float(t[0]) + __uint_as_float(t[1]); }
    { auto t = __builtin_amdgcn_permlane32_swap(__float_as_uint(v), __float_as_uint(v), false, false); v = __uint_as_float(t[0]) + __uint_as_float(t[1]); }
    return v;
}

template <int KG>
DEVI void g2_body(G2St& T, LAS char* lds, char* ws, const u16* x, u16* out, int rt, int ct, int wm STAMP_ARGS2) {
    constexpr int kg = KG;
    const int bid = blockIdx.x, idx = bid >> 3, wid = KG * 4 + wm;
    const int kh = (bid >> 2) & 1, side = kh ^ 1, rt2 = 2 * rt + ((bid >> 1) & 1), ct2 = 4 * (bid & 1) + (idx & 3);
    const char* flq = ws + WS_FL + (rt * 4 + (bid & 2)) * 512;
    const int lane2 = fresh_lane(), g = lane2 >> 4, i16 = lane2 & 15, q = i16 >> 2, p = lane2 & 3;
    {
        const char* a0 = ws + WS_HO + (size_t)kg * SLAB_B + (size_t)(8 * rt2 + 2 * wm) * 1024;
        T.pa = a0 + (size_t)(64 * side) * SLAB_B;
        T.pad = a0 + (size_t)128 * SLAB_B;
        T.va0 = lane2 * 16 + kg * 1024; T.va1 = T.va0 ^ 1024;
        T.flagA = ws + WS_FL + (kg ^ 1) * 16384 + (rt * 4 + wm) * 512 + ct * 4;
        T.flagZ = ws + WS_FL + (rt * 4 + wm) * 512 + 256 + kg * 128 + ct * 4;
        T.hl = (unsigned)((g & 1) * 1024 + (g >> 1) * 256 + i16 * 16);
        T.poller = wid == 0;
        T.flags = T.poller ? flq : ws + WS_XB + (size_t)bid * 32768;
        T.foa = (unsigned)((lane2 >> 5) * 512 + side * 64 + (lane2 & 7) * 8);
        T.foz = (unsigned)((lane2 >> 5) * 512 + 256 + (lane2 & 31) * 8);
    }
#pragma unroll
    for (int n = 0; n < 4; ++n) {
        T.pB[0][n] = lds + kg * 4096 + (8 * g + q) * 128 + (((2 * n + (p >> 1)) ^ ((q & 2) | ((g & 1) << 2))) * 16) + (p & 1) * 8;
#pragma unroll
        for (int k = 1; k < (G2_NS + 3) / 4; ++k) { T.pB[k][n] = T.pB[0][n] + k * 4 * G2_ST; asm volatile("" : "+v"(T.pB[k][n])); }
        asm volatile("" : "+v"(T.pB[0][n]));
    }
    MEMFENCE(); SBAR(); BAR(); MEMFENCE(); SBAR();
    g2_a<0, 0>(T); g2_a<0, 1>(T); g2_a<1, 0>(T); g2_a<1, 1>(T);
    SBAR();
#pragma unroll
    for (int n = 0; n < 8; ++n) { T.B[n][0] = g2_ldb<0>(T, n, 0); T.B[n][1] = g2_ldb<0>(T, n, 1); }
    SBAR();
    STAMP_SEG(3);
    g2_run<KG, 0, 8>(T);
    STAMP_SEG(4);
    g2_run<KG, 8, 24>(T);
    const int orow = 128 * rt2 + 32 * wm + 16 * kg + i16, ocol = 128 * ct2 + 64 * kh + (g & 1) * 16 + (g >> 1) * 8;
    T.xw = lds + G2_XOFF + wid * 4096 + lane2 * 16;
    T.xr = lds + G2_XOFF + (wid ^ 4) * 4096 + lane2 * 16;
    T.snd = ws + WS_XB + (size_t)bid * 32768 + wid * 4096 + lane2 * 16;
    T.rcv = ws + WS_XB + (size_t)(bid ^ 4) * 32768 + wid * 4096 + lane2 * 16;
    T.xp = x + (size_t)orow * D_ + ocol;
    g2_run<KG, 24, 36>(T);
    STAMP_SEG(5);

    MFMA_DRAIN(); SBAR();
#pragma unroll
    for (int j = 0; j < 4; ++j) *(LAS f32x4*)(T.xw + j * 1024) = T.acc[1][j];
    WAIT_LGKM0();
    MEMFENCE(); SBAR(); BAR(); MEMFENCE(); SBAR();
#pragma unroll
    for (int j = 0; j < 4; ++j) T.acc[0][j] += *(LAS const f32x4*)(T.xr + j * 1024);
    asm volatile("s_waitcnt vmcnt(0)" : "+v"(T.xres[0]), "+v"(T.xres[1]), "+v"(T.rc[0]), "+v"(T.rc[1]), "+v"(T.rc[2]), "+v"(T.rc[3]) :: "memory");
    for (unsigned spin = 0;; ++spin) {
        const unsigned mx = umax4((u32x4){umax4(T.rc[0]), umax4(T.rc[1]), umax4(T.rc[2]), umax4(T.rc[3])});
        if (!__any(mx == 0xffffffffu) || spin > SPIN_CAP) break;
        __builtin_amdgcn_s_sleep(4);
        asm volatile("global_load_dwordx4 %0, %4, off sc1\n\tglobal_load_dwordx4 %1, %4, off offset:1024 sc1\n\tglobal_load_dwordx4 %2, %4, off offset:2048 sc1\n\tglobal_load_dwordx4 %3, %4, off offset:3072 sc1\n\ts_waitcnt vmcnt(0)"
                     : "=&v"(T.rc[0]), "=&v"(T.rc[1]), "=&v"(T.rc[2]), "=&v"(T.rc[3]) : "v"(T.rcv) : "memory");
    }
    u16* orp = out + (size_t)orow * D_ + ocol;
#pragma unroll
    for (int j = 0; j < 4; j += 2) {
        const f32x4 s0 = T.acc[0][j] + __builtin_bit_cast(f32x4, T.rc[j]), s1 = T.acc[0][j + 1] + __builtin_bit_cast(f32x4, T.rc[j + 1]);
        float v[8];
#pragma unroll
        for (int e = 0; e < 4; ++e) { auto r = __builtin_amdgcn_permlane16_swap(__float_as_uint(s0[e]), __float_as_uint(s1[e]), false, false); v[e] = __uint_as_float(r[0]); v[4 + e] = __uint_as_float(r[1]); }
        const u32x4 xv = T.xres[j >> 1];
        u32x4 o;
#pragma unroll
        for (int e = 0; e < 4; ++e) o[e] = pk_bf16(v[2 * e] + bf_lo(xv[e]), v[2 * e + 1] + bf_hi(xv[e]));
        *(GAS u32x4*)(size_t)(orp + 16 * j) = o;
    }
    STAMP_SEG(6);
    STAMP_OUT(ws, 0, wid, lane2);
    STAMP_OUT(ws, 1, wid, lane2);
}

__global__ __launch_bounds__(512, 2) void k_main(const char* __restrict__ wqkv, const char* __restrict__ wg, const char* __restrict__ wu, const char* __restrict__ wo, const char* __restrict__ wd, char* __restrict__ ws,
                                                const u16* __restrict__ x, u16* __restrict__ out) {
    extern __shared__ __attribute__((aligned(16))) char smem[];
    TL_ENTRY();
    asm volatile("" : "+s"(wqkv), "+s"(wg), "+s"(wu), "+s"(wo), "+s"(wd), "+s"(ws), "+s"(x), "+s"(out));
    LAS char* lds = (LAS char*)smem;
    const int tid = threadIdx.x, lane = tid & 63;
    const int wid = __builtin_amdgcn_readfirstlane(tid >> 6);
    const int bid = blockIdx.x, xcd = bid & 7, idx = bid >> 3;
    const int ct = 4 * xcd + (idx & 3), rt = idx >> 2;
    const unsigned ldsb = (unsigned)(size_t)lds;
    const int r8 = lane & 7, c8 = lane >> 3, row = 256 * rt + 8 * ct + r8;
    u32x4 xv[2];
    unsigned ep;
    asm volatile("global_load_dwordx4 %0, %3, %4\n\tglobal_load_dwordx4 %1, %3, %4 offset:128\n\ts_load_dword %2, %5, 0x0"
                 : "=&v"(xv[0]), "=&v"(xv[1]), "=&s"(ep) : "v"((unsigned)(row * 2048 + (16 * wid + c8) * 16)), "s"(x), "s"(ws + WS_EP) : "memory");
    G1Ld L;
    {
        const int c = wid * 64 + lane, rho = c / 14, cc = c - rho * 14;
        const int k = (rho & ~12) | ((rho & 4) << 1) | ((rho & 8) >> 1);
        const int c2 = cc < 12 ? cc : 11;
        const bool last = wid == 7;
        L.pa = ws + WS_XN + (size_t)(16 * rt + 2 * wid) * 1024;
        L.stepa = (unsigned)SLAB_B;
        L.va = lane * 16;
        L.la = ldsb + (1 + 2 * wid) * 1024;
        L.p2 = last ? ws + WS_XN + (size_t)(rt == 0 ? 0 : 16 * rt - 1) * 1024 : wqkv + ct * 64;
        L.step2 = last ? (unsigned)SLAB_B : 32u * 3072u * 2u;
        L.v2 = last ? (unsigned)((rt == 0 ? (lane & 48) : lane) * 16) : (unsigned)(k * 6144 + (c2 >> 2) * 2048 + (c2 & 3) * 16);
        L.l2 = last ? ldsb : ldsb + G1_BOFF + wid * 1024;
        L.s2 = last ? G1_AST : G1_BST;
        L.pg = wg + ct * 224;
        L.pu = wu + ct * 224;
        L.stepg = 32u * 3584u * 2u;
        L.vg = last ? 0u : (unsigned)(k * 7168 + cc * 16);
        L.lg = last ? ldsb + G1_BOFF + 192 : ldsb + G1_BOFF + G1_REG + wid * 1024;
        L.lu = last ? ldsb + G1_BOFF + 192 : ldsb + G1_BOFF + 2 * G1_REG + wid * 1024;
        L.ma = ~0ull; L.m2 = ~0ull; L.mg = last ? 1ull : ~0ull;
    }
    L.warm = wid < 4 ? wo + bid * 8192 + wid * 2048 : wd + bid * 28672 + (wid & 3) * 7168;
    L.lw = ldsb + G1_XOFF + (wid & 3) * 1024; L.ldum = ldsb + G1_BOFF + 192;
    L.pf = ws + WS_XB + (size_t)bid * 32768 + (wid & 3) * 8192;
    L.ts = wid == 0 ? 3072u : wid == 3 ? 1024u : wid == 7 ? 2048u : wid >= 5 ? 8192u : 3584u;
    L.tb = (wid == 0 ? wqkv : wid == 1 ? wg : wid == 2 ? wu : wid == 3 ? wo : wid == 4 ? wd : wid == 5 ? ws : wid == 6 ? ws + (16u << 20) : (const char*)out) + (size_t)idx * 64 * L.ts;
    const int kh = (bid >> 2) & 1, side = kh ^ 1, rt2 = 2 * rt + ((bid >> 1) & 1), ct2 = 4 * (bid & 1) + (idx & 3);
    const int kg = wid >> 2, wm = wid & 3;
    G2St T;
    T.W.pwo = wo + (size_t)(side * 512) * 2048 + ct2 * 256;
    T.W.pwh = wd + (size_t)(side * 1792 + kg * 80) * 2048 + ct2 * 256;
    T.W.pwd = wd + (size_t)(96 + (wid >> 1) * 96) * 2048 + ct2 * 256;
    T.W.ldsw = ldsb + wid * 1024;
    STAMP_DECL STAMP_START();
    if (wid < 7) { g1_p2<0>(L); g1_pg<0>(L); g1_pu<0>(L); asm volatile("s_waitcnt vmcnt(3)" ::: "memory"); }
    else asm volatile("s_waitcnt vmcnt(0)" ::: "memory");
    asm volatile("s_waitcnt lgkmcnt(0)" : "+v"(xv[0]), "+v"(xv[1]), "+s"(ep) :: "memory");
    T.tag = ep * 1664525u + 1013904223u;
    P0S(2);
    {
        LAS float* red = (LAS float*)(lds + G1_XOFF);
        f32x2 f[8];
#pragma unroll
        for (int j = 0; j < 8; ++j) f[j] = (f32x2){bf_lo(xv[j >> 2][j & 3]), bf_hi(xv[j >> 2][j & 3])};
        {
            const u32x4 ones = {0xffffffffu, 0xffffffffu, 0xffffffffu, 0xffffffffu};
            asm volatile("global_store_dwordx4 %0, %1, %2 sc1\n\tglobal_store_dwordx4 %0, %1, %2 offset:1024 sc1\n\ts_nop 1"
                         :: "v"(L.va), "v"(ones), "s"(ws + WS_HO + (size_t)(64 * (ct >> 4) + (ct & 15)) * SLAB_B + (size_t)(16 * rt + 2 * wid) * 1024) : "memory");
        }
        const f32x2 s2 = ((f[0] + f[1]) + (f[2] + f[3])) + ((f[4] + f[5]) + (f[6] + f[7]));
        const float mw = sum_x8(s2[0] + s2[1]) * (1.0f / 128);
        f32x2 q2 = {0.f, 0.f};
#pragma unroll
        for (int j = 0; j < 8; ++j) { const f32x2 d = f[j] - mw; q2 = __builtin_elementwise_fma(d, d, q2); }
        const float qw = sum_x8(q2[0] + q2[1]);
        if (c8 == 0) *(LAS f32x2*)(red + r8 * 16 + wid * 2) = (f32x2){mw, qw};
        WAIT_LGKM0(); MEMFENCE(); BAR(); MEMFENCE();
        f32x4 st[4];
#pragma unroll
        for (int k = 0; k < 4; ++k) st[k] = *(LAS const f32x4*)(red + r8 * 16 + k * 4);
        const float mean = (((st[0][0] + st[0][2]) + (st[1][0] + st[1][2])) + ((st[2][0] + st[2][2]) + (st[3][0] + st[3][2]))) * 0.125f;
        float qt = 0.f, dt = 0.f;
#pragma unroll
        for (int k = 0; k < 4; ++k) { const float d0 = st[k][0] - mean, d1 = st[k][2] - mean; qt += st[k][1] + st[k][3]; dt = fmaf(d0, d0, fmaf(d1, d1, dt)); }
        const float mb = rbf(mean);
        const float vb = rbf(fmaf(dt, 128.f, qt) * (1.0f / D_));
        const float den = rbf(__builtin_amdgcn_sqrtf(rbf(vb + 1e-5f)));
        const float r0 = __builtin_amdgcn_rcpf(den), rc = fmaf(fmaf(-den, r0, 1.0f), r0, r0);
#pragma unroll
        for (int i = 0; i < 2; ++i) {
            u32x4 o;
#pragma unroll
            for (int j = 0; j < 4; ++j) {
                const f32x2 c = f[i * 4 + j] - mb;
                const unsigned d = pk_bf16(c[0], c[1]);
                const f32x2 y = (f32x2){bf_lo(d), bf_hi(d)} * rc;
                o[j] = pk_bf16(y[0], y[1]);
            }
            const int ch = 16 * wid + 8 * i + c8;
            asm volatile("global_store_dwordx4 %0, %1, %2 sc1\n\ts_nop 1" :: "v"((unsigned)((ch >> 2) * (int)SLAB_B + (row >> 4) * 1024 + ((ch & 3) * 16 + (row & 15)) * 16)), "v"(o), "s"(ws + WS_XN) : "memory");
        }
        P0S(3);
        asm volatile("s_waitcnt vmcnt(0)\n\ts_barrier" ::: "memory");
        P0S(4);
        if (wid < 7) {
            if (!W_EARLY) { g1_p2<0>(L); g1_pg<0>(L); g1_pu<0>(L); }
            g1_p2<1>(L); g1_pg<1>(L); g1_pu<1>(L);
        } else {
            const bool next = ct >= 30 && rt < 7;
            asm volatile("s_mov_b64 exec, %3\n\tglobal_store_dword %0, %1, %2 sc1\n\ts_mov_b64 exec, -1"
                         :: "v"((unsigned)((rt * 32 + lane) * 256 + (lane < 32 ? ct : ct + 2) * 4)), "v"(T.tag), "s"(ws + WS_LF), "s"(next ? ~0ull : 0xffffffffull) : "memory");
            flags_wait<LN_SLEEP>((u32x2){~T.tag, 0u}, ws + WS_LF + (rt * 32 + ct) * 256, (unsigned)(lane < 16 || rt == 0 ? (lane & 15) * 8 : 128), T.tag);
        }
        MEMFENCE(); BAR(); MEMFENCE();
    }
    STAMP_SEG(1);
    if (wid < 4) { g1_body<0>(L, T, lds, ws, rt, ct, wm, kh STAMP_PASS); g2_body<0>(T, lds, ws, x, out, rt, ct, wm STAMP_PASS2); }
    else { g1_body<1>(L, T, lds, ws, rt, ct, wm, kh STAMP_PASS); g2_body<1>(T, lds, ws, x, out, rt, ct, wm STAMP_PASS2); }
    TL_EXIT(ws, 0);
    TL_EXIT(ws, 1);
    TL_EXIT(ws, 2);

}

extern "C" void kernel_launch(void* const* d_in, const int* in_sizes, int n_in,
                              void* d_out, int out_size, void* d_ws, size_t ws_size,
                              hipStream_t stream) {
    const u16* x = (const u16*)d_in[0];
    const char* wqkv = (const char*)d_in[1];
    const char* wo = (const char*)d_in[2];
    const char* wg = (const char*)d_in[4];
    const char* wu = (const char*)d_in[5];
    const char* wd = (const char*)d_in[6];
    char* ws = (char*)d_ws;
    (void)hipFuncSetAttribute((const void*)k_main, hipFuncAttributeMaxDynamicSharedMemorySize, G1_LDS);
    k_main<<<256, 512, G1_LDS, stream>>>(wqkv, wg, wu, wo, wd, ws, x, (u16*)d_out);
}
```

```cpp
#include <hip/hip_runtime.h>
#include <hip/hip_bf16.h>
#include <stdint.h>

typedef __bf16 bf16x8 __attribute__((ext_vector_type(8)));
typedef __bf16 bf16x4 __attribute__((ext_vector_type(4)));
typedef __bf16 bf16x2 __attribute__((ext_vector_type(2)));
typedef float f32x4 __attribute__((ext_vector_type(4)));
typedef float f32x2 __attribute__((ext_vector_type(2)));
typedef unsigned u32x4 __attribute__((ext_vector_type(4)));
typedef unsigned u32x2 __attribute__((ext_vector_type(2)));
typedef unsigned short u16;
#define LAS __attribute__((address_space(3)))
#define GAS __attribute__((address_space(1)))
#define DEVI __device__ __forceinline__

constexpr int S_ = 2048, D_ = 1024, F_ = 3584;
constexpr int MB_ = S_ / 16;
constexpr size_t SLAB_B = (size_t)MB_ * 1024;
constexpr size_t WS_XN = 0;
constexpr size_t WS_HO = 32 * SLAB_B;
constexpr size_t WS_XB = WS_HO + 144 * SLAB_B;
constexpr size_t WS_FL = WS_XB + 256 * 32768;
constexpr size_t WS_LF = WS_FL + 32768;
constexpr size_t WS_EP = WS_LF + 65536;

DEVI unsigned pk_bf16(float lo, float hi) {
    bf16x2 v = {(__bf16)lo, (__bf16)hi};
    return __builtin_bit_cast(unsigned, v);
}
DEVI float bf_lo(unsigned u) { return __uint_as_float(u << 16); }
DEVI float bf_hi(unsigned u) { return __uint_as_float(u & 0xffff0000u); }
DEVI float rbf(float f) { return (float)(__bf16)f; }
DEVI bf16x8 as_frag(u32x4 v) { return __builtin_bit_cast(bf16x8, v); }
#define MFMA16(a, b, c) __builtin_amdgcn_mfma_f32_16x16x32_bf16((a), (b), (c), 0, 0, 0)
#define SBAR() __builtin_amdgcn_sched_barrier(0)
#define MFMA_ACC(c, a, b) asm volatile("v_mfma_f32_16x16x32_bf16 %0, %1, %2, %0" : "+v"(c) : "v"(a), "v"(b))
#define MFMA_NEW(c, a, b) asm volatile("v_mfma_f32_16x16x32_bf16 %0, %1, %2, 0" : "=&v"(c) : "v"(a), "v"(b))
#define MFMA_DRAIN() asm volatile("s_nop 15" ::: "memory")
#define BAR() __builtin_amdgcn_s_barrier()

#define TL_ENTRY()
#define TL_EXIT(ws, kern)
#define STAMP_DECL
#define STAMP_ARGS
#define STAMP_PASS
#define STAMP_ARGS2
#define STAMP_PASS2
#define STAMP_SEG(i)
#define STAMP_START()
#define STAMP_SEGI(i)
#define STAMP_OUT(ws, kern, wid, lane)

#define P0S(i)
DEVI int fresh_lane() { int l; asm volatile("v_mbcnt_lo_u32_b32 %0, -1, 0\n\tv_mbcnt_hi_u32_b32 %0, -1, %0" : "=v"(l)); return l; }

DEVI bf16x8 tr_frag(LAS const char* p, int off0, int off1) {
    bf16x4 lo = __builtin_amdgcn_ds_read_tr16_b64_v4bf16((LAS bf16x4*)(p + off0));
    bf16x4 hi = __builtin_amdgcn_ds_read_tr16_b64_v4bf16((LAS bf16x4*)(p + off1));
    return __builtin_shufflevector(lo, hi, 0, 1, 2, 3, 4, 5, 6, 7);
}

DEVI void glds16(const char* sbase, unsigned voff, unsigned ldsdst) {
    unsigned keep;
    asm volatile("s_mov_b32 %0, m0\n\ts_mov_b32 m0, %3\n\ts_nop 0\n\tglobal_load_lds_dwordx4 %1, %2\n\ts_mov_b32 m0, %0"
                 : "=&s"(keep) : "v"(voff), "s"(sbase), "s"(ldsdst) : "memory");
}

constexpr int G1_AST = 17 * 1024;
constexpr int G1_BST = 21 * 1024;
constexpr int G1_REG = 7168;
constexpr int G1_BOFF = 4 * G1_AST;
constexpr int G1_XOFF = G1_BOFF + 4 * G1_BST;
constexpr int G1_LDS = G1_XOFF + 8192;

typedef unsigned long long u64;
#define FRAG_REGS "v192", "v193", "v194", "v195", "v196", "v197", "v198", "v199", "v200", "v201", "v202", "v203", "v204", "v205", "v206", "v207", "v208", "v209", "v210", "v211", "v212", "v213", "v214", "v215", "v216", "v217", "v218", "v219", "v220", "v221", "v222", "v223", "v224", "v225", "v226", "v227", "v228", "v229", "v230", "v231", "v232", "v233", "v234", "v235", "v236", "v237", "v238", "v239", "v240", "v241", "v242", "v243", "v244", "v245", "v246", "v247", "v248", "v249", "v250", "v251", "v252", "v253", "v254", "v255"
constexpr int FA = 192, FB = 208, FAH = 248, FBH = 252;
struct G1Ld {
    const char *pa, *p2, *pg, *pu, *px;
    unsigned stepa, step2, stepg, stepx;
    unsigned va, v2, vg, vx;
    unsigned la, l2, lx;
    const char* warm; unsigned lw, ldum;
    char* pf;
    int ma, mb, mx, mw;
    const char* ta; unsigned ts, td; int mt;
};

#define WAIT_LGKM0() __builtin_amdgcn_s_waitcnt(0xC07F)
#define MEMFENCE() asm volatile("" ::: "memory")
#define OPAQUE_S(x) asm volatile("" : "+s"(x))

template <int LOFF, int KIND>
DEVI void g1_piece(const char* base, unsigned voff, unsigned lds, int msk) {
    asm volatile("s_cmp_eq_u32 %3, 0\n\ts_cbranch_scc1 .Lsk_%=\n\ts_add_u32 m0, %2, %4\n\ts_mov_b32 exec_lo, %3\n\ts_ashr_i32 exec_hi, %3, 31\n\t"
                 ".if %5 == 0\n\tglobal_load_lds_dwordx4 %0, %1\n\t.else\n\tglobal_load_lds_dwordx4 %0, %1 sc1\n\t.endif\n\t"
                 ".if %5 == 2\n\tglobal_load_lds_dwordx4 %0, %1 offset:1024 sc1\n\t.endif\n\t"
                 "s_mov_b64 exec, -1\n.Lsk_%=:"
                 :: "v"(voff), "s"(base), "s"(lds), "s"(msk), "i"(LOFF), "i"(KIND) : "memory", "scc");
}
template <int SLOT> DEVI void g1_pa(G1Ld& L) { g1_piece<SLOT * G1_AST, 2>(L.pa, L.va, L.la, L.ma); L.pa += L.stepa; OPAQUE_S(L.pa); }
template <int SLOT> DEVI void g1_px(G1Ld& L) { g1_piece<SLOT * G1_AST, 1>(L.px, L.vx, L.lx, L.mx); L.px += L.stepx; OPAQUE_S(L.px); }
template <int SLOT> DEVI void g1_p2(G1Ld& L) { g1_piece<SLOT * G1_BST, 0>(L.p2, L.v2, L.l2, L.mb); L.p2 += L.step2; OPAQUE_S(L.p2); }
template <int SLOT> DEVI void g1_pg(G1Ld& L) { g1_piece<SLOT * G1_BST + G1_REG, 0>(L.pg, L.vg, L.l2, L.mb); L.pg += L.stepg; OPAQUE_S(L.pg); }
template <int SLOT> DEVI void g1_pu(G1Ld& L) { g1_piece<SLOT * G1_BST + 2 * G1_REG, 0>(L.pu, L.vg, L.l2, L.mb); L.pu += L.stepg; OPAQUE_S(L.pu); }
template <int GRP, int SLOT> DEVI void g1_issue(G1Ld& L) { g1_pa<SLOT>(L); if (GRP == 0) g1_px<SLOT>(L); g1_p2<SLOT>(L); g1_pg<SLOT>(L); g1_pu<SLOT>(L); }
DEVI void g1_touch(G1Ld& L, int it) {
    asm volatile("s_cmp_ge_i32 %2, %3\n\ts_cbranch_scc1 .Lsk_%=\n\tglobal_load_dword %0, %1, off\n.Lsk_%=:" : "+v"(L.td) : "v"(L.ta), "s"(it), "s"(L.mt) : "memory", "scc");
    L.ta += L.ts; asm volatile("" : "+v"(L.ta));
}
template <int GRP> DEVI void g1_tail(G1Ld& L, int it) {
    OPAQUE_S(it);
    const bool tail = it == 7;
    L.p2 = tail ? L.p2 - L.step2 : L.p2; L.pg = tail ? L.pg - L.stepg : L.pg; L.pu = tail ? L.pu - L.stepg : L.pu;
    L.step2 = tail ? 0u : L.step2; L.stepg = tail ? 0u : L.stepg;
    L.mb = tail ? L.mb & 1 : L.mb;
    if (GRP == 0) { L.pa = tail ? L.warm : L.pa; L.px = tail ? L.px - L.stepx : L.px; L.stepx = tail ? 0u : L.stepx; L.mx = tail ? 1 : L.mx; }
    else { L.pa = tail ? L.pa - L.stepa : L.pa; L.ma = tail ? L.ma & 1 : L.ma; L.mw = tail ? 0 : L.mw; }
    L.stepa = tail ? 0u : L.stepa;
}

struct G1Ptr { unsigned pA, pH, pB01, pB23, pBh01, pBh23; };

template <int REG, int OFF> DEVI void ds_tr(unsigned base) { asm volatile("ds_read_b64_tr_b16 v[%c1:%c2], %0 offset:%c3" :: "v"(base), "i"(REG), "i"(REG + 1), "i"(OFF) : "memory"); }
template <int REG, int OFF> DEVI void ds_128(unsigned base) { asm volatile("ds_read_b128 v[%c1:%c2], %0 offset:%c3" :: "v"(base), "i"(REG), "i"(REG + 3), "i"(OFF) : "memory"); }
template <int RA_, int RB_> DEVI void mfma_fr(f32x4& c) { asm volatile("v_mfma_f32_16x16x32_bf16 %0, v[%c1:%c2], v[%c3:%c4], %0" : "+v"(c) : "i"(RA_), "i"(RA_ + 3), "i"(RB_), "i"(RB_ + 3)); }
template <int GRP, int SL, int n, int h> DEVI void g1_ldb(const G1Ptr& P) {
    constexpr int off = (SL & 1) * G1_BST + (GRP == 0 ? (n < 6 ? n * 32 : (n < 8 ? G1_REG + (n - 1) * 32 : 2 * G1_REG + (n - 3) * 32))
                                                      : (n < 5 ? G1_REG + n * 32 : 2 * G1_REG + (n - 5) * 32));
    ds_tr<FB + 4 * n + 2 * h, off + h * 8 * 224>((SL < 2) ? P.pB01 : P.pB23);
}
template <int SL, int m> DEVI void g1_lda(const G1Ptr& P) { ds_128<FA + 4 * m, SL * G1_AST + (1 + m) * 1024>(P.pA); }
template <int SL> DEVI void g1_ldah(const G1Ptr& P) { ds_128<FAH, SL * G1_AST>(P.pH); }
template <int SL, int h> DEVI void g1_ldbh(const G1Ptr& P) { ds_tr<FBH + 2 * h, (SL & 1) * G1_BST + h * 8 * 224>((SL < 2) ? P.pBh01 : P.pBh23); }

template <int GRP, int ST>
DEVI void g1_stage(f32x4 (&acc)[4][10], f32x4& acch, G1Ld& L, int it, const G1Ptr& P) {
    constexpr int NX = (ST + 1) & 3, IS = (ST + 3) & 3;
#define MM(m, n) do { if (GRP == 0 && ((n) == 4 || (n) == 5)) mfma_fr<FA + 4 * (m), FB + 4 * (n)>(acc[m][n]); else mfma_fr<FB + 4 * (n), FA + 4 * (m)>(acc[m][n]); } while (0)
#define RB(n, h) g1_ldb<GRP, NX, n, h>(P)
#define RC(n, h) g1_ldb<GRP, ST, n, h>(P)
#define RA(m) g1_lda<NX, m>(P)
#define G0(x) do { if (GRP == 0) { x; } } while (0)
    asm volatile("s_waitcnt lgkmcnt(9)" ::: "memory");
    MM(0, 0); RC(8, 0);
    asm volatile("s_waitcnt lgkmcnt(9)" ::: "memory");
    MM(1, 0); RC(8, 1);
    MM(1, 1); RC(9, 0); MM(0, 1); RC(9, 1);
    MM(0, 2); G0(g1_pa<IS>(L)); MM(1, 2); G0(g1_px<IS>(L));
    MM(1, 3); G0(g1_p2<IS>(L)); MM(0, 3); G0(g1_pg<IS>(L));
    MM(0, 4); G0(g1_pu<IS>(L)); MM(1, 4);
    G0((mfma_fr<FBH, FAH>(acch)));
    asm volatile("s_waitcnt vmcnt(%0) lgkmcnt(0)\n\ts_barrier" :: "i"(GRP == 0 ? 12 : 10) : "memory", FRAG_REGS);
    if (GRP == 1) {
        if (ST == 0) {
            g1_tail<1>(L, it);
            asm volatile("s_or_b32 %0, %1, %2\n\ts_cbranch_scc1 .Lsk_%=\n\ts_waitcnt vmcnt(0)\n.Lsk_%=:" : "=s"(L.ldum) : "s"(L.ma), "s"(L.mw) : "memory", "scc");
        }
        g1_issue<1, ST>(L);
        g1_touch(L, it);
        if (ST == 1) { g1_piece<0, 0>(L.warm, L.va, L.lw, L.mw); L.warm += 1024; OPAQUE_S(L.warm); }
        if (ST == 2) {
            const u32x4 ones = {0xffffffffu, 0xffffffffu, 0xffffffffu, 0xffffffffu};
            asm volatile("global_store_dwordx4 %0, %1, %2 sc1\n\ts_nop 1" :: "v"(L.va), "v"(ones), "s"(L.pf) : "memory", FRAG_REGS);
            L.pf += 1024; OPAQUE_S(L.pf);
        }
    }
    MM(2, 0); MM(3, 0); RB(0, 0);
    MM(3, 1); RB(0, 1); MM(2, 1); RB(1, 0);
    MM(2, 2); RB(1, 1); MM(3, 2); RB(2, 0);
    MM(3, 3); RB(2, 1); MM(2, 3); RB(3, 0);
    MM(2, 4); RB(3, 1); MM(3, 4); RB(4, 0);
    MM(0, 5); RB(4, 1); MM(1, 5); G0(g1_ldah<NX>(P));
    MM(1, 6); G0((g1_ldbh<NX, 0>(P))); MM(0, 6); G0((g1_ldbh<NX, 1>(P)));
    MM(0, 7); MM(1, 7);
    MM(1, 8); MM(0, 8);
    MM(0, 9); RA(0); MM(1, 9); RA(1);
    MM(2, 5); MM(3, 5); RB(5, 0);
    MM(3, 6); RB(5, 1); MM(2, 6); RB(6, 0);
    MM(2, 7); RB(6, 1); MM(3, 7); RB(7, 0);
    MM(3, 8); RB(7, 1); MM(2, 8);
    MM(2, 9); RA(2); MM(3, 9); RA(3);
#undef MM
#undef RB
#undef RC
#undef RA
#undef G0
}

DEVI void st16(char* dst, u32x4 v) {
    asm volatile("global_store_dwordx4 %0, %1, off sc1\n\ts_nop 1" :: "v"(dst), "v"(v) : "memory");
}

DEVI unsigned glu2(float g0, float g1, float u0, float u1) {
    const f32x2 t = (f32x2){g0, g1} * -1.4426950408889634f;
    const f32x2 d = (f32x2){__builtin_amdgcn_exp2f(t[0]), __builtin_amdgcn_exp2f(t[1])} + 1.0f;
    const f32x2 h = (f32x2){u0, u1} * (f32x2){__builtin_amdgcn_rcpf(d[0]), __builtin_amdgcn_rcpf(d[1])};
    return pk_bf16(h[0], h[1]);
}

DEVI void glu_store(const f32x4& g0, const f32x4& u0, const f32x4& g1, const f32x4& u1, char* dst) {
    unsigned a0 = glu2(g0[0], g0[1], u0[0], u0[1]), a1 = glu2(g0[2], g0[3], u0[2], u0[3]);
    unsigned b0 = glu2(g1[0], g1[1], u1[0], u1[1]), b1 = glu2(g1[2], g1[3], u1[2], u1[3]);
    auto r0 = __builtin_amdgcn_permlane16_swap(a0, b0, false, false);
    auto r1 = __builtin_amdgcn_permlane16_swap(a1, b1, false, false);
    st16(dst, (u32x4){r0[0], r1[0], r0[1], r1[1]});
    SBAR();
}

#define W_EARLY 1
#define LN_SLEEP 2
#define POLL_SLEEP 8
#define G2_RCV 34
#define G2_PLACE 1
#define G2_NS 8
constexpr int G2_ST = 16384;
constexpr int G2_WL = G2_NS - 1;
constexpr int G2_XOFF = G2_NS <= 8 ? G2_NS * G2_ST : 0;
constexpr int G2_F = 32, G2_XST = 33;
constexpr int G2_FY = 8, G2_FZ = 12;
constexpr int G2_PA = 1, G2_PY = 9, G2_PZ = 15;
constexpr unsigned SPIN_CAP = 1u << 17;
static_assert(G2_RCV > G2_F && G2_RCV <= 35 && G2_NS >= 6 && G2_NS <= 9, "");
constexpr int g2_grp(int t) { return t < G2_F ? t : G2_F + 2 * (t - G2_F); }
constexpr int g2_afirst(int s) { return s < 2 ? 2 + 2 * s : s + 4 <= 35 ? g2_grp(s + 4) : 40; }
constexpr int g2_alast(int s) { return g2_afirst(s + 1); }
constexpr int g2_tops(int s) { return s == 0 || s == G2_FY || s == G2_FZ ? 1 : 0; }
constexpr int g2_wops(int s) { return s + G2_WL <= 35 ? 2 : 0; }
constexpr int g2_xops(int s) { return (s < 8 && (s & 1) ? 1 : 0) + (s == G2_PA || s == G2_PY || s == G2_PZ ? 1 : 0) + (s == G2_F ? 4 : 0) + (s == G2_XST ? 2 : 0) + (s == G2_RCV ? 4 : 0); }
constexpr int g2_wait(int t) {
    const int need = g2_grp(t + 1) - 1;
    int n = 4, last = need < 2 ? 2 * need + 2 : 0;
    for (int s = 0; s < t; ++s) {
        n += g2_tops(s) + g2_wops(s);
        if (s + G2_WL == t + 1 && n > last) last = n;
        for (int g = g2_afirst(s); g < g2_alast(s); ++g) { n += 2; if (g == need && n > last) last = n; }
        n += g2_xops(s);
        if ((t == 4 && s == G2_PA) || (t == 12 && s == G2_PY) || (t == 20 && s == G2_PZ) || (t == G2_FY && s == 3) || (t == G2_FZ && s == 7)) last = n > last ? n : last;
    }
    return n - last;
}

struct G2W {
    const char *pwo, *pwh, *pwd;
    unsigned vw0, vw1, vw2, ldsw;
};
struct G2St {
    G2W W;
    const char *pa, *pad;
    unsigned va0, va1;
    LAS const char* pB[(G2_NS + 3) / 4][4];
    LAS char* xw; LAS const char* xr;
    char* snd; const char* rcv; const u16* xp;
    f32x4 dg[2][4], du[2][4]; char* dd[2]; unsigned hl, hq[4];
    char *flagA, *flagZ; const char* flags; unsigned foa, foz, tag; u32x2 hf; bool poller;
    u32x4 A[40][2];
    bf16x4 B[8][2];
    f32x4 acc[2][8];
    u32x4 xres[2], rc[4];
};
template <int LOFF> DEVI void g2_glds(const char* base, unsigned voff, unsigned lds) {
    asm volatile("s_add_u32 m0, %2, %3\n\ts_nop 0\n\tglobal_load_lds_dwordx4 %0, %1" :: "v"(voff), "s"(base), "s"(lds), "i"(LOFF) : "memory", "scc");
}
template <int ST, int I> DEVI void g2_w(const G2W& W) {
    constexpr int SLOT = ST % G2_NS;
    const char* src = ST < 8 ? W.pwo + ST * 64 * 2048 : ST < G2_F ? W.pwh + (((ST - 8) & 7) * 224 + ((ST - 8) >> 3) * 32) * 2048 : W.pwd + (ST - G2_F) * 896 * 2048;
    g2_glds<SLOT * G2_ST + I * 8192>(src, I == 0 ? W.vw0 : ST < G2_F ? W.vw1 : W.vw2, W.ldsw);
}
template <int G> DEVI const char* g2_asrc(const G2St& T) {
    return G < G2_F ? T.pa + (size_t)G * 2 * SLAB_B : T.pad + (size_t)(G - G2_F) * 2 * SLAB_B; }
template <int G, int I> DEVI void g2_a(G2St& T) {
    asm volatile("global_load_dwordx4 %0, %1, %2 sc1" : "=v"(T.A[G][I]) : "v"(I ? T.va1 : T.va0), "s"(g2_asrc<G>(T)) : "memory");
}
template <int ST> DEVI bf16x4 g2_ldb(G2St& T, int n, int h) {
    constexpr int SLOT = ST % G2_NS;
    return __builtin_amdgcn_ds_read_tr16_b64_v4bf16((LAS bf16x4*)(T.pB[SLOT / 4][n & 3] + (SLOT % 4) * G2_ST + (n >> 2) * 8192 + h * 512));
}
DEVI bf16x8 cat(bf16x4 lo, bf16x4 hi) { return __builtin_shufflevector(lo, hi, 0, 1, 2, 3, 4, 5, 6, 7); }
template <int OFF> DEVI void flag_set(char* p, unsigned tag) { const unsigned zero = 0u; asm volatile("s_mov_b64 exec, 1\n\tglobal_store_dword %0, %1, %2 offset:%3 sc1\n\ts_mov_b64 exec, -1" :: "v"(zero), "v"(tag), "s"(p), "i"(OFF) : "memory"); }
template <int SLEEP> DEVI void flags_wait(u32x2 f, const char* base, unsigned voff, unsigned tag) {
#pragma unroll 1
    for (unsigned spin = 0; __any(f[0] != tag || f[1] != tag) && spin < SPIN_CAP; ++spin) {
        if (spin) __builtin_amdgcn_s_sleep(SLEEP);
        asm volatile("global_load_dwordx2 %0, %1, %2 sc1\n\ts_waitcnt vmcnt(0)" : "=&v"(f) : "v"(voff), "s"(base) : "memory");
    }
}
DEVI unsigned umax4(u32x4 v) { const unsigned a = v[0] > v[1] ? v[0] : v[1], b = v[2] > v[3] ? v[2] : v[3]; return a > b ? a : b; }
DEVI u32x4 never_ones(u32x4 w) {
#pragma unroll
    for (int e = 0; e < 4; ++e) w[e] = w[e] < 0xfffffffeu ? w[e] : 0xfffffffeu;
    return w;
}

template <int KG, int t>
DEVI void g2_stage(G2St& T) {
    constexpr bool TB = t >= G2_F, ISW = g2_wops(t) != 0, RL = t < 35, CHK = t == 4 || t == 12 || t == 20;
    constexpr int G0 = g2_grp(t), G1 = TB ? G0 + 1 : G0, NA = g2_alast(t) - g2_afirst(t), GN = NA ? g2_afirst(t) : 0, GN1 = NA == 2 ? GN + 1 : GN;
    u32x4 (&Aa)[2] = T.A[G0]; u32x4 (&Ab)[2] = T.A[G1];
    if (TB) asm volatile("s_waitcnt vmcnt(%4)" : "+v"(Aa[0]), "+v"(Aa[1]), "+v"(Ab[0]), "+v"(Ab[1]) : "i"(g2_wait(t)) : "memory");
    else if (CHK) asm volatile("s_waitcnt vmcnt(%3)" : "+v"(Aa[0]), "+v"(Aa[1]), "+v"(T.hf) : "i"(g2_wait(t)) : "memory");
    else asm volatile("s_waitcnt vmcnt(%2)" : "+v"(Aa[0]), "+v"(Aa[1]) : "i"(g2_wait(t)) : "memory");
    if (t == 0) flag_set<0>(T.flagA, T.tag);
    if (t == G2_FY) flag_set<128>(T.flagA, T.tag);
    if (t == G2_FZ) flag_set<0>(T.flagZ, T.tag);
    if (t < 8) {
#pragma unroll 1
        for (unsigned spin = 0; spin < SPIN_CAP; ++spin) {
            const unsigned a = umax4(Aa[0]), b = umax4(Aa[1]);
            if (!__any((a > b ? a : b) == 0xffffffffu)) break;
            __builtin_amdgcn_s_sleep(POLL_SLEEP);
            asm volatile("global_load_dwordx4 %0, %2, %4 sc1\n\tglobal_load_dwordx4 %1, %3, %4 sc1\n\ts_waitcnt vmcnt(0)" : "=&v"(Aa[0]), "=&v"(Aa[1]) : "v"(T.va0), "v"(T.va1), "s"(g2_asrc<G0>(T)) : "memory");
        }
    }
    if (KG == 0 && CHK && T.poller) flags_wait<POLL_SLEEP>(T.hf, t == 12 ? T.flags + 128 : T.flags, t == 20 ? T.foz : T.foa, T.tag);
    if (t == G2_F) WAIT_LGKM0();
    { MEMFENCE(); SBAR(); BAR(); MEMFENCE(); SBAR(); }
    f32x4 ho[4];
    if (t == G2_F) {
#pragma unroll
        for (int j = 0; j < 4; ++j) ho[j] = *(LAS const f32x4*)(T.xr + j * 1024);
        SBAR();
    }
#define AB2DS true
#define RL2(n, h) do { if (AB2DS && RL && (n) >= 0) { T.B[(n) >= 0 ? (n) : 0][h] = g2_ldb<t + 1>(T, (n) >= 0 ? (n) : 0, h); SBAR(); } } while (0)
#define P2(n) do { constexpr int c_ = TB ? ((n) & 3) : (n); u32x4 (&A_)[2] = (n) < 4 ? Aa : Ab; const bf16x8 b_ = cat(T.B[n][0], T.B[n][1]); \
                   if (t == 0) MFMA_NEW(T.acc[(n) & 1][c_], b_, A_[(n) & 1]); else MFMA_ACC(T.acc[(n) & 1][c_], b_, A_[(n) & 1]); SBAR(); RL2((n) - 1, 1); D((n) * 2); \
                   if (t == 0) MFMA_NEW(T.acc[~(n) & 1][c_], b_, A_[~(n) & 1]); else MFMA_ACC(T.acc[~(n) & 1][c_], b_, A_[~(n) & 1]); SBAR(); RL2(n, 0); D((n) * 2 + 1); } while (0)
#define VW(i) do { if (ISW) { g2_w<ISW ? t + G2_WL : 0, i>(T.W); SBAR(); } } while (0)
#define VA(i) do { if (NA == 2) { g2_a<(i) < 2 ? GN : GN1, (i) & 1>(T); SBAR(); } else if (NA && ((i) & 1)) { g2_a<GN, (i) / 2>(T); SBAR(); } } while (0)
    f32x4& dg = T.dg[(t >> 2) & 1][t & 3]; f32x4& du = T.du[(t >> 2) & 1][t & 3];
#define D(i) do { if (t < 8 && (i) < 13) { asm volatile("" : "+v"(dg)); \
        if ((i) == 0) dg = dg * -1.4426950408889634f; \
        else if ((i) < 5) dg[((i) - 1) & 3] = __builtin_amdgcn_exp2f(dg[((i) - 1) & 3]); \
        else if ((i) == 5) dg = dg + 1.0f; \
        else if ((i) < 10) dg[((i) - 6) & 3] = __builtin_amdgcn_rcpf(dg[((i) - 6) & 3]); \
        else if ((i) == 10) dg = dg * du; \
        else { T.hq[(t & 1) * 2 + (((i) - 11) & 1)] = pk_bf16(dg[(((i) - 11) & 1) * 2], dg[(((i) - 11) & 1) * 2 + 1]); asm volatile("" : "+v"(T.hq[(t & 1) * 2 + (((i) - 11) & 1)])); } \
        if ((i) < 11) asm volatile("" : "+v"(dg)); \
        SBAR(); } } while (0)
    if (KG == 1) { VW(0); VW(1); VA(0); VA(1); VA(2); VA(3); P2(0); P2(1); P2(2); P2(3); P2(4); P2(5); P2(6); P2(7); }
    else { P2(0); P2(1); P2(2); P2(3); VW(0); P2(4); VW(1); P2(5); VA(0); VA(1); P2(6); VA(2); VA(3); P2(7); }
    RL2(7, 1);
#undef RL2
#undef VW
#undef VA
#undef D
#undef P2
    if (t < 8 && (t & 1)) {
        auto r0 = __builtin_amdgcn_permlane16_swap(T.hq[0], T.hq[2], false, false);
        auto r1 = __builtin_amdgcn_permlane16_swap(T.hq[1], T.hq[3], false, false);
        const u32x4 w = {r0[0], r1[0], r0[1], r1[1]};
        asm volatile("global_store_dwordx4 %0, %1, %2 offset:%3 sc1\n\ts_nop 1" :: "v"(T.hl), "v"(w), "s"(T.dd[(t >> 2) & 1]), "i"((t & 2) * 1024) : "memory");
        SBAR();
    }
    if (t == G2_PA || t == G2_PY || t == G2_PZ) {
        asm volatile("global_load_dwordx2 %0, %1, %2 offset:%3 sc1" : "=v"(T.hf) : "v"(t == G2_PZ ? T.foz : T.foa), "s"(T.flags), "i"(t == G2_PY ? 128 : 0) : "memory"); SBAR();
    }
    if (t == G2_F - 1) {
        MFMA_DRAIN(); SBAR();
#pragma unroll
        for (int j = 0; j < 4; ++j) *(LAS f32x4*)(T.xw + j * 1024) = T.acc[1][4 + j];
        SBAR();
    }
    if (t == G2_F) {
#pragma unroll
        for (int j = 0; j < 4; ++j) {
            const u32x4 w = never_ones(__builtin_bit_cast(u32x4, T.acc[0][4 + j] + ho[j]));
            asm volatile("global_store_dwordx4 %0, %1, off offset:%2 sc1\n\ts_nop 1" :: "v"(T.snd), "v"(w), "i"(j * 1024) : "memory");
        }
        SBAR();
    }
    if (t == G2_XST) { asm volatile("global_load_dwordx4 %0, %2, off\n\tglobal_load_dwordx4 %1, %2, off offset:64" : "=&v"(T.xres[0]), "=&v"(T.xres[1]) : "v"(T.xp) : "memory"); SBAR(); }
    if (t == G2_RCV) {
        asm volatile("global_load_dwordx4 %0, %4, off sc1\n\tglobal_load_dwordx4 %1, %4, off offset:1024 sc1\n\tglobal_load_dwordx4 %2, %4, off offset:2048 sc1\n\tglobal_load_dwordx4 %3, %4, off offset:3072 sc1"
                     : "=&v"(T.rc[0]), "=&v"(T.rc[1]), "=&v"(T.rc[2]), "=&v"(T.rc[3]) : "v"(T.rcv) : "memory");
        SBAR();
    }
}
template <int KG, int t, int E> DEVI void g2_run(G2St& T) { g2_stage<KG, t>(T); if constexpr (t + 1 < E) g2_run<KG, t + 1, E>(T); }

DEVI unsigned bsel(unsigned m, unsigned a, unsigned b) { return (m & a) | (~m & b); }
typedef short s16x4 __attribute__((ext_vector_type(4)));
#define MFMA16K16(a, b, c) __builtin_amdgcn_mfma_f32_16x16x16bf16_1k(__builtin_bit_cast(s16x4, a), __builtin_bit_cast(s16x4, b), (c), 0, 0, 0)
template <bool MAX> DEVI float rows_reduce(const float (&v)[4]) {
    auto a = __builtin_amdgcn_permlane16_swap(__float_as_uint(v[0]), __float_as_uint(v[1]), false, false);
    auto b = __builtin_amdgcn_permlane16_swap(__float_as_uint(v[2]), __float_as_uint(v[3]), false, false);
    const float t = MAX ? fmaxf(__uint_as_float(a[0]), __uint_as_float(a[1])) : __uint_as_float(a[0]) + __uint_as_float(a[1]);
    const float u = MAX ? fmaxf(__uint_as_float(b[0]), __uint_as_float(b[1])) : __uint_as_float(b[0]) + __uint_as_float(b[1]);
    auto c = __builtin_amdgcn_permlane32_swap(__float_as_uint(t), __float_as_uint(u), false, false);
    return MAX ? fmaxf(__uint_as_float(c[0]), __uint_as_float(c[1])) : __uint_as_float(c[0]) + __uint_as_float(c[1]);
}
DEVI void rows_spread(float w, float (&v)[4]) {
    auto c = __builtin_amdgcn_permlane32_swap(__float_as_uint(w), __float_as_uint(w), false, false);
    auto a = __builtin_amdgcn_permlane16_swap(c[0], c[0], false, false);
    auto b = __builtin_amdgcn_permlane16_swap(c[1], c[1], false, false);
    v[0] = __uint_as_float(a[0]); v[1] = __uint_as_float(a[1]); v[2] = __uint_as_float(b[0]); v[3] = __uint_as_float(b[1]);
}

template <int GRP>
DEVI void g1_body(G1Ld& L, G2St& T, LAS char* lds, char* ws, int rt, int ct, int wm, int kh STAMP_ARGS) {
    G1Ptr P;
    {
        const int lane = fresh_lane(), g = lane >> 4, q = (lane & 15) >> 2, p = lane & 3;
        const unsigned ldsb = (unsigned)(size_t)lds;
        P.pA = ldsb + wm * 4096 + lane * 16;
        P.pH = ldsb + lane * 16;
        const int brow = (16 * (g >> 1) + 4 * (g & 1) + q) * 224 + p * 8;
        P.pB01 = ldsb + G1_BOFF + brow;
        P.pB23 = P.pB01 + 2 * G1_BST;
        P.pBh01 = P.pB01 + (2 + wm) * 32;
        P.pBh23 = P.pB23 + (2 + wm) * 32;
    }
    f32x4 acc[4][10], acch = {0.f, 0.f, 0.f, 0.f};
#pragma unroll
    for (int m = 0; m < 4; ++m)
#pragma unroll
        for (int n = 0; n < 10; ++n) acc[m][n] = (f32x4){0.f, 0.f, 0.f, 0.f};

    g1_pa<0>(L); if (GRP == 0) g1_px<0>(L);
    g1_pa<1>(L); if (GRP == 0) g1_px<1>(L);
#pragma unroll
    for (int m = 0; m < 4; ++m)
        asm volatile("" : "+v"(acc[m][0]), "+v"(acc[m][1]), "+v"(acc[m][2]), "+v"(acc[m][3]), "+v"(acc[m][4]), "+v"(acc[m][5]), "+v"(acc[m][6]), "+v"(acc[m][7]), "+v"(acc[m][8]), "+v"(acc[m][9]), "+v"(acch));
    asm volatile("s_waitcnt vmcnt(%0)\n\ts_barrier" :: "i"(GRP == 0 ? 3 : 2) : "memory", FRAG_REGS);
    g1_ldb<GRP, 0, 0, 0>(P); g1_ldb<GRP, 0, 0, 1>(P); g1_ldb<GRP, 0, 1, 0>(P); g1_ldb<GRP, 0, 1, 1>(P); g1_ldb<GRP, 0, 2, 0>(P); g1_ldb<GRP, 0, 2, 1>(P);
    g1_ldb<GRP, 0, 3, 0>(P); g1_ldb<GRP, 0, 3, 1>(P); g1_ldb<GRP, 0, 4, 0>(P); g1_ldb<GRP, 0, 4, 1>(P);
    if (GRP == 0) { g1_ldah<0>(P); g1_ldbh<0, 0>(P); g1_ldbh<0, 1>(P); }
    g1_lda<0, 0>(P); g1_lda<0, 1>(P);
    g1_ldb<GRP, 0, 5, 0>(P); g1_ldb<GRP, 0, 5, 1>(P); g1_ldb<GRP, 0, 6, 0>(P); g1_ldb<GRP, 0, 6, 1>(P); g1_ldb<GRP, 0, 7, 0>(P); g1_ldb<GRP, 0, 7, 1>(P);
    g1_lda<0, 2>(P); g1_lda<0, 3>(P);
    g1_issue<GRP, 2>(L);
    if (GRP == 1) g1_issue<1, 3>(L);
    STAMP_SEG(0);
#pragma unroll 1
    for (int it = 0; it < 8; ++it) {
        g1_stage<GRP, 0>(acc, acch, L, it, P);
        if (GRP == 0) g1_tail<0>(L, it);
        g1_stage<GRP, 1>(acc, acch, L, it, P);
        g1_stage<GRP, 2>(acc, acch, L, it, P);
        g1_stage<GRP, 3>(acc, acch, L, it, P);
    }
    asm volatile("s_waitcnt vmcnt(0) lgkmcnt(0)\n\ts_nop 15" : "+v"(L.td) :: "memory", FRAG_REGS);
    SBAR();
    STAMP_SEG(0);

    const int lane = fresh_lane(), g = lane >> 4, i16 = lane & 15;
    char* ho = ws + WS_HO;
    const int mb0 = 16 * rt + 4 * wm;
    const unsigned hl = (unsigned)((g & 1) * 1024 + (g >> 1) * 256 + i16 * 16);
    G2W& W = T.W;
    {
        const int wid = GRP * 4 + wm, r = lane >> 3, c = (lane & 7) ^ ((r & 2) | ((wid & 1) << 2));
        W.vw0 = (unsigned)((8 * wid + r) * 2048 + c * 16 + kh * 128);
        W.vw1 = W.vw0 ^ 128;
        W.vw2 = W.vw0 + 448 * 2048;
    }
    char* hs = ho + (size_t)(64 * (ct >> 4) + (ct & 15)) * SLAB_B + (size_t)mb0 * 1024;
#define GW(i) do { if ((i) < 2 * G2_WL) { g2_w<(i) / 2, (i) % 2>(W); SBAR(); } } while (0)
#define GLU1(n, m) glu_store(acc[m][n], acc[m][5 + (n)], acc[(m) + 1][n], acc[(m) + 1][5 + (n)], hs + (size_t)(16 * (1 + (n) / 2)) * SLAB_B + ((n) & 1) * 512 + (m) * 1024 + hl)
    if (GRP == 1) {
        SBAR(); BAR(); MEMFENCE(); SBAR();
        GW(0); GW(1); GW(2); GW(3); GLU1(0, 0); GW(4); GW(5); GLU1(0, 2); GW(6); GW(7); GLU1(1, 0); GW(8); GW(9); GLU1(1, 2); GW(10); GW(11); GLU1(2, 0); GW(12); GW(13); GLU1(2, 2); GW(14); GW(15);
        asm volatile("s_waitcnt vmcnt(%0)" :: "i"(2 * G2_WL + 4) : "memory");
#pragma unroll
        for (int m = 0; m < 4; ++m) { T.dg[0][m] = acc[m][3]; T.dg[1][m] = acc[m][4]; T.du[0][m] = acc[m][8]; T.du[1][m] = acc[m][9]; }
        T.dd[0] = hs + (size_t)32 * SLAB_B + 512; T.dd[1] = hs + (size_t)48 * SLAB_B;
    } else {
        u32x4 Qf[4], Kf[4]; u32x2 Vh[4][2];
#pragma unroll
        for (int m = 0; m < 4; ++m) {
            Qf[m] = (u32x4){pk_bf16(acc[m][0][0], acc[m][0][1]), pk_bf16(acc[m][0][2], acc[m][0][3]), pk_bf16(acc[m][1][0], acc[m][1][1]), pk_bf16(acc[m][1][2], acc[m][1][3])};
            Kf[m] = (u32x4){pk_bf16(acc[m][2][0], acc[m][2][1]), pk_bf16(acc[m][2][2], acc[m][2][3]), pk_bf16(acc[m][3][0], acc[m][3][1]), pk_bf16(acc[m][3][2], acc[m][3][3])};
#pragma unroll
            for (int b = 0; b < 2; ++b) Vh[m][b] = (u32x2){pk_bf16(acc[m][4 + b][0], acc[m][4 + b][1]), pk_bf16(acc[m][4 + b][2], acc[m][4 + b][3])};
        }
        LAS char* xs = lds + G1_XOFF;
        if (wm < 3) {
            *(LAS u32x4*)(xs + (wm + 1) * 2048 + lane * 16) = Kf[3];
            *(LAS u32x4*)(xs + (wm + 1) * 2048 + 1024 + lane * 16) = (u32x4){Vh[3][0][0], Vh[3][0][1], Vh[3][1][0], Vh[3][1][1]};
        }
        {
            const unsigned h0 = pk_bf16(acch[0], acch[1]), h1 = pk_bf16(acch[2], acch[3]);
            if (wm < 2) {
                *(LAS u32x2*)(xs + lane * 16 + wm * 8) = (u32x2){h0, h1};
            } else {
                LAS char* t = xs + 1024 + ((i16 >> 2) * 16 + 4 * g) * 16 + (wm - 2) * 8 + (i16 & 3) * 2;
                *(LAS u16*)(t) = (u16)h0; *(LAS u16*)(t + 16) = (u16)(h0 >> 16); *(LAS u16*)(t + 32) = (u16)h1; *(LAS u16*)(t + 48) = (u16)(h1 >> 16);
            }
        }
        asm volatile("s_waitcnt lgkmcnt(0)" ::: "memory");
        SBAR(); BAR(); MEMFENCE(); SBAR();
        GW(0); GW(1);
        const u32x4 Kp = *(LAS const u32x4*)(xs + wm * 2048 + lane * 16);
        const u32x4 Vp = *(LAS const u32x4*)(xs + wm * 2048 + 1024 + lane * 16);
        const int dc = i16 - 4 * g - (i16 & 1);
        unsigned modd = (i16 & 1) ? ~0u : 0u, mc0 = dc >= 0 ? ~0u : 0u, mc1 = dc >= 2 ? ~0u : 0u;
        asm volatile("" : "+v"(modd), "+v"(mc0), "+v"(mc1));
        const unsigned half = (modd & 0xffff0000u) | (~modd & 0x0000ffffu);
        const unsigned pp0 = half & ~mc0, pp1 = half & ~mc1, pc0 = half & mc0, pc1 = half & mc1;
        const float sc = 0.17677669529663687f * 1.4426950408889634f;
        const f32x4 z = {0.f, 0.f, 0.f, 0.f};
        float e0[4], e1[4], red[4];
#pragma unroll
        for (int m = 0; m < 4; ++m) {
            u32x4 s0 = __builtin_bit_cast(u32x4, MFMA16(as_frag(m ? Kf[m ? m - 1 : 0] : Kp), as_frag(Qf[m]), z));
            const u32x4 s1 = __builtin_bit_cast(u32x4, MFMA16(as_frag(Kf[m]), as_frag(Qf[m]), z));
            if (m == 0 && rt == 0 && wm == 0) { const unsigned c = __float_as_uint(-1e9f / 0.17677669529663687f); s0 = (u32x4){c, c, c, c}; }
            e0[m] = __uint_as_float(bsel(modd, bsel(mc0, s1[1], s0[1]), bsel(mc0, s1[0], s0[0])));
            e1[m] = __uint_as_float(bsel(modd, bsel(mc1, s1[3], s0[3]), bsel(mc1, s1[2], s0[2])));
            red[m] = fmaxf(e0[m], e1[m]);
        }
        {
            rows_spread(rows_reduce<true>(red), red);
        }
#pragma unroll
        for (int m = 0; m < 4; ++m) {
            e0[m] = __builtin_amdgcn_exp2f((e0[m] - red[m]) * sc); e1[m] = __builtin_amdgcn_exp2f((e1[m] - red[m]) * sc);
            red[m] = e0[m] + e1[m];
        }
        {
            float w = __builtin_amdgcn_rcpf(rows_reduce<false>(red));
            rows_spread(w, red);
        }
        char* od = hs + (((g & 1) << 1) | (g >> 1)) * 256 + i16 * 16;
#pragma unroll
        for (int m = 0; m < 4; ++m) {
            const float p0 = e0[m] * red[m], p1 = e1[m] * red[m];
            const unsigned w0 = pk_bf16(p0, p0), w1 = pk_bf16(p1, p1);
            const u32x2 Pp = {w0 & pp0, w1 & pp1}, Pc = {w0 & pc0, w1 & pc1};
            const u32x2 Vp0 = m ? Vh[m ? m - 1 : 0][0] : (u32x2){Vp[0], Vp[1]}, Vp1 = m ? Vh[m ? m - 1 : 0][1] : (u32x2){Vp[2], Vp[3]};
            const f32x4 o0 = MFMA16K16(Vh[m][0], Pc, MFMA16K16(Vp0, Pp, z));
            const f32x4 o1 = MFMA16K16(Vh[m][1], Pc, MFMA16K16(Vp1, Pp, z));
            unsigned x0 = pk_bf16(o0[0], o0[1]), x1 = pk_bf16(o0[2], o0[3]), y0 = pk_bf16(o1[0], o1[1]), y1 = pk_bf16(o1[2], o1[3]);
            auto r0 = __builtin_amdgcn_permlane16_swap(x0, y0, false, false);
            auto r1 = __builtin_amdgcn_permlane16_swap(x1, y1, false, false);
            st16(od + m * 1024, never_ones((u32x4){r0[0], r1[0], r0[1], r1[1]}));
        }
        SBAR();
        GW(2); GW(3); GW(4); GW(5); GW(6); GW(7); GW(8); GW(9); GW(10); GW(11); GW(12); GW(13); GW(14); GW(15);
        asm volatile("s_waitcnt vmcnt(%0)" :: "i"(2 * G2_WL + 2) : "memory");
#pragma unroll
        for (int m = 0; m < 4; ++m) { T.dg[0][m] = acc[m][6]; T.dg[1][m] = acc[m][7]; T.du[0][m] = acc[m][8]; T.du[1][m] = acc[m][9]; }
        T.dd[0] = hs + (size_t)48 * SLAB_B + 512; T.dd[1] = ho + (size_t)(128 + (ct >> 1)) * SLAB_B + (ct & 1) * 512 + (size_t)mb0 * 1024;
        if (blockIdx.x == 0 && wm == 1) {
            flags_wait<POLL_SLEEP>((u32x2){~T.tag, 0u}, ws + WS_LF, (unsigned)(g * 8192 + i16 * 8), T.tag);
            flags_wait<POLL_SLEEP>((u32x2){~T.tag, 0u}, ws + WS_LF + 32768, (unsigned)(g * 8192 + i16 * 8), T.tag);
            flag_set<0>(ws + WS_EP, T.tag);
        }
    }
#undef GW
#undef GLU1
    STAMP_SEG(2);
}

DEVI float sum_x8(float v) {
    v += __uint_as_float(__builtin_amdgcn_update_dpp(0u, __float_as_uint(v), 0x128  , 0xf, 0xf, false));
    { auto t = __builtin_amdgcn_permlane16_swap(__float_as_uint(v), __float_as_uint(v), false, false); v = __uint_as_float(t[0]) + __uint_as_float(t[1]); }
    { auto t = __builtin_amdgcn_permlane32_swap(__float_as_uint(v), __float_as_uint(v), false, false); v = __uint_as_float(t[0]) + __uint_as_float(t[1]); }
    return v;
}

template <int KG>
DEVI void g2_body(G2St& T, LAS char* lds, char* ws, const u16* x, u16* out, int rt, int ct, int wm STAMP_ARGS2) {
    constexpr int kg = KG;
    const int bid = blockIdx.x, idx = bid >> 3, wid = KG * 4 + wm;
    const int kh = (bid >> 2) & 1, side = kh ^ 1, rt2 = 2 * rt + ((bid >> 1) & 1), ct2 = 4 * (bid & 1) + (idx & 3);
    const char* flq = ws + WS_FL + (rt * 4 + (bid & 2)) * 512;
    const int lane2 = fresh_lane(), g = lane2 >> 4, i16 = lane2 & 15, q = i16 >> 2, p = lane2 & 3;
    {
        const char* a0 = ws + WS_HO + (size_t)kg * SLAB_B + (size_t)(8 * rt2 + 2 * wm) * 1024;
        T.pa = a0 + (size_t)(64 * side) * SLAB_B;
        T.pad = a0 + (size_t)128 * SLAB_B;
        T.va0 = lane2 * 16 + kg * 1024; T.va1 = T.va0 ^ 1024;
        T.flagA = ws + WS_FL + (kg ^ 1) * 16384 + (rt * 4 + wm) * 512 + ct * 4;
        T.flagZ = ws + WS_FL + (rt * 4 + wm) * 512 + 256 + kg * 128 + ct * 4;
        T.hl = (unsigned)((g & 1) * 1024 + (g >> 1) * 256 + i16 * 16);
        T.poller = wid == 0;
        T.flags = T.poller ? flq : ws + WS_XB + (size_t)bid * 32768;
        T.foa = (unsigned)((lane2 >> 5) * 512 + side * 64 + (lane2 & 7) * 8);
        T.foz = (unsigned)((lane2 >> 5) * 512 + 256 + (lane2 & 31) * 8);
    }
#pragma unroll
    for (int n = 0; n < 4; ++n) {
        T.pB[0][n] = lds + kg * 4096 + (8 * g + q) * 128 + (((2 * n + (p >> 1)) ^ ((q & 2) | ((g & 1) << 2))) * 16) + (p & 1) * 8;
#pragma unroll
        for (int k = 1; k < (G2_NS + 3) / 4; ++k) { T.pB[k][n] = T.pB[0][n] + k * 4 * G2_ST; asm volatile("" : "+v"(T.pB[k][n])); }
        asm volatile("" : "+v"(T.pB[0][n]));
    }
    MEMFENCE(); SBAR(); BAR(); MEMFENCE(); SBAR();
    g2_a<0, 0>(T); g2_a<0, 1>(T); g2_a<1, 0>(T); g2_a<1, 1>(T);
    SBAR();
#pragma unroll
    for (int n = 0; n < 8; ++n) { T.B[n][0] = g2_ldb<0>(T, n, 0); T.B[n][1] = g2_ldb<0>(T, n, 1); }
    SBAR();
    STAMP_SEG(3);
    g2_run<KG, 0, 8>(T);
    STAMP_SEG(4);
    g2_run<KG, 8, 24>(T);
    const int orow = 128 * rt2 + 32 * wm + 16 * kg + i16, ocol = 128 * ct2 + 64 * kh + (g & 1) * 16 + (g >> 1) * 8;
    T.xw = lds + G2_XOFF + wid * 4096 + lane2 * 16;
    T.xr = lds + G2_XOFF + (wid ^ 4) * 4096 + lane2 * 16;
    T.snd = ws + WS_XB + (size_t)bid * 32768 + wid * 4096 + lane2 * 16;
    T.rcv = ws + WS_XB + (size_t)(bid ^ 4) * 32768 + wid * 4096 + lane2 * 16;
    T.xp = x + (size_t)orow * D_ + ocol;
    g2_run<KG, 24, 36>(T);
    STAMP_SEG(5);

    MFMA_DRAIN(); SBAR();
#pragma unroll
    for (int j = 0; j < 4; ++j) *(LAS f32x4*)(T.xw + j * 1024) = T.acc[1][j];
    WAIT_LGKM0();
    MEMFENCE(); SBAR(); BAR(); MEMFENCE(); SBAR();
#pragma unroll
    for (int j = 0; j < 4; ++j) T.acc[0][j] += *(LAS const f32x4*)(T.xr + j * 1024);
    asm volatile("s_waitcnt vmcnt(0)" : "+v"(T.xres[0]), "+v"(T.xres[1]), "+v"(T.rc[0]), "+v"(T.rc[1]), "+v"(T.rc[2]), "+v"(T.rc[3]) :: "memory");
    for (unsigned spin = 0;; ++spin) {
        const unsigned mx = umax4((u32x4){umax4(T.rc[0]), umax4(T.rc[1]), umax4(T.rc[2]), umax4(T.rc[3])});
        if (!__any(mx == 0xffffffffu) || spin > SPIN_CAP) break;
        __builtin_amdgcn_s_sleep(4);
        asm volatile("global_load_dwordx4 %0, %4, off sc1\n\tglobal_load_dwordx4 %1, %4, off offset:1024 sc1\n\tglobal_load_dwordx4 %2, %4, off offset:2048 sc1\n\tglobal_load_dwordx4 %3, %4, off offset:3072 sc1\n\ts_waitcnt vmcnt(0)"
                     : "=&v"(T.rc[0]), "=&v"(T.rc[1]), "=&v"(T.rc[2]), "=&v"(T.rc[3]) : "v"(T.rcv) : "memory");
    }
    u16* orp = out + (size_t)orow * D_ + ocol;
#pragma unroll
    for (int j = 0; j < 4; j += 2) {
        const f32x4 s0 = T.acc[0][j] + __builtin_bit_cast(f32x4, T.rc[j]), s1 = T.acc[0][j + 1] + __builtin_bit_cast(f32x4, T.rc[j + 1]);
        float v[8];
#pragma unroll
        for (int e = 0; e < 4; ++e) { auto r = __builtin_amdgcn_permlane16_swap(__float_as_uint(s0[e]), __float_as_uint(s1[e]), false, false); v[e] = __uint_as_float(r[0]); v[4 + e] = __uint_as_float(r[1]); }
        const u32x4 xv = T.xres[j >> 1];
        u32x4 o;
#pragma unroll
        for (int e = 0; e < 4; ++e) o[e] = pk_bf16(v[2 * e] + bf_lo(xv[e]), v[2 * e + 1] + bf_hi(xv[e]));
        *(GAS u32x4*)(size_t)(orp + 16 * j) = o;
    }
    STAMP_SEG(6);
    STAMP_OUT(ws, 0, wid, lane2);
    STAMP_OUT(ws, 1, wid, lane2);
}

__global__ __launch_bounds__(512, 2) void k_main(const char* __restrict__ wqkv, const char* __restrict__ wg, const char* __restrict__ wu, const char* __restrict__ wo, const char* __restrict__ wd, char* __restrict__ ws,
                                                const u16* __restrict__ x, u16* __restrict__ out) {
    extern __shared__ __attribute__((aligned(16))) char smem[];
    TL_ENTRY();
    asm volatile("" : "+s"(wqkv), "+s"(wg), "+s"(wu), "+s"(wo), "+s"(wd), "+s"(ws), "+s"(x), "+s"(out));
    LAS char* lds = (LAS char*)smem;
    const int tid = threadIdx.x, lane = tid & 63;
    const int wid = __builtin_amdgcn_readfirstlane(tid >> 6);
    const int bid = blockIdx.x, xcd = bid & 7, idx = bid >> 3;
    const int ct = 4 * xcd + (idx & 3), rt = idx >> 2;
    const unsigned ldsb = (unsigned)(size_t)lds;
    const int r8 = lane & 7, c8 = lane >> 3, row = 256 * rt + 8 * ct + r8;
    u32x4 xv[2];
    unsigned ep;
    asm volatile("global_load_dwordx4 %0, %3, %4\n\tglobal_load_dwordx4 %1, %3, %4 offset:128\n\ts_load_dword %2, %5, 0x0"
                 : "=&v"(xv[0]), "=&v"(xv[1]), "=&s"(ep) : "v"((unsigned)(row * 2048 + (16 * wid + c8) * 16)), "s"(x), "s"(ws + WS_EP) : "memory");
    G1Ld L;
    {
        const int c = wid * 64 + lane, rho = c / 14, cc = c - rho * 14;
        const int k = (rho & ~12) | ((rho & 4) << 1) | ((rho & 8) >> 1);
        const int c2 = cc < 12 ? cc : 11;
        const bool last = wid == 7;
        L.pa = ws + WS_XN + (size_t)(16 * rt + 2 * wid) * 1024;
        L.stepa = (unsigned)SLAB_B;
        L.va = lane * 16;
        L.la = ldsb + (1 + 2 * wid) * 1024;
        L.px = ws + WS_XN + (size_t)(wid < 2 ? 16 * rt + 14 + wid : rt == 0 ? 0 : 16 * rt - 1) * 1024;
        L.stepx = (unsigned)SLAB_B;
        L.vx = (unsigned)((wid >= 2 && rt == 0 ? (lane & 48) : lane) * 16);
        L.lx = wid < 2 ? ldsb + (15 + wid) * 1024 : ldsb;
        L.p2 = wqkv + ct * 64;
        L.step2 = 32u * 3072u * 2u;
        L.v2 = (unsigned)(k * 6144 + (c2 >> 2) * 2048 + (c2 & 3) * 16);
        L.l2 = ldsb + G1_BOFF + wid * 1024;
        L.pg = wg + ct * 224;
        L.pu = wu + ct * 224;
        L.stepg = 32u * 3584u * 2u;
        L.vg = (unsigned)(k * 7168 + cc * 16);
        L.ma = __builtin_amdgcn_readfirstlane(last ? 0 : -1); L.mb = L.ma; L.mx = __builtin_amdgcn_readfirstlane(wid == 3 ? 1 : -1); L.mw = -1; L.mt = __builtin_amdgcn_readfirstlane(last ? 2 : 0);
        const int j = lane < 60 ? lane / 20 : 2, i = lane < 60 ? lane - 20 * j : 19, trow = 32 * (8 + j) + idx;
        L.ta = i < 6 ? wqkv + (size_t)trow * 6144 + (i >> 1) * 2048 + xcd * 256 + (i & 1) * 128 : (i < 13 ? wg + (i - 6) * 128 : wu + (i - 13) * 128) + (size_t)trow * 7168 + xcd * 896;
        L.ts = i < 6 ? 96u * 6144u : 96u * 7168u;
        L.td = (unsigned)lane;
    }
    L.warm = wid < 4 ? wo + bid * 8192 + wid * 2048 : wd + bid * 28672 + (wid & 3) * 7168;
    L.lw = ldsb + G1_XOFF + (wid & 3) * 1024;
    L.pf = ws + WS_XB + (size_t)bid * 32768 + (wid & 3) * 8192;
    const int kh = (bid >> 2) & 1, side = kh ^ 1, rt2 = 2 * rt + ((bid >> 1) & 1), ct2 = 4 * (bid & 1) + (idx & 3);
    const int kg = wid >> 2, wm = wid & 3;
    G2St T;
    T.W.pwo = wo + (size_t)(side * 512) * 2048 + ct2 * 256;
    T.W.pwh = wd + (size_t)(side * 1792 + kg * 80) * 2048 + ct2 * 256;
    T.W.pwd = wd + (size_t)(96 + (wid >> 1) * 96) * 2048 + ct2 * 256;
    T.W.ldsw = ldsb + wid * 1024;
    STAMP_DECL STAMP_START();
    if (wid < 7) { g1_p2<0>(L); g1_pg<0>(L); g1_pu<0>(L); asm volatile("s_waitcnt vmcnt(3)" ::: "memory"); }
    else asm volatile("s_waitcnt vmcnt(0)" ::: "memory");
    asm volatile("s_waitcnt lgkmcnt(0)" : "+v"(xv[0]), "+v"(xv[1]), "+s"(ep) :: "memory");
    T.tag = __builtin_amdgcn_readfirstlane(ep * 1664525u + 1013904223u);
    OPAQUE_S(T.tag);
    P0S(2);
    {
        LAS float* red = (LAS float*)(lds + G1_XOFF);
        f32x2 f[8];
#pragma unroll
        for (int j = 0; j < 8; ++j) f[j] = (f32x2){bf_lo(xv[j >> 2][j & 3]), bf_hi(xv[j >> 2][j & 3])};
        {
            const u32x4 ones = {0xffffffffu, 0xffffffffu, 0xffffffffu, 0xffffffffu};
            asm volatile("global_store_dwordx4 %0, %1, %2 sc1\n\tglobal_store_dwordx4 %0, %1, %2 offset:1024 sc1\n\ts_nop 1"
                         :: "v"(L.va), "v"(ones), "s"(ws + WS_HO + (size_t)(64 * (ct >> 4) + (ct & 15)) * SLAB_B + (size_t)(16 * rt + 2 * wid) * 1024) : "memory");
        }
        const f32x2 s2 = ((f[0] + f[1]) + (f[2] + f[3])) + ((f[4] + f[5]) + (f[6] + f[7]));
        const float mw = sum_x8(s2[0] + s2[1]) * (1.0f / 128);
        f32x2 q2 = {0.f, 0.f};
#pragma unroll
        for (int j = 0; j < 8; ++j) { const f32x2 d = f[j] - mw; q2 = __builtin_elementwise_fma(d, d, q2); }
        const float qw = sum_x8(q2[0] + q2[1]);
        if (c8 == 0) *(LAS f32x2*)(red + r8 * 16 + wid * 2) = (f32x2){mw, qw};
        WAIT_LGKM0(); MEMFENCE(); BAR(); MEMFENCE();
        f32x4 st[4];
#pragma unroll
        for (int k = 0; k < 4; ++k) st[k] = *(LAS const f32x4*)(red + r8 * 16 + k * 4);
        const float mean = (((st[0][0] + st[0][2]) + (st[1][0] + st[1][2])) + ((st[2][0] + st[2][2]) + (st[3][0] + st[3][2]))) * 0.125f;
        float qt = 0.f, dt = 0.f;
#pragma unroll
        for (int k = 0; k < 4; ++k) { const float d0 = st[k][0] - mean, d1 = st[k][2] - mean; qt += st[k][1] + st[k][3]; dt = fmaf(d0, d0, fmaf(d1, d1, dt)); }
        const float mb = rbf(mean);
        const float vb = rbf(fmaf(dt, 128.f, qt) * (1.0f / D_));
        const float den = rbf(__builtin_amdgcn_sqrtf(rbf(vb + 1e-5f)));
        const float r0 = __builtin_amdgcn_rcpf(den), rc = fmaf(fmaf(-den, r0, 1.0f), r0, r0);
#pragma unroll
        for (int i = 0; i < 2; ++i) {
            u32x4 o;
#pragma unroll
            for (int j = 0; j < 4; ++j) {
                const f32x2 c = f[i * 4 + j] - mb;
                const unsigned d = pk_bf16(c[0], c[1]);
                const f32x2 y = (f32x2){bf_lo(d), bf_hi(d)} * rc;
                o[j] = pk_bf16(y[0], y[1]);
            }
            const int ch = 16 * wid + 8 * i + c8;
            asm volatile("global_store_dwordx4 %0, %1, %2 sc1\n\ts_nop 1" :: "v"((unsigned)((ch >> 2) * (int)SLAB_B + (row >> 4) * 1024 + ((ch & 3) * 16 + (row & 15)) * 16)), "v"(o), "s"(ws + WS_XN) : "memory");
        }
        P0S(3);
        asm volatile("s_waitcnt vmcnt(0)\n\ts_barrier" ::: "memory");
        P0S(4);
        if (wid < 7) {
            if (!W_EARLY) { g1_p2<0>(L); g1_pg<0>(L); g1_pu<0>(L); }
            g1_p2<1>(L); g1_pg<1>(L); g1_pu<1>(L);
        } else {
            const bool next = ct >= 30 && rt < 7;
            asm volatile("s_mov_b64 exec, %3\n\tglobal_store_dword %0, %1, %2 sc1\n\ts_mov_b64 exec, -1"
                         :: "v"((unsigned)((rt * 32 + lane) * 256 + (lane < 32 ? ct : ct + 2) * 4)), "v"(T.tag), "s"(ws + WS_LF), "s"(next ? ~0ull : 0xffffffffull) : "memory");
            flags_wait<LN_SLEEP>((u32x2){~T.tag, 0u}, ws + WS_LF + (rt * 32 + ct) * 256, (unsigned)(lane < 16 || rt == 0 ? (lane & 15) * 8 : 128), T.tag);
        }
        MEMFENCE(); BAR(); MEMFENCE();
    }
    STAMP_SEG(1);
    if (wid < 4) {
        g1_body<0>(L, T, lds, ws, rt, ct, wm, kh STAMP_PASS); g2_body<0>(T, lds, ws, x, out, rt, ct, wm STAMP_PASS2);
        TL_EXIT(ws, 0); TL_EXIT(ws, 1); TL_EXIT(ws, 2);
        __builtin_amdgcn_endpgm();
    }
    g1_body<1>(L, T, lds, ws, rt, ct, wm, kh STAMP_PASS); g2_body<1>(T, lds, ws, x, out, rt, ct, wm STAMP_PASS2);
    TL_EXIT(ws, 0); TL_EXIT(ws, 1); TL_EXIT(ws, 2);
    __builtin_amdgcn_endpgm();
}

extern "C" void kernel_launch(void* const* d_in, const int* in_sizes, int n_in,
                              void* d_out, int out_size, void* d_ws, size_t ws_size,
                              hipStream_t stream) {
    const u16* x = (const u16*)d_in[0];
    const char* wqkv = (const char*)d_in[1];
    const char* wo = (const char*)d_in[2];
    const char* wg = (const char*)d_in[4];
    const char* wu = (const char*)d_in[5];
    const char* wd = (const char*)d_in[6];
    char* ws = (char*)d_ws;
    (void)hipFuncSetAttribute((const void*)k_main, hipFuncAttributeMaxDynamicSharedMemorySize, G1_LDS);
    k_main<<<256, 512, G1_LDS, stream>>>(wqkv, wg, wu, wo, wd, ws, x, (u16*)d_out);
}
```

```cpp
#include <hip/hip_runtime.h>
#include <hip/hip_bf16.h>
#include <stdint.h>

typedef __bf16 bf16x8 __attribute__((ext_vector_type(8)));
typedef __bf16 bf16x4 __attribute__((ext_vector_type(4)));
typedef __bf16 bf16x2 __attribute__((ext_vector_type(2)));
typedef float f32x4 __attribute__((ext_vector_type(4)));
typedef float f32x2 __attribute__((ext_vector_type(2)));
typedef unsigned u32x4 __attribute__((ext_vector_type(4)));
typedef unsigned u32x2 __attribute__((ext_vector_type(2)));
typedef unsigned short u16;
#define LAS __attribute__((address_space(3)))
#define GAS __attribute__((address_space(1)))
#define DEVI __device__ __forceinline__

constexpr int S_ = 2048, D_ = 1024, F_ = 3584;
constexpr int MB_ = S_ / 16;
constexpr size_t SLAB_B = (size_t)MB_ * 1024;
constexpr size_t WS_XN = 0;
constexpr size_t WS_HO = 32 * SLAB_B;
constexpr size_t WS_XB = WS_HO + 144 * SLAB_B;
constexpr size_t WS_FL = WS_XB + 256 * 32768;
constexpr size_t WS_LF = WS_FL + 32768;
constexpr size_t WS_EP = WS_LF + 65536;

DEVI unsigned pk_bf16(float lo, float hi) {
    bf16x2 v = {(__bf16)lo, (__bf16)hi};
    return __builtin_bit_cast(unsigned, v);
}
DEVI float bf_lo(unsigned u) { return __uint_as_float(u << 16); }
DEVI float bf_hi(unsigned u) { return __uint_as_float(u & 0xffff0000u); }
DEVI float rbf(float f) { return (float)(__bf16)f; }
DEVI bf16x8 as_frag(u32x4 v) { return __builtin_bit_cast(bf16x8, v); }
#define MFMA16(a, b, c) __builtin_amdgcn_mfma_f32_16x16x32_bf16((a), (b), (c), 0, 0, 0)
#define SBAR() __builtin_amdgcn_sched_barrier(0)
#define MFMA_ACC(c, a, b) asm volatile("v_mfma_f32_16x16x32_bf16 %0, %1, %2, %0" : "+v"(c) : "v"(a), "v"(b))
#define MFMA_NEW(c, a, b) asm volatile("v_mfma_f32_16x16x32_bf16 %0, %1, %2, 0" : "=&v"(c) : "v"(a), "v"(b))
#define MFMA_DRAIN() asm volatile("s_nop 15" ::: "memory")
#define BAR() __builtin_amdgcn_s_barrier()

#define TL_ENTRY()
#define TL_EXIT(ws, kern)
#define STAMP_DECL
#define STAMP_ARGS
#define STAMP_PASS
#define STAMP_ARGS2
#define STAMP_PASS2
#define STAMP_SEG(i)
#define STAMP_START()
#define STAMP_SEGI(i)
#define STAMP_OUT(ws, kern, wid, lane)

#define P0S(i)
DEVI int fresh_lane() { int l; asm volatile("v_mbcnt_lo_u32_b32 %0, -1, 0\n\tv_mbcnt_hi_u32_b32 %0, -1, %0" : "=v"(l)); return l; }

DEVI bf16x8 tr_frag(LAS const char* p, int off0, int off1) {
    bf16x4 lo = __builtin_amdgcn_ds_read_tr16_b64_v4bf16((LAS bf16x4*)(p + off0));
    bf16x4 hi = __builtin_amdgcn_ds_read_tr16_b64_v4bf16((LAS bf16x4*)(p + off1));
    return __builtin_shufflevector(lo, hi, 0, 1, 2, 3, 4, 5, 6, 7);
}

DEVI void glds16(const char* sbase, unsigned voff, unsigned ldsdst) {
    unsigned keep;
    asm volatile("s_mov_b32 %0, m0\n\ts_mov_b32 m0, %3\n\ts_nop 0\n\tglobal_load_lds_dwordx4 %1, %2\n\ts_mov_b32 m0, %0"
                 : "=&s"(keep) : "v"(voff), "s"(sbase), "s"(ldsdst) : "memory");
}

constexpr int G1_AST = 17 * 1024;
constexpr int G1_BST = 21 * 1024;
constexpr int G1_REG = 7168;
constexpr int G1_BOFF = 4 * G1_AST;
constexpr int G1_XOFF = G1_BOFF + 4 * G1_BST;
constexpr int G1_LDS = G1_XOFF + 8192;

#define W_WHEN 0
#define T_WHEN 1
#define T_J 3
#define T_EVERY 1
#define T_S0 2
static_assert((32 - T_S0) % T_J == 0, "");
#define T_BURST 2
typedef unsigned long long u64;
#define FRAG_REGS "v191", "v192", "v193", "v194", "v195", "v196", "v197", "v198", "v199", "v200", "v201", "v202", "v203", "v204", "v205", "v206", "v207", "v208", "v209", "v210", "v211", "v212", "v213", "v214", "v215", "v216", "v217", "v218", "v219", "v220", "v221", "v222", "v223", "v224", "v225", "v226", "v227", "v228", "v229", "v230", "v231", "v232", "v233", "v234", "v235", "v236", "v237", "v238", "v239", "v240", "v241", "v242", "v243", "v244", "v245", "v246", "v247", "v248", "v249", "v250", "v251", "v252", "v253", "v254", "v255"
constexpr int FA = 192, FB = 208, FAH = 248, FBH = 252;
struct G1Ld {
    const char *pa, *p2, *pg, *pu, *px;
    unsigned stepa, step2, stepg, stepx;
    unsigned va, v2, vg, vx;
    unsigned la, l2, lx;
    char* pf;
    int ma, mb, mx;
    const char *ta, *tb; unsigned ts, tt; int mt, m7;
};

#define WAIT_LGKM0() __builtin_amdgcn_s_waitcnt(0xC07F)
#define MEMFENCE() asm volatile("" ::: "memory")
#define OPAQUE_S(x) asm volatile("" : "+s"(x))

template <int B, int YES, int NO> DEVI int s_if_lt(int a) { int r; asm("s_cmp_lt_u32 %1, %2\n\ts_cselect_b32 %0, %3, %4" : "=s"(r) : "s"(a), "i"(B), "i"(YES), "i"(NO) : "scc"); return r; }
template <int LOFF, int KIND>
DEVI void g1_piece(const char* base, unsigned voff, unsigned lds, int msk) {
    asm volatile("s_cmp_eq_u32 %3, 0\n\ts_cbranch_scc1 .Lsk_%=\n\ts_add_u32 m0, %2, %4\n\ts_mov_b32 exec_lo, %3\n\ts_ashr_i32 exec_hi, %3, 31\n\t"
                 ".if %5 == 0\n\tglobal_load_lds_dwordx4 %0, %1\n\t.else\n\tglobal_load_lds_dwordx4 %0, %1 sc1\n\t.endif\n\t"
                 ".if %5 == 2\n\tglobal_load_lds_dwordx4 %0, %1 offset:1024 sc1\n\t.endif\n\t"
                 "s_mov_b64 exec, -1\n.Lsk_%=:"
                 :: "v"(voff), "s"(base), "s"(lds), "s"(msk), "i"(LOFF), "i"(KIND) : "memory", "scc");
}
template <int SLOT> DEVI void g1_pa(G1Ld& L) { g1_piece<SLOT * G1_AST, 2>(L.pa, L.va, L.la, L.ma); L.pa += L.stepa; OPAQUE_S(L.pa); }
template <int SLOT> DEVI void g1_px(G1Ld& L) { g1_piece<SLOT * G1_AST, 1>(L.px, L.vx, L.lx, L.mx); L.px += L.stepx; OPAQUE_S(L.px); }
template <int SLOT> DEVI void g1_p2(G1Ld& L) { g1_piece<SLOT * G1_BST, 0>(L.p2, L.v2, L.l2, L.mb); L.p2 += L.step2; OPAQUE_S(L.p2); }
template <int SLOT> DEVI void g1_pg(G1Ld& L) { g1_piece<SLOT * G1_BST + G1_REG, 0>(L.pg, L.vg, L.l2, L.mb); L.pg += L.stepg; OPAQUE_S(L.pg); }
template <int SLOT> DEVI void g1_pu(G1Ld& L) { g1_piece<SLOT * G1_BST + 2 * G1_REG, 0>(L.pu, L.vg, L.l2, L.mb); L.pu += L.stepg; OPAQUE_S(L.pu); }
template <int GRP, int SLOT> DEVI void g1_issue(G1Ld& L) { g1_pa<SLOT>(L); if (GRP == 0) g1_px<SLOT>(L); g1_p2<SLOT>(L); g1_pg<SLOT>(L); g1_pu<SLOT>(L); }
DEVI void g1_touch(const char*& p, unsigned step, int i, int n) {
    asm volatile("s_cmp_ge_u32 %1, %2\n\ts_cbranch_scc1 .Lsk_%=\n\tglobal_load_dword v191, %0, off\n.Lsk_%=:" :: "v"(p), "s"(i), "s"(n) : "memory", "scc", "v191");
    p += step; asm volatile("" : "+v"(p));
}
template <int GRP> DEVI void g1_tail(G1Ld& L, int it) {
    OPAQUE_S(it);
    const bool tail = it == 7;
    L.p2 = tail ? L.p2 - L.step2 : L.p2; L.pg = tail ? L.pg - L.stepg : L.pg; L.pu = tail ? L.pu - L.stepg : L.pu;
    L.step2 = tail ? 0u : L.step2; L.stepg = tail ? 0u : L.stepg;
    L.mb = tail ? L.mb & 1 : L.mb;
    if (GRP == 0) { L.px = tail ? L.px - L.stepx : L.px; L.stepx = tail ? 0u : L.stepx; L.mx = tail ? 1 : L.mx; }
    L.pa = tail ? L.pa - L.stepa : L.pa; L.ma = tail ? L.ma & 1 : L.ma;
    L.stepa = tail ? 0u : L.stepa;
}

struct G1Ptr { unsigned pA, pH, pB01, pB23, pBh01, pBh23; };

template <int REG, int OFF> DEVI void ds_tr(unsigned base) { asm volatile("ds_read_b64_tr_b16 v[%c1:%c2], %0 offset:%c3" :: "v"(base), "i"(REG), "i"(REG + 1), "i"(OFF) : "memory"); }
template <int REG, int OFF> DEVI void ds_128(unsigned base) { asm volatile("ds_read_b128 v[%c1:%c2], %0 offset:%c3" :: "v"(base), "i"(REG), "i"(REG + 3), "i"(OFF) : "memory"); }
template <int RA_, int RB_> DEVI void mfma_fr(f32x4& c) { asm volatile("v_mfma_f32_16x16x32_bf16 %0, v[%c1:%c2], v[%c3:%c4], %0" : "+v"(c) : "i"(RA_), "i"(RA_ + 3), "i"(RB_), "i"(RB_ + 3)); }
template <int GRP, int SL, int n, int h> DEVI void g1_ldb(const G1Ptr& P) {
    constexpr int off = (SL & 1) * G1_BST + (GRP == 0 ? (n < 6 ? n * 32 : (n < 8 ? G1_REG + (n - 1) * 32 : 2 * G1_REG + (n - 3) * 32))
                                                      : (n < 5 ? G1_REG + n * 32 : 2 * G1_REG + (n - 5) * 32));
    ds_tr<FB + 4 * n + 2 * h, off + h * 8 * 224>((SL < 2) ? P.pB01 : P.pB23);
}
template <int SL, int m> DEVI void g1_lda(const G1Ptr& P) { ds_128<FA + 4 * m, SL * G1_AST + (1 + m) * 1024>(P.pA); }
template <int SL> DEVI void g1_ldah(const G1Ptr& P) { ds_128<FAH, SL * G1_AST>(P.pH); }
template <int SL, int h> DEVI void g1_ldbh(const G1Ptr& P) { ds_tr<FBH + 2 * h, (SL & 1) * G1_BST + h * 8 * 224>((SL < 2) ? P.pBh01 : P.pBh23); }

template <int GRP, int ST>
DEVI void g1_stage(f32x4 (&acc)[4][10], f32x4& acch, G1Ld& L, int it, const G1Ptr& P) {
    constexpr int NX = (ST + 1) & 3, IS = (ST + 3) & 3;
#define MM(m, n) do { if (GRP == 0 && ((n) == 4 || (n) == 5)) mfma_fr<FA + 4 * (m), FB + 4 * (n)>(acc[m][n]); else mfma_fr<FB + 4 * (n), FA + 4 * (m)>(acc[m][n]); } while (0)
#define RB(n, h) g1_ldb<GRP, NX, n, h>(P)
#define RC(n, h) g1_ldb<GRP, ST, n, h>(P)
#define RA(m) g1_lda<NX, m>(P)
#define G0(x) do { if (GRP == 0) { x; } } while (0)
    asm volatile("s_waitcnt lgkmcnt(9)" ::: "memory");
    MM(0, 0); RC(8, 0);
    asm volatile("s_waitcnt lgkmcnt(9)" ::: "memory");
    MM(1, 0); RC(8, 1);
    MM(1, 1); RC(9, 0); MM(0, 1); RC(9, 1);
    MM(0, 2); G0(g1_pa<IS>(L)); MM(1, 2); G0(g1_px<IS>(L));
    MM(1, 3); G0(g1_p2<IS>(L)); MM(0, 3); G0(g1_pg<IS>(L));
    MM(0, 4); G0(g1_pu<IS>(L)); MM(1, 4);
    G0((mfma_fr<FBH, FAH>(acch)));
    asm volatile("s_waitcnt vmcnt(%0) lgkmcnt(0)\n\ts_barrier" :: "i"(GRP == 0 ? 12 : 10) : "memory", FRAG_REGS);
    if (GRP == 1) {
        if (ST == 0) g1_tail<1>(L, it);
        g1_issue<1, ST>(L);
        if (ST % T_EVERY == 0) g1_touch(L.ta, L.ts, it * (4 / T_EVERY) + ST / T_EVERY, L.mt);
        if (ST & 1) g1_touch(L.tb, L.tt, it - 2, L.m7);
        if (ST == 2) {
            const u32x4 ones = {0xffffffffu, 0xffffffffu, 0xffffffffu, 0xffffffffu};
            asm volatile("global_store_dwordx4 %0, %1, %2 sc1\n\ts_nop 1" :: "v"(L.va), "v"(ones), "s"(L.pf) : "memory", FRAG_REGS);
            L.pf += 1024; OPAQUE_S(L.pf);
        }
    }
    MM(2, 0); MM(3, 0); RB(0, 0);
    MM(3, 1); RB(0, 1); MM(2, 1); RB(1, 0);
    MM(2, 2); RB(1, 1); MM(3, 2); RB(2, 0);
    MM(3, 3); RB(2, 1); MM(2, 3); RB(3, 0);
    MM(2, 4); RB(3, 1); MM(3, 4); RB(4, 0);
    MM(0, 5); RB(4, 1); MM(1, 5); G0(g1_ldah<NX>(P));
    MM(1, 6); G0((g1_ldbh<NX, 0>(P))); MM(0, 6); G0((g1_ldbh<NX, 1>(P)));
    MM(0, 7); MM(1, 7);
    MM(1, 8); MM(0, 8);
    MM(0, 9); RA(0); MM(1, 9); RA(1);
    MM(2, 5); MM(3, 5); RB(5, 0);
    MM(3, 6); RB(5, 1); MM(2, 6); RB(6, 0);
    MM(2, 7); RB(6, 1); MM(3, 7); RB(7, 0);
    MM(3, 8); RB(7, 1); MM(2, 8);
    MM(2, 9); RA(2); MM(3, 9); RA(3);
#undef MM
#undef RB
#undef RC
#undef RA
#undef G0
}

DEVI void st16(char* dst, u32x4 v) {
    asm volatile("global_store_dwordx4 %0, %1, off sc1\n\ts_nop 1" :: "v"(dst), "v"(v) : "memory");
}

DEVI unsigned glu2(float g0, float g1, float u0, float u1) {
    const f32x2 t = (f32x2){g0, g1} * -1.4426950408889634f;
    const f32x2 d = (f32x2){__builtin_amdgcn_exp2f(t[0]), __builtin_amdgcn_exp2f(t[1])} + 1.0f;
    const f32x2 h = (f32x2){u0, u1} * (f32x2){__builtin_amdgcn_rcpf(d[0]), __builtin_amdgcn_rcpf(d[1])};
    return pk_bf16(h[0], h[1]);
}

DEVI void glu_store(const f32x4& g0, const f32x4& u0, const f32x4& g1, const f32x4& u1, char* dst) {
    unsigned a0 = glu2(g0[0], g0[1], u0[0], u0[1]), a1 = glu2(g0[2], g0[3], u0[2], u0[3]);
    unsigned b0 = glu2(g1[0], g1[1], u1[0], u1[1]), b1 = glu2(g1[2], g1[3], u1[2], u1[3]);
    auto r0 = __builtin_amdgcn_permlane16_swap(a0, b0, false, false);
    auto r1 = __builtin_amdgcn_permlane16_swap(a1, b1, false, false);
    st16(dst, (u32x4){r0[0], r1[0], r0[1], r1[1]});
    SBAR();
}

#define LN_SLEEP 2
#define POLL_SLEEP 8
#define G2_RCV 34
#define G2_PLACE 1
#define G2_NS 8
constexpr int G2_ST = 16384;
constexpr int G2_WL = G2_NS - 1;
constexpr int G2_XOFF = G2_NS <= 8 ? G2_NS * G2_ST : 0;
constexpr int G2_F = 32, G2_XST = 33;
constexpr int G2_FY = 8, G2_FZ = 12;
constexpr int G2_PA = 1, G2_PY = 9, G2_PZ = 15;
constexpr unsigned SPIN_CAP = 1u << 17;
static_assert(G2_RCV > G2_F && G2_RCV <= 35 && G2_NS >= 6 && G2_NS <= 9, "");
constexpr int g2_grp(int t) { return t < G2_F ? t : G2_F + 2 * (t - G2_F); }
constexpr int g2_afirst(int s) { return s < 2 ? 2 + 2 * s : s + 4 <= 35 ? g2_grp(s + 4) : 40; }
constexpr int g2_alast(int s) { return g2_afirst(s + 1); }
constexpr int g2_tops(int s) { return s == 0 || s == G2_FY || s == G2_FZ ? 1 : 0; }
constexpr int g2_wops(int s) { return s + G2_WL <= 35 ? 2 : 0; }
constexpr int g2_xops(int s) { return (s < 8 && (s & 1) ? 1 : 0) + (s == G2_PA || s == G2_PY || s == G2_PZ ? 1 : 0) + (s == G2_F ? 4 : 0) + (s == G2_XST ? 2 : 0) + (s == G2_RCV ? 4 : 0); }
constexpr int g2_wait(int t) {
    const int need = g2_grp(t + 1) - 1;
    int n = 4, last = need < 2 ? 2 * need + 2 : 0;
    for (int s = 0; s < t; ++s) {
        n += g2_tops(s) + g2_wops(s);
        if (s + G2_WL == t + 1 && n > last) last = n;
        for (int g = g2_afirst(s); g < g2_alast(s); ++g) { n += 2; if (g == need && n > last) last = n; }
        n += g2_xops(s);
        if ((t == 4 && s == G2_PA) || (t == 12 && s == G2_PY) || (t == 20 && s == G2_PZ) || (t == G2_FY && s == 3) || (t == G2_FZ && s == 7)) last = n > last ? n : last;
    }
    return n - last;
}

struct G2W {
    const char *pwo, *pwh, *pwd;
    unsigned vw0, vw1, vw2, ldsw;
};
struct G2St {
    G2W W;
    const char *pa, *pad;
    unsigned va0, va1;
    LAS const char* pB[(G2_NS + 3) / 4][4];
    LAS char* xw; LAS const char* xr;
    char* snd; const char* rcv; const u16* xp;
    f32x4 dg[2][4], du[2][4]; char* dd[2]; unsigned hl, hq[4];
    char *flagA, *flagZ; const char* flags; unsigned foa, foz, tag; u32x2 hf; bool poller;
    u32x4 A[40][2];
    bf16x4 B[8][2];
    f32x4 acc[2][8];
    u32x4 xres[2], rc[4];
};
template <int LOFF> DEVI void g2_glds(const char* base, unsigned voff, unsigned lds) {
    asm volatile("s_add_u32 m0, %2, %3\n\ts_nop 0\n\tglobal_load_lds_dwordx4 %0, %1" :: "v"(voff), "s"(base), "s"(lds), "i"(LOFF) : "memory", "scc");
}
template <int ST, int I> DEVI void g2_w(const G2W& W) {
    constexpr int SLOT = ST % G2_NS;
    const char* src = ST < 8 ? W.pwo + ST * 64 * 2048 : ST < G2_F ? W.pwh + (((ST - 8) & 7) * 224 + ((ST - 8) >> 3) * 32) * 2048 : W.pwd + (ST - G2_F) * 896 * 2048;
    g2_glds<SLOT * G2_ST + I * 8192>(src, I == 0 ? W.vw0 : ST < G2_F ? W.vw1 : W.vw2, W.ldsw);
}
template <int G> DEVI const char* g2_asrc(const G2St& T) {
    return G < G2_F ? T.pa + (size_t)G * 2 * SLAB_B : T.pad + (size_t)(G - G2_F) * 2 * SLAB_B; }
template <int G, int I> DEVI void g2_a(G2St& T) {
    asm volatile("global_load_dwordx4 %0, %1, %2 sc1" : "=v"(T.A[G][I]) : "v"(I ? T.va1 : T.va0), "s"(g2_asrc<G>(T)) : "memory");
}
template <int ST> DEVI bf16x4 g2_ldb(G2St& T, int n, int h) {
    constexpr int SLOT = ST % G2_NS;
    return __builtin_amdgcn_ds_read_tr16_b64_v4bf16((LAS bf16x4*)(T.pB[SLOT / 4][n & 3] + (SLOT % 4) * G2_ST + (n >> 2) * 8192 + h * 512));
}
DEVI bf16x8 cat(bf16x4 lo, bf16x4 hi) { return __builtin_shufflevector(lo, hi, 0, 1, 2, 3, 4, 5, 6, 7); }
template <int OFF> DEVI void flag_set(char* p, unsigned tag) { const unsigned zero = 0u; asm volatile("s_mov_b64 exec, 1\n\tglobal_store_dword %0, %1, %2 offset:%3 sc1\n\ts_mov_b64 exec, -1" :: "v"(zero), "v"(tag), "s"(p), "i"(OFF) : "memory"); }
template <int SLEEP> DEVI void flags_wait(u32x2 f, const char* base, unsigned voff, unsigned tag) {
#pragma unroll 1
    for (unsigned spin = 0; __any(f[0] != tag || f[1] != tag) && spin < SPIN_CAP; ++spin) {
        if (spin) __builtin_amdgcn_s_sleep(SLEEP);
        asm volatile("global_load_dwordx2 %0, %1, %2 sc1\n\ts_waitcnt vmcnt(0)" : "=&v"(f) : "v"(voff), "s"(base) : "memory");
    }
}
DEVI unsigned umax4(u32x4 v) { const unsigned a = v[0] > v[1] ? v[0] : v[1], b = v[2] > v[3] ? v[2] : v[3]; return a > b ? a : b; }
DEVI u32x4 never_ones(u32x4 w) {
#pragma unroll
    for (int e = 0; e < 4; ++e) w[e] = w[e] < 0xfffffffeu ? w[e] : 0xfffffffeu;
    return w;
}

template <int KG, int t>
DEVI void g2_stage(G2St& T) {
    constexpr bool TB = t >= G2_F, ISW = g2_wops(t) != 0, RL = t < 35, CHK = t == 4 || t == 12 || t == 20;
    constexpr int G0 = g2_grp(t), G1 = TB ? G0 + 1 : G0, NA = g2_alast(t) - g2_afirst(t), GN = NA ? g2_afirst(t) : 0, GN1 = NA == 2 ? GN + 1 : GN;
    u32x4 (&Aa)[2] = T.A[G0]; u32x4 (&Ab)[2] = T.A[G1];
    if (TB) asm volatile("s_waitcnt vmcnt(%4)" : "+v"(Aa[0]), "+v"(Aa[1]), "+v"(Ab[0]), "+v"(Ab[1]) : "i"(g2_wait(t)) : "memory");
    else if (CHK) asm volatile("s_waitcnt vmcnt(%3)" : "+v"(Aa[0]), "+v"(Aa[1]), "+v"(T.hf) : "i"(g2_wait(t)) : "memory");
    else asm volatile("s_waitcnt vmcnt(%2)" : "+v"(Aa[0]), "+v"(Aa[1]) : "i"(g2_wait(t)) : "memory");
    if (t == 0) flag_set<0>(T.flagA, T.tag);
    if (t == G2_FY) flag_set<128>(T.flagA, T.tag);
    if (t == G2_FZ) flag_set<0>(T.flagZ, T.tag);
    if (t < 8) {
#pragma unroll 1
        for (unsigned spin = 0; spin < SPIN_CAP; ++spin) {
            const unsigned a = umax4(Aa[0]), b = umax4(Aa[1]);
            if (!__any((a > b ? a : b) == 0xffffffffu)) break;
            __builtin_amdgcn_s_sleep(POLL_SLEEP);
            asm volatile("global_load_dwordx4 %0, %2, %4 sc1\n\tglobal_load_dwordx4 %1, %3, %4 sc1\n\ts_waitcnt vmcnt(0)" : "=&v"(Aa[0]), "=&v"(Aa[1]) : "v"(T.va0), "v"(T.va1), "s"(g2_asrc<G0>(T)) : "memory");
        }
    }
    if (KG == 0 && CHK && T.poller) flags_wait<POLL_SLEEP>(T.hf, t == 12 ? T.flags + 128 : T.flags, t == 20 ? T.foz : T.foa, T.tag);
    if (t == G2_F) WAIT_LGKM0();
    { MEMFENCE(); SBAR(); BAR(); MEMFENCE(); SBAR(); }
    f32x4 ho[4];
    if (t == G2_F) {
#pragma unroll
        for (int j = 0; j < 4; ++j) ho[j] = *(LAS const f32x4*)(T.xr + j * 1024);
        SBAR();
    }
#define AB2DS true
#define RL2(n, h) do { if (AB2DS && RL && (n) >= 0) { T.B[(n) >= 0 ? (n) : 0][h] = g2_ldb<t + 1>(T, (n) >= 0 ? (n) : 0, h); SBAR(); } } while (0)
#define P2(n) do { constexpr int c_ = TB ? ((n) & 3) : (n); u32x4 (&A_)[2] = (n) < 4 ? Aa : Ab; const bf16x8 b_ = cat(T.B[n][0], T.B[n][1]); \
                   if (t == 0) MFMA_NEW(T.acc[(n) & 1][c_], b_, A_[(n) & 1]); else MFMA_ACC(T.acc[(n) & 1][c_], b_, A_[(n) & 1]); SBAR(); RL2((n) - 1, 1); D((n) * 2); \
                   if (t == 0) MFMA_NEW(T.acc[~(n) & 1][c_], b_, A_[~(n) & 1]); else MFMA_ACC(T.acc[~(n) & 1][c_], b_, A_[~(n) & 1]); SBAR(); RL2(n, 0); D((n) * 2 + 1); } while (0)
#define VW(i) do { if (ISW) { g2_w<ISW ? t + G2_WL : 0, i>(T.W); SBAR(); } } while (0)
#define VA(i) do { if (NA == 2) { g2_a<(i) < 2 ? GN : GN1, (i) & 1>(T); SBAR(); } else if (NA && ((i) & 1)) { g2_a<GN, (i) / 2>(T); SBAR(); } } while (0)
    f32x4& dg = T.dg[(t >> 2) & 1][t & 3]; f32x4& du = T.du[(t >> 2) & 1][t & 3];
#define D(i) do { if (t < 8 && (i) < 13) { asm volatile("" : "+v"(dg)); \
        if ((i) == 0) dg = dg * -1.4426950408889634f; \
        else if ((i) < 5) dg[((i) - 1) & 3] = __builtin_amdgcn_exp2f(dg[((i) - 1) & 3]); \
        else if ((i) == 5) dg = dg + 1.0f; \
        else if ((i) < 10) dg[((i) - 6) & 3] = __builtin_amdgcn_rcpf(dg[((i) - 6) & 3]); \
        else if ((i) == 10) dg = dg * du; \
        else { T.hq[(t & 1) * 2 + (((i) - 11) & 1)] = pk_bf16(dg[(((i) - 11) & 1) * 2], dg[(((i) - 11) & 1) * 2 + 1]); asm volatile("" : "+v"(T.hq[(t & 1) * 2 + (((i) - 11) & 1)])); } \
        if ((i) < 11) asm volatile("" : "+v"(dg)); \
        SBAR(); } } while (0)
    if (KG == 1) { VW(0); VW(1); VA(0); VA(1); VA(2); VA(3); P2(0); P2(1); P2(2); P2(3); P2(4); P2(5); P2(6); P2(7); }
    else { P2(0); P2(1); P2(2); P2(3); VW(0); P2(4); VW(1); P2(5); VA(0); VA(1); P2(6); VA(2); VA(3); P2(7); }
    RL2(7, 1);
#undef RL2
#undef VW
#undef VA
#undef D
#undef P2
    if (t < 8 && (t & 1)) {
        auto r0 = __builtin_amdgcn_permlane16_swap(T.hq[0], T.hq[2], false, false);
        auto r1 = __builtin_amdgcn_permlane16_swap(T.hq[1], T.hq[3], false, false);
        const u32x4 w = {r0[0], r1[0], r0[1], r1[1]};
        asm volatile("global_store_dwordx4 %0, %1, %2 offset:%3 sc1\n\ts_nop 1" :: "v"(T.hl), "v"(w), "s"(T.dd[(t >> 2) & 1]), "i"((t & 2) * 1024) : "memory");
        SBAR();
    }
    if (t == G2_PA || t == G2_PY || t == G2_PZ) {
        asm volatile("global_load_dwordx2 %0, %1, %2 offset:%3 sc1" : "=v"(T.hf) : "v"(t == G2_PZ ? T.foz : T.foa), "s"(T.flags), "i"(t == G2_PY ? 128 : 0) : "memory"); SBAR();
    }
    if (t == G2_F - 1) {
        MFMA_DRAIN(); SBAR();
#pragma unroll
        for (int j = 0; j < 4; ++j) *(LAS f32x4*)(T.xw + j * 1024) = T.acc[1][4 + j];
        SBAR();
    }
    if (t == G2_F) {
#pragma unroll
        for (int j = 0; j < 4; ++j) {
            const u32x4 w = never_ones(__builtin_bit_cast(u32x4, T.acc[0][4 + j] + ho[j]));
            asm volatile("global_store_dwordx4 %0, %1, off offset:%2 sc1\n\ts_nop 1" :: "v"(T.snd), "v"(w), "i"(j * 1024) : "memory");
        }
        SBAR();
    }
    if (t == G2_XST) { asm volatile("global_load_dwordx4 %0, %2, off\n\tglobal_load_dwordx4 %1, %2, off offset:64" : "=&v"(T.xres[0]), "=&v"(T.xres[1]) : "v"(T.xp) : "memory"); SBAR(); }
    if (t == G2_RCV) {
        asm volatile("global_load_dwordx4 %0, %4, off sc1\n\tglobal_load_dwordx4 %1, %4, off offset:1024 sc1\n\tglobal_load_dwordx4 %2, %4, off offset:2048 sc1\n\tglobal_load_dwordx4 %3, %4, off offset:3072 sc1"
                     : "=&v"(T.rc[0]), "=&v"(T.rc[1]), "=&v"(T.rc[2]), "=&v"(T.rc[3]) : "v"(T.rcv) : "memory");
        SBAR();
    }
}
template <int KG, int t, int E> DEVI void g2_run(G2St& T) { g2_stage<KG, t>(T); if constexpr (t + 1 < E) g2_run<KG, t + 1, E>(T); }

DEVI unsigned bsel(unsigned m, unsigned a, unsigned b) { return (m & a) | (~m & b); }
typedef short s16x4 __attribute__((ext_vector_type(4)));
#define MFMA16K16(a, b, c) __builtin_amdgcn_mfma_f32_16x16x16bf16_1k(__builtin_bit_cast(s16x4, a), __builtin_bit_cast(s16x4, b), (c), 0, 0, 0)
template <bool MAX> DEVI float rows_reduce(const float (&v)[4]) {
    auto a = __builtin_amdgcn_permlane16_swap(__float_as_uint(v[0]), __float_as_uint(v[1]), false, false);
    auto b = __builtin_amdgcn_permlane16_swap(__float_as_uint(v[2]), __float_as_uint(v[3]), false, false);
    const float t = MAX ? fmaxf(__uint_as_float(a[0]), __uint_as_float(a[1])) : __uint_as_float(a[0]) + __uint_as_float(a[1]);
    const float u = MAX ? fmaxf(__uint_as_float(b[0]), __uint_as_float(b[1])) : __uint_as_float(b[0]) + __uint_as_float(b[1]);
    auto c = __builtin_amdgcn_permlane32_swap(__float_as_uint(t), __float_as_uint(u), false, false);
    return MAX ? fmaxf(__uint_as_float(c[0]), __uint_as_float(c[1])) : __uint_as_float(c[0]) + __uint_as_float(c[1]);
}
DEVI void rows_spread(float w, float (&v)[4]) {
    auto c = __builtin_amdgcn_permlane32_swap(__float_as_uint(w), __float_as_uint(w), false, false);
    auto a = __builtin_amdgcn_permlane16_swap(c[0], c[0], false, false);
    auto b = __builtin_amdgcn_permlane16_swap(c[1], c[1], false, false);
    v[0] = __uint_as_float(a[0]); v[1] = __uint_as_float(a[1]); v[2] = __uint_as_float(b[0]); v[3] = __uint_as_float(b[1]);
}

template <int GRP>
DEVI void g1_body(G1Ld& L, G2St& T, LAS char* lds, char* ws, int rt, int ct, int wm, int kh STAMP_ARGS) {
    G1Ptr P;
    {
        const int lane = fresh_lane(), g = lane >> 4, q = (lane & 15) >> 2, p = lane & 3;
        const unsigned ldsb = (unsigned)(size_t)lds;
        P.pA = ldsb + wm * 4096 + lane * 16;
        P.pH = ldsb + lane * 16;
        const int brow = (16 * (g >> 1) + 4 * (g & 1) + q) * 224 + p * 8;
        P.pB01 = ldsb + G1_BOFF + brow;
        P.pB23 = P.pB01 + 2 * G1_BST;
        P.pBh01 = P.pB01 + (2 + wm) * 32;
        P.pBh23 = P.pB23 + (2 + wm) * 32;
    }
    f32x4 acc[4][10], acch = {0.f, 0.f, 0.f, 0.f};
#pragma unroll
    for (int m = 0; m < 4; ++m)
#pragma unroll
        for (int n = 0; n < 10; ++n) acc[m][n] = (f32x4){0.f, 0.f, 0.f, 0.f};

    g1_pa<0>(L); if (GRP == 0) g1_px<0>(L);
    g1_pa<1>(L); if (GRP == 0) g1_px<1>(L);
#pragma unroll
    for (int m = 0; m < 4; ++m)
        asm volatile("" : "+v"(acc[m][0]), "+v"(acc[m][1]), "+v"(acc[m][2]), "+v"(acc[m][3]), "+v"(acc[m][4]), "+v"(acc[m][5]), "+v"(acc[m][6]), "+v"(acc[m][7]), "+v"(acc[m][8]), "+v"(acc[m][9]), "+v"(acch));
    asm volatile("s_cmp_eq_u32 %1, 0\n\ts_cbranch_scc1 .Lsk_%=\n\ts_waitcnt vmcnt(%0)\n.Lsk_%=:\n\ts_barrier" :: "i"(GRP == 0 ? 3 : 2), "s"(L.ma) : "memory", "scc", FRAG_REGS);
    g1_ldb<GRP, 0, 0, 0>(P); g1_ldb<GRP, 0, 0, 1>(P); g1_ldb<GRP, 0, 1, 0>(P); g1_ldb<GRP, 0, 1, 1>(P); g1_ldb<GRP, 0, 2, 0>(P); g1_ldb<GRP, 0, 2, 1>(P);
    g1_ldb<GRP, 0, 3, 0>(P); g1_ldb<GRP, 0, 3, 1>(P); g1_ldb<GRP, 0, 4, 0>(P); g1_ldb<GRP, 0, 4, 1>(P);
    if (GRP == 0) { g1_ldah<0>(P); g1_ldbh<0, 0>(P); g1_ldbh<0, 1>(P); }
    g1_lda<0, 0>(P); g1_lda<0, 1>(P);
    g1_ldb<GRP, 0, 5, 0>(P); g1_ldb<GRP, 0, 5, 1>(P); g1_ldb<GRP, 0, 6, 0>(P); g1_ldb<GRP, 0, 6, 1>(P); g1_ldb<GRP, 0, 7, 0>(P); g1_ldb<GRP, 0, 7, 1>(P);
    g1_lda<0, 2>(P); g1_lda<0, 3>(P);
    g1_issue<GRP, 2>(L);
    if (GRP == 1) g1_issue<1, 3>(L);
    STAMP_SEG(0);
#pragma unroll 1
    for (int it = 0; it < 8; ++it) {
        g1_stage<GRP, 0>(acc, acch, L, it, P);
        if (GRP == 0) g1_tail<0>(L, it);
        g1_stage<GRP, 1>(acc, acch, L, it, P);
        g1_stage<GRP, 2>(acc, acch, L, it, P);
        g1_stage<GRP, 3>(acc, acch, L, it, P);
    }
    asm volatile("s_waitcnt vmcnt(0) lgkmcnt(0)\n\ts_nop 15" ::: "memory", FRAG_REGS);
    SBAR();
    STAMP_SEG(0);

    const int lane = fresh_lane(), g = lane >> 4, i16 = lane & 15;
    char* ho = ws + WS_HO;
    const int mb0 = 16 * rt + 4 * wm;
    const unsigned hl = (unsigned)((g & 1) * 1024 + (g >> 1) * 256 + i16 * 16);
    G2W& W = T.W;
    {
        const int wid = GRP * 4 + wm, r = lane >> 3, c = (lane & 7) ^ ((r & 2) | ((wid & 1) << 2));
        W.vw0 = (unsigned)((8 * wid + r) * 2048 + c * 16 + kh * 128);
        W.vw1 = W.vw0 ^ 128;
        W.vw2 = W.vw0 + 448 * 2048;
    }
    char* hs = ho + (size_t)(64 * (ct >> 4) + (ct & 15)) * SLAB_B + (size_t)mb0 * 1024;
#define GW(i) do { if ((i) < 2 * G2_WL) { g2_w<(i) / 2, (i) % 2>(W); SBAR(); } } while (0)
#define GLU1(n, m) glu_store(acc[m][n], acc[m][5 + (n)], acc[(m) + 1][n], acc[(m) + 1][5 + (n)], hs + (size_t)(16 * (1 + (n) / 2)) * SLAB_B + ((n) & 1) * 512 + (m) * 1024 + hl)
    if (GRP == 1) {
        SBAR(); BAR(); MEMFENCE(); SBAR();
        GW(0); GW(1); GW(2); GW(3); GLU1(0, 0); GW(4); GW(5); GLU1(0, 2); GW(6); GW(7); GLU1(1, 0); GW(8); GW(9); GLU1(1, 2); GW(10); GW(11); GLU1(2, 0); GW(12); GW(13); GLU1(2, 2); GW(14); GW(15);
        asm volatile("s_waitcnt vmcnt(%0)" :: "i"(2 * G2_WL + 4) : "memory");
#pragma unroll
        for (int m = 0; m < 4; ++m) { T.dg[0][m] = acc[m][3]; T.dg[1][m] = acc[m][4]; T.du[0][m] = acc[m][8]; T.du[1][m] = acc[m][9]; }
        T.dd[0] = hs + (size_t)32 * SLAB_B + 512; T.dd[1] = hs + (size_t)48 * SLAB_B;
    } else {
        u32x4 Qf[4], Kf[4]; u32x2 Vh[4][2];
#pragma unroll
        for (int m = 0; m < 4; ++m) {
            Qf[m] = (u32x4){pk_bf16(acc[m][0][0], acc[m][0][1]), pk_bf16(acc[m][0][2], acc[m][0][3]), pk_bf16(acc[m][1][0], acc[m][1][1]), pk_bf16(acc[m][1][2], acc[m][1][3])};
            Kf[m] = (u32x4){pk_bf16(acc[m][2][0], acc[m][2][1]), pk_bf16(acc[m][2][2], acc[m][2][3]), pk_bf16(acc[m][3][0], acc[m][3][1]), pk_bf16(acc[m][3][2], acc[m][3][3])};
#pragma unroll
            for (int b = 0; b < 2; ++b) Vh[m][b] = (u32x2){pk_bf16(acc[m][4 + b][0], acc[m][4 + b][1]), pk_bf16(acc[m][4 + b][2], acc[m][4 + b][3])};
        }
        LAS char* xs = lds + G1_XOFF;
        if (wm < 3) {
            *(LAS u32x4*)(xs + (wm + 1) * 2048 + lane * 16) = Kf[3];
            *(LAS u32x4*)(xs + (wm + 1) * 2048 + 1024 + lane * 16) = (u32x4){Vh[3][0][0], Vh[3][0][1], Vh[3][1][0], Vh[3][1][1]};
        }
        {
            const unsigned h0 = pk_bf16(acch[0], acch[1]), h1 = pk_bf16(acch[2], acch[3]);
            if (wm < 2) {
                *(LAS u32x2*)(xs + lane * 16 + wm * 8) = (u32x2){h0, h1};
            } else {
                LAS char* t = xs + 1024 + ((i16 >> 2) * 16 + 4 * g) * 16 + (wm - 2) * 8 + (i16 & 3) * 2;
                *(LAS u16*)(t) = (u16)h0; *(LAS u16*)(t + 16) = (u16)(h0 >> 16); *(LAS u16*)(t + 32) = (u16)h1; *(LAS u16*)(t + 48) = (u16)(h1 >> 16);
            }
        }
        asm volatile("s_waitcnt lgkmcnt(0)" ::: "memory");
        SBAR(); BAR(); MEMFENCE(); SBAR();
        GW(0); GW(1);
        const u32x4 Kp = *(LAS const u32x4*)(xs + wm * 2048 + lane * 16);
        const u32x4 Vp = *(LAS const u32x4*)(xs + wm * 2048 + 1024 + lane * 16);
        const int dc = i16 - 4 * g - (i16 & 1);
        unsigned modd = (i16 & 1) ? ~0u : 0u, mc0 = dc >= 0 ? ~0u : 0u, mc1 = dc >= 2 ? ~0u : 0u;
        asm volatile("" : "+v"(modd), "+v"(mc0), "+v"(mc1));
        const unsigned half = (modd & 0xffff0000u) | (~modd & 0x0000ffffu);
        const unsigned pp0 = half & ~mc0, pp1 = half & ~mc1, pc0 = half & mc0, pc1 = half & mc1;
        const float sc = 0.17677669529663687f * 1.4426950408889634f;
        const f32x4 z = {0.f, 0.f, 0.f, 0.f};
        float e0[4], e1[4], red[4];
#pragma unroll
        for (int m = 0; m < 4; ++m) {
            u32x4 s0 = __builtin_bit_cast(u32x4, MFMA16(as_frag(m ? Kf[m ? m - 1 : 0] : Kp), as_frag(Qf[m]), z));
            const u32x4 s1 = __builtin_bit_cast(u32x4, MFMA16(as_frag(Kf[m]), as_frag(Qf[m]), z));
            if (m == 0 && rt == 0 && wm == 0) { const unsigned c = __float_as_uint(-1e9f / 0.17677669529663687f); s0 = (u32x4){c, c, c, c}; }
            e0[m] = __uint_as_float(bsel(modd, bsel(mc0, s1[1], s0[1]), bsel(mc0, s1[0], s0[0])));
            e1[m] = __uint_as_float(bsel(modd, bsel(mc1, s1[3], s0[3]), bsel(mc1, s1[2], s0[2])));
            red[m] = fmaxf(e0[m], e1[m]);
        }
        {
            rows_spread(rows_reduce<true>(red), red);
        }
#pragma unroll
        for (int m = 0; m < 4; ++m) {
            e0[m] = __builtin_amdgcn_exp2f((e0[m] - red[m]) * sc); e1[m] = __builtin_amdgcn_exp2f((e1[m] - red[m]) * sc);
            red[m] = e0[m] + e1[m];
        }
        {
            float w = __builtin_amdgcn_rcpf(rows_reduce<false>(red));
            rows_spread(w, red);
        }
        char* od = hs + (((g & 1) << 1) | (g >> 1)) * 256 + i16 * 16;
#pragma unroll
        for (int m = 0; m < 4; ++m) {
            const float p0 = e0[m] * red[m], p1 = e1[m] * red[m];
            const unsigned w0 = pk_bf16(p0, p0), w1 = pk_bf16(p1, p1);
            const u32x2 Pp = {w0 & pp0, w1 & pp1}, Pc = {w0 & pc0, w1 & pc1};
            const u32x2 Vp0 = m ? Vh[m ? m - 1 : 0][0] : (u32x2){Vp[0], Vp[1]}, Vp1 = m ? Vh[m ? m - 1 : 0][1] : (u32x2){Vp[2], Vp[3]};
            const f32x4 o0 = MFMA16K16(Vh[m][0], Pc, MFMA16K16(Vp0, Pp, z));
            const f32x4 o1 = MFMA16K16(Vh[m][1], Pc, MFMA16K16(Vp1, Pp, z));
            unsigned x0 = pk_bf16(o0[0], o0[1]), x1 = pk_bf16(o0[2], o0[3]), y0 = pk_bf16(o1[0], o1[1]), y1 = pk_bf16(o1[2], o1[3]);
            auto r0 = __builtin_amdgcn_permlane16_swap(x0, y0, false, false);
            auto r1 = __builtin_amdgcn_permlane16_swap(x1, y1, false, false);
            st16(od + m * 1024, never_ones((u32x4){r0[0], r1[0], r0[1], r1[1]}));
        }
        SBAR();
        GW(2); GW(3); GW(4); GW(5); GW(6); GW(7); GW(8); GW(9); GW(10); GW(11); GW(12); GW(13); GW(14); GW(15);
        asm volatile("s_waitcnt vmcnt(%0)" :: "i"(2 * G2_WL + 2) : "memory");
#pragma unroll
        for (int m = 0; m < 4; ++m) { T.dg[0][m] = acc[m][6]; T.dg[1][m] = acc[m][7]; T.du[0][m] = acc[m][8]; T.du[1][m] = acc[m][9]; }
        T.dd[0] = hs + (size_t)48 * SLAB_B + 512; T.dd[1] = ho + (size_t)(128 + (ct >> 1)) * SLAB_B + (ct & 1) * 512 + (size_t)mb0 * 1024;
        if (blockIdx.x == 0 && wm == 1) {
            flags_wait<POLL_SLEEP>((u32x2){~T.tag, 0u}, ws + WS_LF, (unsigned)(g * 8192 + i16 * 8), T.tag);
            flags_wait<POLL_SLEEP>((u32x2){~T.tag, 0u}, ws + WS_LF + 32768, (unsigned)(g * 8192 + i16 * 8), T.tag);
            flag_set<0>(ws + WS_EP, T.tag);
        }
    }
#undef GW
#undef GLU1
    STAMP_SEG(2);
}

DEVI float sum_x8(float v) {
    v += __uint_as_float(__builtin_amdgcn_update_dpp(0u, __float_as_uint(v), 0x128  , 0xf, 0xf, false));
    { auto t = __builtin_amdgcn_permlane16_swap(__float_as_uint(v), __float_as_uint(v), false, false); v = __uint_as_float(t[0]) + __uint_as_float(t[1]); }
    { auto t = __builtin_amdgcn_permlane32_swap(__float_as_uint(v), __float_as_uint(v), false, false); v = __uint_as_float(t[0]) + __uint_as_float(t[1]); }
    return v;
}

template <int KG>
DEVI void g2_body(G2St& T, LAS char* lds, char* ws, const u16* x, u16* out, int rt, int ct, int wm STAMP_ARGS2) {
    constexpr int kg = KG;
    const int bid = blockIdx.x, idx = bid >> 3, wid = KG * 4 + wm;
    const int kh = (bid >> 2) & 1, side = kh ^ 1, rt2 = 2 * rt + ((bid >> 1) & 1), ct2 = 4 * (bid & 1) + (idx & 3);
    const char* flq = ws + WS_FL + (rt * 4 + (bid & 2)) * 512;
    const int lane2 = fresh_lane(), g = lane2 >> 4, i16 = lane2 & 15, q = i16 >> 2, p = lane2 & 3;
    {
        const char* a0 = ws + WS_HO + (size_t)kg * SLAB_B + (size_t)(8 * rt2 + 2 * wm) * 1024;
        T.pa = a0 + (size_t)(64 * side) * SLAB_B;
        T.pad = a0 + (size_t)128 * SLAB_B;
        T.va0 = lane2 * 16 + kg * 1024; T.va1 = T.va0 ^ 1024;
        T.flagA = ws + WS_FL + (kg ^ 1) * 16384 + (rt * 4 + wm) * 512 + ct * 4;
        T.flagZ = ws + WS_FL + (rt * 4 + wm) * 512 + 256 + kg * 128 + ct * 4;
        T.hl = (unsigned)((g & 1) * 1024 + (g >> 1) * 256 + i16 * 16);
        T.poller = wid == 0;
        T.flags = T.poller ? flq : ws + WS_XB + (size_t)bid * 32768;
        T.foa = (unsigned)((lane2 >> 5) * 512 + side * 64 + (lane2 & 7) * 8);
        T.foz = (unsigned)((lane2 >> 5) * 512 + 256 + (lane2 & 31) * 8);
    }
#pragma unroll
    for (int n = 0; n < 4; ++n) {
        T.pB[0][n] = lds + kg * 4096 + (8 * g + q) * 128 + (((2 * n + (p >> 1)) ^ ((q & 2) | ((g & 1) << 2))) * 16) + (p & 1) * 8;
#pragma unroll
        for (int k = 1; k < (G2_NS + 3) / 4; ++k) { T.pB[k][n] = T.pB[0][n] + k * 4 * G2_ST; asm volatile("" : "+v"(T.pB[k][n])); }
        asm volatile("" : "+v"(T.pB[0][n]));
    }
    MEMFENCE(); SBAR(); BAR(); MEMFENCE(); SBAR();
    g2_a<0, 0>(T); g2_a<0, 1>(T); g2_a<1, 0>(T); g2_a<1, 1>(T);
    SBAR();
#pragma unroll
    for (int n = 0; n < 8; ++n) { T.B[n][0] = g2_ldb<0>(T, n, 0); T.B[n][1] = g2_ldb<0>(T, n, 1); }
    SBAR();
    STAMP_SEG(3);
    g2_run<KG, 0, 8>(T);
    STAMP_SEG(4);
    g2_run<KG, 8, 24>(T);
    const int orow = 128 * rt2 + 32 * wm + 16 * kg + i16, ocol = 128 * ct2 + 64 * kh + (g & 1) * 16 + (g >> 1) * 8;
    T.xw = lds + G2_XOFF + wid * 4096 + lane2 * 16;
    T.xr = lds + G2_XOFF + (wid ^ 4) * 4096 + lane2 * 16;
    T.snd = ws + WS_XB + (size_t)bid * 32768 + wid * 4096 + lane2 * 16;
    T.rcv = ws + WS_XB + (size_t)(bid ^ 4) * 32768 + wid * 4096 + lane2 * 16;
    T.xp = x + (size_t)orow * D_ + ocol;
    g2_run<KG, 24, 36>(T);
    STAMP_SEG(5);

    MFMA_DRAIN(); SBAR();
#pragma unroll
    for (int j = 0; j < 4; ++j) *(LAS f32x4*)(T.xw + j * 1024) = T.acc[1][j];
    WAIT_LGKM0();
    MEMFENCE(); SBAR(); BAR(); MEMFENCE(); SBAR();
#pragma unroll
    for (int j = 0; j < 4; ++j) T.acc[0][j] += *(LAS const f32x4*)(T.xr + j * 1024);
    asm volatile("s_waitcnt vmcnt(0)" : "+v"(T.xres[0]), "+v"(T.xres[1]), "+v"(T.rc[0]), "+v"(T.rc[1]), "+v"(T.rc[2]), "+v"(T.rc[3]) :: "memory");
    for (unsigned spin = 0;; ++spin) {
        const unsigned mx = umax4((u32x4){umax4(T.rc[0]), umax4(T.rc[1]), umax4(T.rc[2]), umax4(T.rc[3])});
        if (!__any(mx == 0xffffffffu) || spin > SPIN_CAP) break;
        __builtin_amdgcn_s_sleep(4);
        asm volatile("global_load_dwordx4 %0, %4, off sc1\n\tglobal_load_dwordx4 %1, %4, off offset:1024 sc1\n\tglobal_load_dwordx4 %2, %4, off offset:2048 sc1\n\tglobal_load_dwordx4 %3, %4, off offset:3072 sc1\n\ts_waitcnt vmcnt(0)"
                     : "=&v"(T.rc[0]), "=&v"(T.rc[1]), "=&v"(T.rc[2]), "=&v"(T.rc[3]) : "v"(T.rcv) : "memory");
    }
    u16* orp = out + (size_t)orow * D_ + ocol;
#pragma unroll
    for (int j = 0; j < 4; j += 2) {
        const f32x4 s0 = T.acc[0][j] + __builtin_bit_cast(f32x4, T.rc[j]), s1 = T.acc[0][j + 1] + __builtin_bit_cast(f32x4, T.rc[j + 1]);
        float v[8];
#pragma unroll
        for (int e = 0; e < 4; ++e) { auto r = __builtin_amdgcn_permlane16_swap(__float_as_uint(s0[e]), __float_as_uint(s1[e]), false, false); v[e] = __uint_as_float(r[0]); v[4 + e] = __uint_as_float(r[1]); }
        const u32x4 xv = T.xres[j >> 1];
        u32x4 o;
#pragma unroll
        for (int e = 0; e < 4; ++e) o[e] = pk_bf16(v[2 * e] + bf_lo(xv[e]), v[2 * e + 1] + bf_hi(xv[e]));
        *(GAS u32x4*)(size_t)(orp + 16 * j) = o;
    }
    STAMP_SEG(6);
    STAMP_OUT(ws, 0, wid, lane2);
    STAMP_OUT(ws, 1, wid, lane2);
}

__global__ __launch_bounds__(512, 2) void k_main(const char* __restrict__ wqkv, const char* __restrict__ wg, const char* __restrict__ wu, const char* __restrict__ wo, const char* __restrict__ wd, char* __restrict__ ws,
                                                const u16* __restrict__ x, u16* __restrict__ out) {
    extern __shared__ __attribute__((aligned(16))) char smem[];
    TL_ENTRY();
    asm volatile("" : "+s"(wqkv), "+s"(wg), "+s"(wu), "+s"(wo), "+s"(wd), "+s"(ws), "+s"(x), "+s"(out));
    LAS char* lds = (LAS char*)smem;
    const int tid = threadIdx.x, lane = tid & 63;
    const int wid = __builtin_amdgcn_readfirstlane(tid >> 6);
    const int bid = blockIdx.x, xcd = bid & 7, idx = bid >> 3;
    const int ct = 4 * xcd + (idx & 3), rt = idx >> 2;
    const unsigned ldsb = (unsigned)(size_t)lds;
    const int r8 = lane & 7, c8 = lane >> 3, row = 256 * rt + 8 * ct + r8;
    u32x4 xv[2];
    unsigned ep;
    asm volatile("global_load_dwordx4 %0, %3, %4\n\tglobal_load_dwordx4 %1, %3, %4 offset:128\n\ts_load_dword %2, %5, 0x0"
                 : "=&v"(xv[0]), "=&v"(xv[1]), "=&s"(ep) : "v"((unsigned)(row * 2048 + (16 * wid + c8) * 16)), "s"(x), "s"(ws + WS_EP) : "memory");
    G1Ld L;
    {
        const int c = wid * 64 + lane, rho = c / 14, cc = c - rho * 14;
        const int k = (rho & ~12) | ((rho & 4) << 1) | ((rho & 8) >> 1);
        const int c2 = cc < 12 ? cc : 11;
        L.pa = ws + WS_XN + (size_t)(16 * rt + 2 * wid) * 1024;
        L.stepa = (unsigned)SLAB_B;
        L.va = lane * 16;
        L.la = ldsb + (1 + 2 * wid) * 1024;
        L.px = ws + WS_XN + (size_t)(wid < 2 ? 16 * rt + 14 + wid : rt == 0 ? 0 : 16 * rt - 1) * 1024;
        L.stepx = (unsigned)SLAB_B;
        L.vx = (unsigned)((wid >= 2 && rt == 0 ? (lane & 48) : lane) * 16);
        L.lx = wid < 2 ? ldsb + (15 + wid) * 1024 : ldsb;
        L.p2 = wqkv + ct * 64;
        L.step2 = 32u * 3072u * 2u;
        L.v2 = (unsigned)(k * 6144 + (c2 >> 2) * 2048 + (c2 & 3) * 16);
        L.l2 = ldsb + G1_BOFF + wid * 1024;
        L.pg = wg + ct * 224;
        L.pu = wu + ct * 224;
        L.stepg = 32u * 3584u * 2u;
        L.vg = (unsigned)(k * 7168 + cc * 16);
        L.ma = s_if_lt<7, -1, 0>(wid); L.mb = L.ma; L.mx = s_if_lt<3, -1, 1>(wid); L.mt = s_if_lt<7, 0, (32 - T_S0) / T_J - T_BURST>(wid);
        const int j = lane < 20 * T_J ? lane / 20 : T_J - 1, i = lane < 20 * T_J ? lane - 20 * j : 19, trow = 32 * (T_S0 + j) + idx;
        L.ta = i < 6 ? wqkv + (size_t)trow * 6144 + (i >> 1) * 2048 + xcd * 256 + (i & 1) * 128 : (i < 13 ? wg + (i - 6) * 128 : wu + (i - 13) * 128) + (size_t)trow * 7168 + xcd * 896;
        L.ts = i < 6 ? 32u * T_J * 6144u : 32u * T_J * 7168u;
        L.m7 = s_if_lt<7, 0, 4>(wid);
    }
    L.tb = lane < 8 ? wo + bid * 8192 + lane * 128 : wd + bid * 28672 + (lane < 36 ? lane - 8 : 27) * 128;
    L.tt = lane < 8 ? 1024u : 3584u;
    L.tb -= 4 * L.tt;
    L.pf = ws + WS_XB + (size_t)bid * 32768 + (wid & 3) * 8192;
    const int kh = (bid >> 2) & 1, side = kh ^ 1, rt2 = 2 * rt + ((bid >> 1) & 1), ct2 = 4 * (bid & 1) + (idx & 3);
    const int kg = wid >> 2, wm = wid & 3;
    G2St T;
    T.W.pwo = wo + (size_t)(side * 512) * 2048 + ct2 * 256;
    T.W.pwh = wd + (size_t)(side * 1792 + kg * 80) * 2048 + ct2 * 256;
    T.W.pwd = wd + (size_t)(96 + (wid >> 1) * 96) * 2048 + ct2 * 256;
    T.W.ldsw = ldsb + wid * 1024;
    STAMP_DECL STAMP_START();
    const int m6 = s_if_lt<1, -1, 0>(wid ^ 6), m05 = s_if_lt<6, -1, 0>(wid);
#define W_FIRST() do { g1_p2<0>(L); g1_pg<0>(L); g1_pu<0>(L); \
        g1_piece<G1_BST, 0>(L.p2, L.v2, L.l2, m6); g1_piece<G1_BST + G1_REG, 0>(L.pg, L.vg, L.l2, m6); g1_piece<G1_BST + 2 * G1_REG, 0>(L.pu, L.vg, L.l2, m6); } while (0)
    W_FIRST();
    asm volatile("s_cmp_eq_u32 %3, 6\n\ts_cbranch_scc1 .Lw6_%=\n\ts_cmp_eq_u32 %3, 7\n\ts_cbranch_scc1 .Lw7_%=\n\ts_waitcnt vmcnt(3)\n\ts_branch .Lw_%=\n"
                 ".Lw6_%=:\n\ts_waitcnt vmcnt(6)\n\ts_branch .Lw_%=\n.Lw7_%=:\n\ts_waitcnt vmcnt(0)\n.Lw_%=:\n\ts_waitcnt lgkmcnt(0)"
                 : "+v"(xv[0]), "+v"(xv[1]), "+s"(ep) : "s"(wid) : "memory", "scc");
#pragma unroll
    for (int i = 0; i < (T_WHEN == 1 ? T_BURST : 0); ++i) g1_touch(L.ta, L.ts, 0, L.m7);
    T.tag = __builtin_amdgcn_readfirstlane(ep * 1664525u + 1013904223u);
    OPAQUE_S(T.tag);
    P0S(2);
    {
        LAS float* red = (LAS float*)(lds + G1_XOFF);
        f32x2 f[8];
#pragma unroll
        for (int j = 0; j < 8; ++j) f[j] = (f32x2){bf_lo(xv[j >> 2][j & 3]), bf_hi(xv[j >> 2][j & 3])};
        {
            const u32x4 ones = {0xffffffffu, 0xffffffffu, 0xffffffffu, 0xffffffffu};
            asm volatile("global_store_dwordx4 %0, %1, %2 sc1\n\tglobal_store_dwordx4 %0, %1, %2 offset:1024 sc1\n\ts_nop 1"
                         :: "v"(L.va), "v"(ones), "s"(ws + WS_HO + (size_t)(64 * (ct >> 4) + (ct & 15)) * SLAB_B + (size_t)(16 * rt + 2 * wid) * 1024) : "memory");
        }
        const f32x2 s2 = ((f[0] + f[1]) + (f[2] + f[3])) + ((f[4] + f[5]) + (f[6] + f[7]));
        const float mw = sum_x8(s2[0] + s2[1]) * (1.0f / 128);
        f32x2 q2 = {0.f, 0.f};
#pragma unroll
        for (int j = 0; j < 8; ++j) { const f32x2 d = f[j] - mw; q2 = __builtin_elementwise_fma(d, d, q2); }
        const float qw = sum_x8(q2[0] + q2[1]);
        if (c8 == 0) *(LAS f32x2*)(red + r8 * 16 + wid * 2) = (f32x2){mw, qw};
        WAIT_LGKM0(); MEMFENCE(); BAR(); MEMFENCE();
#pragma unroll
        for (int i = 0; i < (T_WHEN == 3 ? T_BURST : 0); ++i) g1_touch(L.ta, L.ts, 0, L.m7);
        f32x4 st[4];
#pragma unroll
        for (int k = 0; k < 4; ++k) st[k] = *(LAS const f32x4*)(red + r8 * 16 + k * 4);
        const float mean = (((st[0][0] + st[0][2]) + (st[1][0] + st[1][2])) + ((st[2][0] + st[2][2]) + (st[3][0] + st[3][2]))) * 0.125f;
        float qt = 0.f, dt = 0.f;
#pragma unroll
        for (int k = 0; k < 4; ++k) { const float d0 = st[k][0] - mean, d1 = st[k][2] - mean; qt += st[k][1] + st[k][3]; dt = fmaf(d0, d0, fmaf(d1, d1, dt)); }
        const float mb = rbf(mean);
        const float vb = rbf(fmaf(dt, 128.f, qt) * (1.0f / D_));
        const float den = rbf(__builtin_amdgcn_sqrtf(rbf(vb + 1e-5f)));
        const float r0 = __builtin_amdgcn_rcpf(den), rc = fmaf(fmaf(-den, r0, 1.0f), r0, r0);
#pragma unroll
        for (int i = 0; i < 2; ++i) {
            u32x4 o;
#pragma unroll
            for (int j = 0; j < 4; ++j) {
                const f32x2 c = f[i * 4 + j] - mb;
                const unsigned d = pk_bf16(c[0], c[1]);
                const f32x2 y = (f32x2){bf_lo(d), bf_hi(d)} * rc;
                o[j] = pk_bf16(y[0], y[1]);
            }
            const int ch = 16 * wid + 8 * i + c8;
            asm volatile("global_store_dwordx4 %0, %1, %2 sc1\n\ts_nop 1" :: "v"((unsigned)((ch >> 2) * (int)SLAB_B + (row >> 4) * 1024 + ((ch & 3) * 16 + (row & 15)) * 16)), "v"(o), "s"(ws + WS_XN) : "memory");
        }
        P0S(3);
        asm volatile("s_waitcnt vmcnt(0)\n\ts_barrier" ::: "memory");
        P0S(4);
        { const int keep = L.mb; L.mb = m05; g1_p2<1>(L); g1_pg<1>(L); g1_pu<1>(L); L.mb = keep; }
#pragma unroll
        for (int i = 0; i < (T_WHEN == 2 ? T_BURST : 0); ++i) g1_touch(L.ta, L.ts, 0, L.m7);
        if (wid == 6) {
            const bool next = ct >= 30 && rt < 7;
            asm volatile("s_mov_b64 exec, %3\n\tglobal_store_dword %0, %1, %2 sc1\n\ts_mov_b64 exec, -1"
                         :: "v"((unsigned)((rt * 32 + lane) * 256 + (lane < 32 ? ct : ct + 2) * 4)), "v"(T.tag), "s"(ws + WS_LF), "s"(next ? ~0ull : 0xffffffffull) : "memory");
            flags_wait<LN_SLEEP>((u32x2){~T.tag, 0u}, ws + WS_LF + (rt * 32 + ct) * 256, (unsigned)(lane < 16 || rt == 0 ? (lane & 15) * 8 : 128), T.tag);
        }
        MEMFENCE(); BAR(); MEMFENCE();
    }
    STAMP_SEG(1);
    if (wid < 4) {
        g1_body<0>(L, T, lds, ws, rt, ct, wm, kh STAMP_PASS); g2_body<0>(T, lds, ws, x, out, rt, ct, wm STAMP_PASS2);
        TL_EXIT(ws, 0); TL_EXIT(ws, 1); TL_EXIT(ws, 2);
        __builtin_amdgcn_endpgm();
    }
    g1_body<1>(L, T, lds, ws, rt, ct, wm, kh STAMP_PASS); g2_body<1>(T, lds, ws, x, out, rt, ct, wm STAMP_PASS2);
    TL_EXIT(ws, 0); TL_EXIT(ws, 1); TL_EXIT(ws, 2);
    __builtin_amdgcn_endpgm();
}

extern "C" void kernel_launch(void* const* d_in, const int* in_sizes, int n_in,
                              void* d_out, int out_size, void* d_ws, size_t ws_size,
                              hipStream_t stream) {
    const u16* x = (const u16*)d_in[0];
    const char* wqkv = (const char*)d_in[1];
    const char* wo = (const char*)d_in[2];
    const char* wg = (const char*)d_in[4];
    const char* wu = (const char*)d_in[5];
    const char* wd = (const char*)d_in[6];
    char* ws = (char*)d_ws;
    (void)hipFuncSetAttribute((const void*)k_main, hipFuncAttributeMaxDynamicSharedMemorySize, G1_LDS);
    k_main<<<256, 512, G1_LDS, stream>>>(wqkv, wg, wu, wo, wd, ws, x, (u16*)d_out);
}
```

```cpp
#include <hip/hip_runtime.h>
#include <hip/hip_bf16.h>
#include <stdint.h>

typedef __bf16 bf16x8 __attribute__((ext_vector_type(8)));
typedef __bf16 bf16x4 __attribute__((ext_vector_type(4)));
typedef __bf16 bf16x2 __attribute__((ext_vector_type(2)));
typedef float f32x4 __attribute__((ext_vector_type(4)));
typedef float f32x2 __attribute__((ext_vector_type(2)));
typedef unsigned u32x4 __attribute__((ext_vector_type(4)));
typedef unsigned u32x2 __attribute__((ext_vector_type(2)));
typedef unsigned short u16;
#define LAS __attribute__((address_space(3)))
#define GAS __attribute__((address_space(1)))
#define DEVI __device__ __forceinline__

constexpr int S_ = 2048, D_ = 1024, F_ = 3584;
constexpr int MB_ = S_ / 16;
constexpr size_t SLAB_B = (size_t)MB_ * 1024;
constexpr size_t WS_XN = 0;
constexpr size_t WS_HO = 32 * SLAB_B;
constexpr size_t WS_XB = WS_HO + 144 * SLAB_B;
constexpr size_t WS_FL = WS_XB + 256 * 32768;
constexpr size_t WS_LF = WS_FL + 32768;
constexpr size_t WS_EP = WS_LF + 65536;

DEVI unsigned pk_bf16(float lo, float hi) {
    bf16x2 v = {(__bf16)lo, (__bf16)hi};
    return __builtin_bit_cast(unsigned, v);
}
DEVI float bf_lo(unsigned u) { return __uint_as_float(u << 16); }
DEVI float bf_hi(unsigned u) { return __uint_as_float(u & 0xffff0000u); }
DEVI float rbf(float f) { return (float)(__bf16)f; }
DEVI bf16x8 as_frag(u32x4 v) { return __builtin_bit_cast(bf16x8, v); }
#define MFMA16(a, b, c) __builtin_amdgcn_mfma_f32_16x16x32_bf16((a), (b), (c), 0, 0, 0)
#define SBAR() __builtin_amdgcn_sched_barrier(0)
#define MFMA_ACC(c, a, b) asm volatile("v_mfma_f32_16x16x32_bf16 %0, %1, %2, %0" : "+v"(c) : "v"(a), "v"(b))
#define MFMA_NEW(c, a, b) asm volatile("v_mfma_f32_16x16x32_bf16 %0, %1, %2, 0" : "=&v"(c) : "v"(a), "v"(b))
#define MFMA_DRAIN() asm volatile("s_nop 15" ::: "memory")
#define BAR() __builtin_amdgcn_s_barrier()

#define TL_ENTRY()
#define TL_EXIT(ws, kern)
#define STAMP_DECL
#define STAMP_ARGS
#define STAMP_PASS
#define STAMP_ARGS2
#define STAMP_PASS2
#define STAMP_SEG(i)
#define STAMP_START()
#define STAMP_SEGI(i)
#define STAMP_OUT(ws, kern, wid, lane)

#define P0S(i)
DEVI int fresh_lane() { int l; asm volatile("v_mbcnt_lo_u32_b32 %0, -1, 0\n\tv_mbcnt_hi_u32_b32 %0, -1, %0" : "=v"(l)); return l; }

DEVI bf16x8 tr_frag(LAS const char* p, int off0, int off1) {
    bf16x4 lo = __builtin_amdgcn_ds_read_tr16_b64_v4bf16((LAS bf16x4*)(p + off0));
    bf16x4 hi = __builtin_amdgcn_ds_read_tr16_b64_v4bf16((LAS bf16x4*)(p + off1));
    return __builtin_shufflevector(lo, hi, 0, 1, 2, 3, 4, 5, 6, 7);
}

DEVI void glds16(const char* sbase, unsigned voff, unsigned ldsdst) {
    unsigned keep;
    asm volatile("s_mov_b32 %0, m0\n\ts_mov_b32 m0, %3\n\ts_nop 0\n\tglobal_load_lds_dwordx4 %1, %2\n\ts_mov_b32 m0, %0"
                 : "=&s"(keep) : "v"(voff), "s"(sbase), "s"(ldsdst) : "memory");
}

constexpr int G1_AST = 17 * 1024;
constexpr int G1_BST = 21 * 1024;
constexpr int G1_REG = 7168;
constexpr int G1_BOFF = 4 * G1_AST;
constexpr int G1_XOFF = G1_BOFF + 4 * G1_BST;
constexpr int G1_LDS = G1_XOFF + 8192;

#define FLAG_WAVE 7
#define W_WHEN 0
#define T_CONTIG 0
#define CODE_AGAIN 0
#define T_WHEN 1
#define T_J 3
#define T_EVERY 1
#define T_S0 2
static_assert((32 - T_S0) % T_J == 0, "");
#define T_BURST 2
typedef unsigned long long u64;
#define FRAG_REGS "v191", "v192", "v193", "v194", "v195", "v196", "v197", "v198", "v199", "v200", "v201", "v202", "v203", "v204", "v205", "v206", "v207", "v208", "v209", "v210", "v211", "v212", "v213", "v214", "v215", "v216", "v217", "v218", "v219", "v220", "v221", "v222", "v223", "v224", "v225", "v226", "v227", "v228", "v229", "v230", "v231", "v232", "v233", "v234", "v235", "v236", "v237", "v238", "v239", "v240", "v241", "v242", "v243", "v244", "v245", "v246", "v247", "v248", "v249", "v250", "v251", "v252", "v253", "v254", "v255"
constexpr int FA = 192, FB = 208, FAH = 248, FBH = 252;
struct G1Ld {
    const char *pa, *p2, *pg, *pu, *px;
    unsigned stepa, step2, stepg, stepx;
    unsigned va, v2, vg, vx;
    unsigned la, l2, lx;
    char* pf;
    int ma, mb, mx;
    const char *ta, *tb; unsigned ts, tt; int mt, m7; const char* pc; unsigned vc;
};

#define WAIT_LGKM0() __builtin_amdgcn_s_waitcnt(0xC07F)
#define MEMFENCE() asm volatile("" ::: "memory")
#define OPAQUE_S(x) asm volatile("" : "+s"(x))

template <int B, int YES, int NO> DEVI int s_if_lt(int a) { int r; asm("s_cmp_lt_u32 %1, %2\n\ts_cselect_b32 %0, %3, %4" : "=s"(r) : "s"(a), "i"(B), "i"(YES), "i"(NO) : "scc"); return r; }
template <int LOFF, int KIND>
DEVI void g1_piece(const char* base, unsigned voff, unsigned lds, int msk) {
    asm volatile("s_cmp_eq_u32 %3, 0\n\ts_cbranch_scc1 .Lsk_%=\n\ts_add_u32 m0, %2, %4\n\ts_mov_b32 exec_lo, %3\n\ts_ashr_i32 exec_hi, %3, 31\n\t"
                 ".if %5 == 0\n\tglobal_load_lds_dwordx4 %0, %1\n\t.else\n\tglobal_load_lds_dwordx4 %0, %1 sc1\n\t.endif\n\t"
                 ".if %5 == 2\n\tglobal_load_lds_dwordx4 %0, %1 offset:1024 sc1\n\t.endif\n\t"
                 "s_mov_b64 exec, -1\n.Lsk_%=:"
                 :: "v"(voff), "s"(base), "s"(lds), "s"(msk), "i"(LOFF), "i"(KIND) : "memory", "scc");
}
template <int SLOT> DEVI void g1_pa(G1Ld& L) { g1_piece<SLOT * G1_AST, 2>(L.pa, L.va, L.la, L.ma); L.pa += L.stepa; OPAQUE_S(L.pa); }
template <int SLOT> DEVI void g1_px(G1Ld& L) { g1_piece<SLOT * G1_AST, 1>(L.px, L.vx, L.lx, L.mx); L.px += L.stepx; OPAQUE_S(L.px); }
template <int SLOT> DEVI void g1_p2(G1Ld& L) { g1_piece<SLOT * G1_BST, 0>(L.p2, L.v2, L.l2, L.mb); L.p2 += L.step2; OPAQUE_S(L.p2); }
template <int SLOT> DEVI void g1_pg(G1Ld& L) { g1_piece<SLOT * G1_BST + G1_REG, 0>(L.pg, L.vg, L.l2, L.mb); L.pg += L.stepg; OPAQUE_S(L.pg); }
template <int SLOT> DEVI void g1_pu(G1Ld& L) { g1_piece<SLOT * G1_BST + 2 * G1_REG, 0>(L.pu, L.vg, L.l2, L.mb); L.pu += L.stepg; OPAQUE_S(L.pu); }
template <int GRP, int SLOT> DEVI void g1_issue(G1Ld& L) { g1_pa<SLOT>(L); if (GRP == 0) g1_px<SLOT>(L); g1_p2<SLOT>(L); g1_pg<SLOT>(L); g1_pu<SLOT>(L); }
DEVI void g1_touch(const char*& p, unsigned step, int i, int n) {
    asm volatile("s_cmp_ge_u32 %1, %2\n\ts_cbranch_scc1 .Lsk_%=\n\tglobal_load_dword v191, %0, off\n.Lsk_%=:" :: "v"(p), "s"(i), "s"(n) : "memory", "scc", "v191");
    p += step; asm volatile("" : "+v"(p));
}
DEVI void g1_code(const G1Ld& L, int a, int b) {
    asm volatile("s_cmp_lg_u32 %2, %3\n\ts_cbranch_scc1 .Lsk_%=\n\tglobal_load_dword v191, %0, %1\n.Lsk_%=:" :: "v"(L.vc), "s"(L.pc), "s"(a), "s"(b) : "memory", "scc", "v191");
}
template <int GRP> DEVI void g1_tail(G1Ld& L, int it) {
    OPAQUE_S(it);
    const bool tail = it == 7;
    L.p2 = tail ? L.p2 - L.step2 : L.p2; L.pg = tail ? L.pg - L.stepg : L.pg; L.pu = tail ? L.pu - L.stepg : L.pu;
    L.step2 = tail ? 0u : L.step2; L.stepg = tail ? 0u : L.stepg;
    L.mb = tail ? L.mb & 1 : L.mb;
    if (GRP == 0) { L.px = tail ? L.px - L.stepx : L.px; L.stepx = tail ? 0u : L.stepx; L.mx = tail ? 1 : L.mx; }
    L.pa = tail ? L.pa - L.stepa : L.pa; L.ma = tail ? L.ma & 1 : L.ma;
    L.stepa = tail ? 0u : L.stepa;
}

struct G1Ptr { unsigned pA, pH, pB01, pB23, pBh01, pBh23; };

template <int REG, int OFF> DEVI void ds_tr(unsigned base) { asm volatile("ds_read_b64_tr_b16 v[%c1:%c2], %0 offset:%c3" :: "v"(base), "i"(REG), "i"(REG + 1), "i"(OFF) : "memory"); }
template <int REG, int OFF> DEVI void ds_128(unsigned base) { asm volatile("ds_read_b128 v[%c1:%c2], %0 offset:%c3" :: "v"(base), "i"(REG), "i"(REG + 3), "i"(OFF) : "memory"); }
template <int RA_, int RB_> DEVI void mfma_fr(f32x4& c) { asm volatile("v_mfma_f32_16x16x32_bf16 %0, v[%c1:%c2], v[%c3:%c4], %0" : "+v"(c) : "i"(RA_), "i"(RA_ + 3), "i"(RB_), "i"(RB_ + 3)); }
template <int GRP, int SL, int n, int h> DEVI void g1_ldb(const G1Ptr& P) {
    constexpr int off = (SL & 1) * G1_BST + (GRP == 0 ? (n < 6 ? n * 32 : (n < 8 ? G1_REG + (n - 1) * 32 : 2 * G1_REG + (n - 3) * 32))
                                                      : (n < 5 ? G1_REG + n * 32 : 2 * G1_REG + (n - 5) * 32));
    ds_tr<FB + 4 * n + 2 * h, off + h * 8 * 224>((SL < 2) ? P.pB01 : P.pB23);
}
template <int SL, int m> DEVI void g1_lda(const G1Ptr& P) { ds_128<FA + 4 * m, SL * G1_AST + (1 + m) * 1024>(P.pA); }
template <int SL> DEVI void g1_ldah(const G1Ptr& P) { ds_128<FAH, SL * G1_AST>(P.pH); }
template <int SL, int h> DEVI void g1_ldbh(const G1Ptr& P) { ds_tr<FBH + 2 * h, (SL & 1) * G1_BST + h * 8 * 224>((SL < 2) ? P.pBh01 : P.pBh23); }

template <int GRP, int ST>
DEVI void g1_stage(f32x4 (&acc)[4][10], f32x4& acch, G1Ld& L, int it, const G1Ptr& P) {
    constexpr int NX = (ST + 1) & 3, IS = (ST + 3) & 3;
#define MM(m, n) do { if (GRP == 0 && ((n) == 4 || (n) == 5)) mfma_fr<FA + 4 * (m), FB + 4 * (n)>(acc[m][n]); else mfma_fr<FB + 4 * (n), FA + 4 * (m)>(acc[m][n]); } while (0)
#define RB(n, h) g1_ldb<GRP, NX, n, h>(P)
#define RC(n, h) g1_ldb<GRP, ST, n, h>(P)
#define RA(m) g1_lda<NX, m>(P)
#define G0(x) do { if (GRP == 0) { x; } } while (0)
    asm volatile("s_waitcnt lgkmcnt(9)" ::: "memory");
    MM(0, 0); RC(8, 0);
    asm volatile("s_waitcnt lgkmcnt(9)" ::: "memory");
    MM(1, 0); RC(8, 1);
    MM(1, 1); RC(9, 0); MM(0, 1); RC(9, 1);
    MM(0, 2); G0(g1_pa<IS>(L)); MM(1, 2); G0(g1_px<IS>(L));
    MM(1, 3); G0(g1_p2<IS>(L)); MM(0, 3); G0(g1_pg<IS>(L));
    MM(0, 4); G0(g1_pu<IS>(L)); MM(1, 4);
    G0((mfma_fr<FBH, FAH>(acch)));
    asm volatile("s_waitcnt vmcnt(%0) lgkmcnt(0)\n\ts_barrier" :: "i"(GRP == 0 ? 12 : 10) : "memory", FRAG_REGS);
    if (GRP == 1) {
        if (ST == 0) g1_tail<1>(L, it);
        g1_issue<1, ST>(L);
        if (ST % T_EVERY == 0) g1_touch(L.ta, L.ts, it * (4 / T_EVERY) + ST / T_EVERY, L.mt);
        if (ST & 1) g1_touch(L.tb, L.tt, it - 2, L.m7);
        if (CODE_AGAIN && ST == 2) g1_code(L, it * L.m7, CODE_AGAIN * 4);
        if (ST == 2) {
            const u32x4 ones = {0xffffffffu, 0xffffffffu, 0xffffffffu, 0xffffffffu};
            asm volatile("global_store_dwordx4 %0, %1, %2 sc1\n\ts_nop 1" :: "v"(L.va), "v"(ones), "s"(L.pf) : "memory", FRAG_REGS);
            L.pf += 1024; OPAQUE_S(L.pf);
        }
    }
    MM(2, 0); MM(3, 0); RB(0, 0);
    MM(3, 1); RB(0, 1); MM(2, 1); RB(1, 0);
    MM(2, 2); RB(1, 1); MM(3, 2); RB(2, 0);
    MM(3, 3); RB(2, 1); MM(2, 3); RB(3, 0);
    MM(2, 4); RB(3, 1); MM(3, 4); RB(4, 0);
    MM(0, 5); RB(4, 1); MM(1, 5); G0(g1_ldah<NX>(P));
    MM(1, 6); G0((g1_ldbh<NX, 0>(P))); MM(0, 6); G0((g1_ldbh<NX, 1>(P)));
    MM(0, 7); MM(1, 7);
    MM(1, 8); MM(0, 8);
    MM(0, 9); RA(0); MM(1, 9); RA(1);
    MM(2, 5); MM(3, 5); RB(5, 0);
    MM(3, 6); RB(5, 1); MM(2, 6); RB(6, 0);
    MM(2, 7); RB(6, 1); MM(3, 7); RB(7, 0);
    MM(3, 8); RB(7, 1); MM(2, 8);
    MM(2, 9); RA(2); MM(3, 9); RA(3);
#undef MM
#undef RB
#undef RC
#undef RA
#undef G0
}

DEVI void st16(char* dst, u32x4 v) {
    asm volatile("global_store_dwordx4 %0, %1, off sc1\n\ts_nop 1" :: "v"(dst), "v"(v) : "memory");
}

DEVI unsigned glu2(float g0, float g1, float u0, float u1) {
    const f32x2 t = (f32x2){g0, g1} * -1.4426950408889634f;
    const f32x2 d = (f32x2){__builtin_amdgcn_exp2f(t[0]), __builtin_amdgcn_exp2f(t[1])} + 1.0f;
    const f32x2 h = (f32x2){u0, u1} * (f32x2){__builtin_amdgcn_rcpf(d[0]), __builtin_amdgcn_rcpf(d[1])};
    return pk_bf16(h[0], h[1]);
}

DEVI void glu_store(const f32x4& g0, const f32x4& u0, const f32x4& g1, const f32x4& u1, char* dst) {
    unsigned a0 = glu2(g0[0], g0[1], u0[0], u0[1]), a1 = glu2(g0[2], g0[3], u0[2], u0[3]);
    unsigned b0 = glu2(g1[0], g1[1], u1[0], u1[1]), b1 = glu2(g1[2], g1[3], u1[2], u1[3]);
    auto r0 = __builtin_amdgcn_permlane16_swap(a0, b0, false, false);
    auto r1 = __builtin_amdgcn_permlane16_swap(a1, b1, false, false);
    st16(dst, (u32x4){r0[0], r1[0], r0[1], r1[1]});
    SBAR();
}

#define LN_SLEEP 2
#define POLL_SLEEP 8
#define G2_RCV 34
#define G2_NS 8
constexpr int G2_ST = 16384;
#define SPREAD 1
#define OUT_MODE 0
#define G2_BE 2
constexpr int G2_WL = G2_NS - G2_BE;
constexpr int G2_XOFF = G2_NS <= 8 ? G2_NS * G2_ST : 0;
constexpr int G2_F = 32, G2_XST = 33;
constexpr int G2_FY = 8, G2_FZ = 12;
constexpr int G2_PA = 1, G2_PY = 9, G2_PZ = 15;
constexpr unsigned SPIN_CAP = 1u << 17;
static_assert(G2_WL <= 8 && G2_RCV > G2_F && G2_RCV <= 35 && G2_NS >= 6 && G2_NS <= 9, "");
constexpr int g2_grp(int t) { return t < G2_F ? t : G2_F + 2 * (t - G2_F); }
constexpr int g2_afirst(int s) { return s < 2 ? 2 + 2 * s : s + 4 <= 35 ? g2_grp(s + 4) : 40; }
constexpr int g2_alast(int s) { return g2_afirst(s + 1); }
constexpr int g2_tops(int s) { return s == 0 || s == G2_FY || s == G2_FZ ? 1 : 0; }
constexpr int g2_wops(int s) { return s + G2_WL <= 35 ? 2 : 0; }
constexpr int g2_xops(int s) { return (s < 8 && (s & 1) ? 1 : 0) + (s == G2_PA || s == G2_PY || s == G2_PZ ? 1 : 0) + (s == G2_F ? 4 : 0) + (s == G2_XST ? 2 : 0) + (s == G2_RCV ? 4 : 0); }
constexpr int g2_wait(int t) {
    const int need = g2_grp(t + 1) - 1;
    int n = 4, last = need < 2 ? 2 * need + 2 : 0;
    for (int s = 0; s < t; ++s) {
        n += g2_tops(s) + g2_wops(s);
        if (t % G2_BE == 0 && s + G2_WL <= t + G2_BE && g2_wops(s) && n > last) last = n;
        for (int g = g2_afirst(s); g < g2_alast(s); ++g) { n += 2; if (g == need && n > last) last = n; }
        n += g2_xops(s);
        if ((t == 4 && s == G2_PA) || (t == 12 && s == G2_PY) || (t == 20 && s == G2_PZ) || (t == G2_FY && s == 3) || (t == G2_FZ && s == 7)) last = n > last ? n : last;
    }
    return n - last;
}

struct G2W {
    const char *pwo, *pwh, *pwd;
    unsigned vw0, vw1, vw2, ldsw;
};
struct G2St {
    G2W W;
    const char *pa, *pad;
    unsigned va0, va1;
    LAS const char* pB[(G2_NS + 3) / 4][4];
    LAS char* xw; LAS const char* xr;
    char* snd; const char* rcv; const u16* xp;
    f32x4 dg[2][4], du[2][4]; char* dd[2]; unsigned hl, hq[4];
    char *flagA, *flagZ; const char* flags; unsigned foa, foz, tag; u32x2 hf; bool poller;
    u32x4 A[40][2];
    bf16x4 B[8][2];
    f32x4 acc[2][8];
    u32x4 xres[2], rc[4];
};
template <int LOFF> DEVI void g2_glds(const char* base, unsigned voff, unsigned lds) {
    asm volatile("s_add_u32 m0, %2, %3\n\ts_nop 0\n\tglobal_load_lds_dwordx4 %0, %1" :: "v"(voff), "s"(base), "s"(lds), "i"(LOFF) : "memory", "scc");
}
template <int ST, int I> DEVI void g2_w(const G2W& W) {
    constexpr int SLOT = ST % G2_NS;
    const char* src = ST < 8 ? W.pwo + ST * 64 * 2048 : ST < G2_F ? W.pwh + (((ST - 8) & 7) * 224 + ((ST - 8) >> 3) * 32) * 2048 : W.pwd + (ST - G2_F) * 896 * 2048;
    g2_glds<SLOT * G2_ST + I * 8192>(src, I == 0 ? W.vw0 : ST < G2_F ? W.vw1 : W.vw2, W.ldsw);
}
template <int G> DEVI const char* g2_asrc(const G2St& T) {
    return G < G2_F ? T.pa + (size_t)G * 2 * SLAB_B : T.pad + (size_t)(G - G2_F) * 2 * SLAB_B; }
template <int G, int I> DEVI void g2_a(G2St& T) {
    asm volatile("global_load_dwordx4 %0, %1, %2 sc1" : "=v"(T.A[G][I]) : "v"(I ? T.va1 : T.va0), "s"(g2_asrc<G>(T)) : "memory");
}
template <int ST> DEVI bf16x4 g2_ldb(G2St& T, int n, int h) {
    constexpr int SLOT = ST % G2_NS;
    return __builtin_amdgcn_ds_read_tr16_b64_v4bf16((LAS bf16x4*)(T.pB[SLOT / 4][n & 3] + (SLOT % 4) * G2_ST + (n >> 2) * 8192 + h * 512));
}
DEVI bf16x8 cat(bf16x4 lo, bf16x4 hi) { return __builtin_shufflevector(lo, hi, 0, 1, 2, 3, 4, 5, 6, 7); }
template <int OFF> DEVI void flag_set(char* p, unsigned tag) { const unsigned zero = 0u; asm volatile("s_mov_b64 exec, 1\n\tglobal_store_dword %0, %1, %2 offset:%3 sc1\n\ts_mov_b64 exec, -1" :: "v"(zero), "v"(tag), "s"(p), "i"(OFF) : "memory"); }
template <int SLEEP> DEVI void flags_wait(u32x2 f, const char* base, unsigned voff, unsigned tag) {
#pragma unroll 1
    for (unsigned spin = 0; __any(f[0] != tag || f[1] != tag) && spin < SPIN_CAP; ++spin) {
        if (spin) __builtin_amdgcn_s_sleep(SLEEP);
        asm volatile("global_load_dwordx2 %0, %1, %2 sc1\n\ts_waitcnt vmcnt(0)" : "=&v"(f) : "v"(voff), "s"(base) : "memory");
    }
}
DEVI unsigned umax4(u32x4 v) { const unsigned a = v[0] > v[1] ? v[0] : v[1], b = v[2] > v[3] ? v[2] : v[3]; return a > b ? a : b; }
DEVI u32x4 never_ones(u32x4 w) {
#pragma unroll
    for (int e = 0; e < 4; ++e) w[e] = w[e] < 0xfffffffeu ? w[e] : 0xfffffffeu;
    return w;
}

template <int KG, int t>
DEVI void g2_stage(G2St& T) {
    constexpr bool TB = t >= G2_F, ISW = g2_wops(t) != 0, RL = t < 35, CHK = t == 4 || t == 12 || t == 20;
    constexpr int G0 = g2_grp(t), G1 = TB ? G0 + 1 : G0, NA = g2_alast(t) - g2_afirst(t), GN = NA ? g2_afirst(t) : 0, GN1 = NA == 2 ? GN + 1 : GN;
    u32x4 (&Aa)[2] = T.A[G0]; u32x4 (&Ab)[2] = T.A[G1];
    if (TB) asm volatile("s_waitcnt vmcnt(%4)" : "+v"(Aa[0]), "+v"(Aa[1]), "+v"(Ab[0]), "+v"(Ab[1]) : "i"(g2_wait(t)) : "memory");
    else if (CHK) asm volatile("s_waitcnt vmcnt(%3)" : "+v"(Aa[0]), "+v"(Aa[1]), "+v"(T.hf) : "i"(g2_wait(t)) : "memory");
    else asm volatile("s_waitcnt vmcnt(%2)" : "+v"(Aa[0]), "+v"(Aa[1]) : "i"(g2_wait(t)) : "memory");
    if (t == 0) flag_set<0>(T.flagA, T.tag);
    if (t == G2_FY) flag_set<128>(T.flagA, T.tag);
    if (t == G2_FZ) flag_set<0>(T.flagZ, T.tag);
    if (t < 8) {
#pragma unroll 1
        for (unsigned spin = 0; spin < SPIN_CAP; ++spin) {
            const unsigned a = umax4(Aa[0]), b = umax4(Aa[1]);
            if (!__any((a > b ? a : b) == 0xffffffffu)) break;
            __builtin_amdgcn_s_sleep(POLL_SLEEP);
            asm volatile("global_load_dwordx4 %0, %2, %4 sc1\n\tglobal_load_dwordx4 %1, %3, %4 sc1\n\ts_waitcnt vmcnt(0)" : "=&v"(Aa[0]), "=&v"(Aa[1]) : "v"(T.va0), "v"(T.va1), "s"(g2_asrc<G0>(T)) : "memory");
        }
    }
    if (KG == 0 && CHK && T.poller) flags_wait<POLL_SLEEP>(T.hf, t == 12 ? T.flags + 128 : T.flags, t == 20 ? T.foz : T.foa, T.tag);
    if (t == G2_F) WAIT_LGKM0();
    if (t % G2_BE == 0) { MEMFENCE(); SBAR(); BAR(); MEMFENCE(); SBAR(); }
    f32x4 ho[4];
    if (t == G2_F) {
#pragma unroll
        for (int j = 0; j < 4; ++j) ho[j] = *(LAS const f32x4*)(T.xr + j * 1024);
        SBAR();
    }
#define AB2DS true
#define RL2(n, h) do { if (AB2DS && RL && (n) >= 0) { T.B[(n) >= 0 ? (n) : 0][h] = g2_ldb<t + 1>(T, (n) >= 0 ? (n) : 0, h); SBAR(); } } while (0)
#define P2(n) do { constexpr int c_ = TB ? ((n) & 3) : (n); u32x4 (&A_)[2] = (n) < 4 ? Aa : Ab; const bf16x8 b_ = cat(T.B[n][0], T.B[n][1]); \
                   if (t == 0) MFMA_NEW(T.acc[(n) & 1][c_], b_, A_[(n) & 1]); else MFMA_ACC(T.acc[(n) & 1][c_], b_, A_[(n) & 1]); SBAR(); RL2((n) - 1, 1); D((n) * 2); \
                   if (t == 0) MFMA_NEW(T.acc[~(n) & 1][c_], b_, A_[~(n) & 1]); else MFMA_ACC(T.acc[~(n) & 1][c_], b_, A_[~(n) & 1]); SBAR(); RL2(n, 0); D((n) * 2 + 1); } while (0)
#define VW(i) do { if (ISW) { g2_w<ISW ? t + G2_WL : 0, i>(T.W); SBAR(); } } while (0)
#define VA(i) do { if (NA == 2) { g2_a<(i) < 2 ? GN : GN1, (i) & 1>(T); SBAR(); } else if (NA && ((i) & 1)) { g2_a<GN, (i) / 2>(T); SBAR(); } } while (0)
    f32x4& dg = T.dg[(t >> 2) & 1][t & 3]; f32x4& du = T.du[(t >> 2) & 1][t & 3];
#define D(i) do { if (t < 8 && (i) < 13) { asm volatile("" : "+v"(dg)); \
        if ((i) == 0) dg = dg * -1.4426950408889634f; \
        else if ((i) < 5) dg[((i) - 1) & 3] = __builtin_amdgcn_exp2f(dg[((i) - 1) & 3]); \
        else if ((i) == 5) dg = dg + 1.0f; \
        else if ((i) < 10) dg[((i) - 6) & 3] = __builtin_amdgcn_rcpf(dg[((i) - 6) & 3]); \
        else if ((i) == 10) dg = dg * du; \
        else { T.hq[(t & 1) * 2 + (((i) - 11) & 1)] = pk_bf16(dg[(((i) - 11) & 1) * 2], dg[(((i) - 11) & 1) * 2 + 1]); asm volatile("" : "+v"(T.hq[(t & 1) * 2 + (((i) - 11) & 1)])); } \
        if ((i) < 11) asm volatile("" : "+v"(dg)); \
        SBAR(); } } while (0)
#define SEND(j) do { if (t == G2_F) { const u32x4 w = never_ones(__builtin_bit_cast(u32x4, T.acc[0][4 + (j)] + ho[j])); \
        asm volatile("global_store_dwordx4 %0, %1, off offset:%2 sc1\n\ts_nop 1" :: "v"(T.snd), "v"(w), "i"((j) * 1024) : "memory"); SBAR(); } } while (0)
    static_assert(g2_wops(G2_F) == 0 && g2_alast(G2_F) == g2_afirst(G2_F), "the sends are the only memory operations of their stage");
    if (KG == 1) { VW(0); VW(1); VA(0); VA(1); VA(2); VA(3); SEND(0); SEND(1); SEND(2); SEND(3); P2(0); P2(1); P2(2); P2(3); P2(4); P2(5); P2(6); P2(7); }
    else { P2(0); SEND(0); P2(1); SEND(1); P2(2); SEND(2); P2(3); SEND(3); VW(0); P2(4); VW(1); P2(5); VA(0); VA(1); P2(6); VA(2); VA(3); P2(7); }
    RL2(7, 1);
#undef SEND
#undef RL2
#undef VW
#undef VA
#undef D
#undef P2
    if (t < 8 && (t & 1)) {
        auto r0 = __builtin_amdgcn_permlane16_swap(T.hq[0], T.hq[2], false, false);
        auto r1 = __builtin_amdgcn_permlane16_swap(T.hq[1], T.hq[3], false, false);
        const u32x4 w = {r0[0], r1[0], r0[1], r1[1]};
        asm volatile("global_store_dwordx4 %0, %1, %2 offset:%3 sc1\n\ts_nop 1" :: "v"(T.hl), "v"(w), "s"(T.dd[(t >> 2) & 1]), "i"((t & 2) * 1024) : "memory");
        SBAR();
    }
    if (t == G2_PA || t == G2_PY || t == G2_PZ) {
        asm volatile("global_load_dwordx2 %0, %1, %2 offset:%3 sc1" : "=v"(T.hf) : "v"(t == G2_PZ ? T.foz : T.foa), "s"(T.flags), "i"(t == G2_PY ? 128 : 0) : "memory"); SBAR();
    }
    if (t == G2_F - 1) {
        MFMA_DRAIN(); SBAR();
#pragma unroll
        for (int j = 0; j < 4; ++j) *(LAS f32x4*)(T.xw + j * 1024) = T.acc[1][4 + j];
        SBAR();
    }
    if (t == G2_XST) { asm volatile("global_load_dwordx4 %0, %2, off\n\tglobal_load_dwordx4 %1, %2, off offset:64" : "=&v"(T.xres[0]), "=&v"(T.xres[1]) : "v"(T.xp) : "memory"); SBAR(); }
    if (t == G2_RCV) {
        asm volatile("global_load_dwordx4 %0, %4, off sc1\n\tglobal_load_dwordx4 %1, %4, off offset:1024 sc1\n\tglobal_load_dwordx4 %2, %4, off offset:2048 sc1\n\tglobal_load_dwordx4 %3, %4, off offset:3072 sc1"
                     : "=&v"(T.rc[0]), "=&v"(T.rc[1]), "=&v"(T.rc[2]), "=&v"(T.rc[3]) : "v"(T.rcv) : "memory");
        SBAR();
    }
}
template <int KG, int t, int E> DEVI void g2_run(G2St& T) { g2_stage<KG, t>(T); if constexpr (t + 1 < E) g2_run<KG, t + 1, E>(T); }

DEVI unsigned bsel(unsigned m, unsigned a, unsigned b) { return (m & a) | (~m & b); }
typedef short s16x4 __attribute__((ext_vector_type(4)));
#define MFMA16K16(a, b, c) __builtin_amdgcn_mfma_f32_16x16x16bf16_1k(__builtin_bit_cast(s16x4, a), __builtin_bit_cast(s16x4, b), (c), 0, 0, 0)
template <bool MAX> DEVI float rows_reduce(const float (&v)[4]) {
    auto a = __builtin_amdgcn_permlane16_swap(__float_as_uint(v[0]), __float_as_uint(v[1]), false, false);
    auto b = __builtin_amdgcn_permlane16_swap(__float_as_uint(v[2]), __float_as_uint(v[3]), false, false);
    const float t = MAX ? fmaxf(__uint_as_float(a[0]), __uint_as_float(a[1])) : __uint_as_float(a[0]) + __uint_as_float(a[1]);
    const float u = MAX ? fmaxf(__uint_as_float(b[0]), __uint_as_float(b[1])) : __uint_as_float(b[0]) + __uint_as_float(b[1]);
    auto c = __builtin_amdgcn_permlane32_swap(__float_as_uint(t), __float_as_uint(u), false, false);
    return MAX ? fmaxf(__uint_as_float(c[0]), __uint_as_float(c[1])) : __uint_as_float(c[0]) + __uint_as_float(c[1]);
}
DEVI void rows_spread(float w, float (&v)[4]) {
    auto c = __builtin_amdgcn_permlane32_swap(__float_as_uint(w), __float_as_uint(w), false, false);
    auto a = __builtin_amdgcn_permlane16_swap(c[0], c[0], false, false);
    auto b = __builtin_amdgcn_permlane16_swap(c[1], c[1], false, false);
    v[0] = __uint_as_float(a[0]); v[1] = __uint_as_float(a[1]); v[2] = __uint_as_float(b[0]); v[3] = __uint_as_float(b[1]);
}

template <int GRP>
DEVI void g1_body(G1Ld& L, G2St& T, LAS char* lds, char* ws, int rt, int ct, int wm, int kh STAMP_ARGS) {
    G1Ptr P;
    {
        const int lane = fresh_lane(), g = lane >> 4, q = (lane & 15) >> 2, p = lane & 3;
        const unsigned ldsb = (unsigned)(size_t)lds;
        P.pA = ldsb + wm * 4096 + lane * 16;
        P.pH = ldsb + lane * 16;
        const int brow = (16 * (g >> 1) + 4 * (g & 1) + q) * 224 + p * 8;
        P.pB01 = ldsb + G1_BOFF + brow;
        P.pB23 = P.pB01 + 2 * G1_BST;
        P.pBh01 = P.pB01 + (2 + wm) * 32;
        P.pBh23 = P.pB23 + (2 + wm) * 32;
    }
    f32x4 acc[4][10], acch = {0.f, 0.f, 0.f, 0.f};
#pragma unroll
    for (int m = 0; m < 4; ++m)
#pragma unroll
        for (int n = 0; n < 10; ++n) acc[m][n] = (f32x4){0.f, 0.f, 0.f, 0.f};

    g1_pa<0>(L); if (GRP == 0) g1_px<0>(L);
    g1_pa<1>(L); if (GRP == 0) g1_px<1>(L);
#pragma unroll
    for (int m = 0; m < 4; ++m)
        asm volatile("" : "+v"(acc[m][0]), "+v"(acc[m][1]), "+v"(acc[m][2]), "+v"(acc[m][3]), "+v"(acc[m][4]), "+v"(acc[m][5]), "+v"(acc[m][6]), "+v"(acc[m][7]), "+v"(acc[m][8]), "+v"(acc[m][9]), "+v"(acch));
    asm volatile("s_cmp_eq_u32 %1, 0\n\ts_cbranch_scc1 .Lsk_%=\n\ts_waitcnt vmcnt(%0)\n.Lsk_%=:\n\ts_barrier" :: "i"(GRP == 0 ? 3 : 2), "s"(L.ma) : "memory", "scc", FRAG_REGS);
    g1_ldb<GRP, 0, 0, 0>(P); g1_ldb<GRP, 0, 0, 1>(P); g1_ldb<GRP, 0, 1, 0>(P); g1_ldb<GRP, 0, 1, 1>(P); g1_ldb<GRP, 0, 2, 0>(P); g1_ldb<GRP, 0, 2, 1>(P);
    g1_ldb<GRP, 0, 3, 0>(P); g1_ldb<GRP, 0, 3, 1>(P); g1_ldb<GRP, 0, 4, 0>(P); g1_ldb<GRP, 0, 4, 1>(P);
    if (GRP == 0) { g1_ldah<0>(P); g1_ldbh<0, 0>(P); g1_ldbh<0, 1>(P); }
    g1_lda<0, 0>(P); g1_lda<0, 1>(P);
    g1_ldb<GRP, 0, 5, 0>(P); g1_ldb<GRP, 0, 5, 1>(P); g1_ldb<GRP, 0, 6, 0>(P); g1_ldb<GRP, 0, 6, 1>(P); g1_ldb<GRP, 0, 7, 0>(P); g1_ldb<GRP, 0, 7, 1>(P);
    g1_lda<0, 2>(P); g1_lda<0, 3>(P);
    g1_issue<GRP, 2>(L);
    if (GRP == 1) g1_issue<1, 3>(L);
    STAMP_SEG(0);
#pragma unroll 1
    for (int it = 0; it < 8; ++it) {
        g1_stage<GRP, 0>(acc, acch, L, it, P);
        if (GRP == 0) g1_tail<0>(L, it);
        g1_stage<GRP, 1>(acc, acch, L, it, P);
        g1_stage<GRP, 2>(acc, acch, L, it, P);
        g1_stage<GRP, 3>(acc, acch, L, it, P);
    }
    asm volatile("s_waitcnt vmcnt(0) lgkmcnt(0)\n\ts_nop 15" ::: "memory", FRAG_REGS);
    SBAR();
    STAMP_SEG(0);

    const int lane = fresh_lane(), g = lane >> 4, i16 = lane & 15;
    char* ho = ws + WS_HO;
    const int mb0 = 16 * rt + 4 * wm;
    const unsigned hl = (unsigned)((g & 1) * 1024 + (g >> 1) * 256 + i16 * 16);
    G2W& W = T.W;
    {
        const int wid = GRP * 4 + wm, r = lane >> 3, c = (lane & 7) ^ ((r & 2) | ((wid & 1) << 2));
        W.vw0 = (unsigned)((8 * wid + r) * 2048 + c * 16 + kh * 128);
        W.vw1 = W.vw0 ^ 128;
        W.vw2 = W.vw0 + 448 * 2048;
    }
    char* hs = ho + (size_t)(64 * (ct >> 4) + (ct & 15)) * SLAB_B + (size_t)mb0 * 1024;
#define GW(i) do { if ((i) < 2 * G2_WL) { SBAR(); g2_w<(i) / 2, (i) % 2>(W); SBAR(); } } while (0)
#define GLU1(n, m) glu_store(acc[m][n], acc[m][5 + (n)], acc[(m) + 1][n], acc[(m) + 1][5 + (n)], hs + (size_t)(16 * (1 + (n) / 2)) * SLAB_B + ((n) & 1) * 512 + (m) * 1024 + hl)
    if (GRP == 1) {
        SBAR(); BAR(); MEMFENCE(); SBAR();
        GW(0); GW(1); GW(2); GW(3); GLU1(0, 0); GW(4); GW(5); GLU1(0, 2); GW(6); GW(7); GLU1(1, 0); GW(8); GW(9); GLU1(1, 2); GW(10); GW(11); GLU1(2, 0); GW(12); GW(13); GLU1(2, 2); GW(14); GW(15);
        asm volatile("s_waitcnt vmcnt(%0)" :: "i"(2 * G2_WL + 4) : "memory");
#pragma unroll
        for (int m = 0; m < 4; ++m) { T.dg[0][m] = acc[m][3]; T.dg[1][m] = acc[m][4]; T.du[0][m] = acc[m][8]; T.du[1][m] = acc[m][9]; }
        T.dd[0] = hs + (size_t)32 * SLAB_B + 512; T.dd[1] = hs + (size_t)48 * SLAB_B;
    } else {
        u32x4 Qf[4], Kf[4]; u32x2 Vh[4][2];
#pragma unroll
        for (int m = 0; m < 4; ++m) {
            Qf[m] = (u32x4){pk_bf16(acc[m][0][0], acc[m][0][1]), pk_bf16(acc[m][0][2], acc[m][0][3]), pk_bf16(acc[m][1][0], acc[m][1][1]), pk_bf16(acc[m][1][2], acc[m][1][3])};
            Kf[m] = (u32x4){pk_bf16(acc[m][2][0], acc[m][2][1]), pk_bf16(acc[m][2][2], acc[m][2][3]), pk_bf16(acc[m][3][0], acc[m][3][1]), pk_bf16(acc[m][3][2], acc[m][3][3])};
#pragma unroll
            for (int b = 0; b < 2; ++b) Vh[m][b] = (u32x2){pk_bf16(acc[m][4 + b][0], acc[m][4 + b][1]), pk_bf16(acc[m][4 + b][2], acc[m][4 + b][3])};
        }
        LAS char* xs = lds + G1_XOFF;
        if (wm < 3) {
            *(LAS u32x4*)(xs + (wm + 1) * 2048 + lane * 16) = Kf[3];
            *(LAS u32x4*)(xs + (wm + 1) * 2048 + 1024 + lane * 16) = (u32x4){Vh[3][0][0], Vh[3][0][1], Vh[3][1][0], Vh[3][1][1]};
        }
        {
            const unsigned h0 = pk_bf16(acch[0], acch[1]), h1 = pk_bf16(acch[2], acch[3]);
            if (wm < 2) {
                *(LAS u32x2*)(xs + lane * 16 + wm * 8) = (u32x2){h0, h1};
            } else {
                LAS char* t = xs + 1024 + ((i16 >> 2) * 16 + 4 * g) * 16 + (wm - 2) * 8 + (i16 & 3) * 2;
                *(LAS u16*)(t) = (u16)h0; *(LAS u16*)(t + 16) = (u16)(h0 >> 16); *(LAS u16*)(t + 32) = (u16)h1; *(LAS u16*)(t + 48) = (u16)(h1 >> 16);
            }
        }
        asm volatile("s_waitcnt lgkmcnt(0)" ::: "memory");
        SBAR(); BAR(); MEMFENCE(); SBAR();
        GW(0); GW(1);
        const u32x4 Kp = *(LAS const u32x4*)(xs + wm * 2048 + lane * 16);
        const u32x4 Vp = *(LAS const u32x4*)(xs + wm * 2048 + 1024 + lane * 16);
        const int dc = i16 - 4 * g - (i16 & 1);
        unsigned modd = (i16 & 1) ? ~0u : 0u, mc0 = dc >= 0 ? ~0u : 0u, mc1 = dc >= 2 ? ~0u : 0u;
        asm volatile("" : "+v"(modd), "+v"(mc0), "+v"(mc1));
        const unsigned half = (modd & 0xffff0000u) | (~modd & 0x0000ffffu);
        const unsigned pp0 = half & ~mc0, pp1 = half & ~mc1, pc0 = half & mc0, pc1 = half & mc1;
        const float sc = 0.17677669529663687f * 1.4426950408889634f;
        const f32x4 z = {0.f, 0.f, 0.f, 0.f};
        float e0[4], e1[4], red[4];
#pragma unroll
        for (int m = 0; m < 4; ++m) {
            u32x4 s0 = __builtin_bit_cast(u32x4, MFMA16(as_frag(m ? Kf[m ? m - 1 : 0] : Kp), as_frag(Qf[m]), z));
            const u32x4 s1 = __builtin_bit_cast(u32x4, MFMA16(as_frag(Kf[m]), as_frag(Qf[m]), z));
            if (m == 0 && rt == 0 && wm == 0) { const unsigned c = __float_as_uint(-1e9f / 0.17677669529663687f); s0 = (u32x4){c, c, c, c}; }
            e0[m] = __uint_as_float(bsel(modd, bsel(mc0, s1[1], s0[1]), bsel(mc0, s1[0], s0[0])));
            e1[m] = __uint_as_float(bsel(modd, bsel(mc1, s1[3], s0[3]), bsel(mc1, s1[2], s0[2])));
            red[m] = fmaxf(e0[m], e1[m]);
        }
        asm volatile("" : "+v"(red[0]), "+v"(red[1]), "+v"(red[2]), "+v"(red[3])); GW(2); GW(3);
        {
            rows_spread(rows_reduce<true>(red), red);
        }
        asm volatile("" : "+v"(red[0]), "+v"(red[1]), "+v"(red[2]), "+v"(red[3])); GW(4); GW(5);
#pragma unroll
        for (int m = 0; m < 4; ++m) {
            e0[m] = __builtin_amdgcn_exp2f((e0[m] - red[m]) * sc); e1[m] = __builtin_amdgcn_exp2f((e1[m] - red[m]) * sc);
            red[m] = e0[m] + e1[m];
        }
        asm volatile("" : "+v"(red[0]), "+v"(red[1]), "+v"(red[2]), "+v"(red[3])); GW(6); GW(7);
        {
            float w = __builtin_amdgcn_rcpf(rows_reduce<false>(red));
            rows_spread(w, red);
        }
        char* od = hs + (((g & 1) << 1) | (g >> 1)) * 256 + i16 * 16;
#pragma unroll
        for (int m = 0; m < 4; ++m) {
            const float p0 = e0[m] * red[m], p1 = e1[m] * red[m];
            const unsigned w0 = pk_bf16(p0, p0), w1 = pk_bf16(p1, p1);
            const u32x2 Pp = {w0 & pp0, w1 & pp1}, Pc = {w0 & pc0, w1 & pc1};
            const u32x2 Vp0 = m ? Vh[m ? m - 1 : 0][0] : (u32x2){Vp[0], Vp[1]}, Vp1 = m ? Vh[m ? m - 1 : 0][1] : (u32x2){Vp[2], Vp[3]};
            const f32x4 o0 = MFMA16K16(Vh[m][0], Pc, MFMA16K16(Vp0, Pp, z));
            const f32x4 o1 = MFMA16K16(Vh[m][1], Pc, MFMA16K16(Vp1, Pp, z));
            unsigned x0 = pk_bf16(o0[0], o0[1]), x1 = pk_bf16(o0[2], o0[3]), y0 = pk_bf16(o1[0], o1[1]), y1 = pk_bf16(o1[2], o1[3]);
            auto r0 = __builtin_amdgcn_permlane16_swap(x0, y0, false, false);
            auto r1 = __builtin_amdgcn_permlane16_swap(x1, y1, false, false);
            st16(od + m * 1024, never_ones((u32x4){r0[0], r1[0], r0[1], r1[1]}));
        }
        SBAR();
        GW(8); GW(9); GW(10); GW(11); GW(12); GW(13); GW(14); GW(15);
        asm volatile("s_waitcnt vmcnt(%0)" :: "i"(2 * G2_WL + 2) : "memory");
#pragma unroll
        for (int m = 0; m < 4; ++m) { T.dg[0][m] = acc[m][6]; T.dg[1][m] = acc[m][7]; T.du[0][m] = acc[m][8]; T.du[1][m] = acc[m][9]; }
        T.dd[0] = hs + (size_t)48 * SLAB_B + 512; T.dd[1] = ho + (size_t)(128 + (ct >> 1)) * SLAB_B + (ct & 1) * 512 + (size_t)mb0 * 1024;
        if (blockIdx.x == 0 && wm == 1) {
            flags_wait<POLL_SLEEP>((u32x2){~T.tag, 0u}, ws + WS_LF, (unsigned)(g * 8192 + i16 * 8), T.tag);
            flags_wait<POLL_SLEEP>((u32x2){~T.tag, 0u}, ws + WS_LF + 32768, (unsigned)(g * 8192 + i16 * 8), T.tag);
            flag_set<0>(ws + WS_EP, T.tag);
        }
    }
#undef GW
#undef GLU1
    STAMP_SEG(2);
}

DEVI float sum_x8(float v) {
    v += __uint_as_float(__builtin_amdgcn_update_dpp(0u, __float_as_uint(v), 0x128  , 0xf, 0xf, false));
    { auto t = __builtin_amdgcn_permlane16_swap(__float_as_uint(v), __float_as_uint(v), false, false); v = __uint_as_float(t[0]) + __uint_as_float(t[1]); }
    { auto t = __builtin_amdgcn_permlane32_swap(__float_as_uint(v), __float_as_uint(v), false, false); v = __uint_as_float(t[0]) + __uint_as_float(t[1]); }
    return v;
}

template <int KG>
DEVI void g2_body(G2St& T, LAS char* lds, char* ws, const u16* x, u16* out, int rt, int ct, int wm STAMP_ARGS2) {
    constexpr int kg = KG;
    const int bid = blockIdx.x, idx = bid >> 3, wid = KG * 4 + wm;
    const int kh = (bid >> 2) & 1, side = kh ^ 1, rt2 = 2 * rt + ((bid >> 1) & 1), ct2 = 4 * (bid & 1) + (idx & 3);
    const char* flq = ws + WS_FL + (rt * 4 + (bid & 2)) * 512;
    const int lane2 = fresh_lane(), g = lane2 >> 4, i16 = lane2 & 15, q = i16 >> 2, p = lane2 & 3;
    {
        const char* a0 = ws + WS_HO + (size_t)kg * SLAB_B + (size_t)(8 * rt2 + 2 * wm) * 1024;
        T.pa = a0 + (size_t)(64 * side) * SLAB_B;
        T.pad = a0 + (size_t)128 * SLAB_B;
        T.va0 = lane2 * 16 + kg * 1024; T.va1 = T.va0 ^ 1024;
        T.flagA = ws + WS_FL + (kg ^ 1) * 16384 + (rt * 4 + wm) * 512 + ct * 4;
        T.flagZ = ws + WS_FL + (rt * 4 + wm) * 512 + 256 + kg * 128 + ct * 4;
        T.hl = (unsigned)((g & 1) * 1024 + (g >> 1) * 256 + i16 * 16);
        T.poller = wid == 0;
        T.flags = T.poller ? flq : ws + WS_XB + (size_t)bid * 32768;
        T.foa = (unsigned)((lane2 >> 5) * 512 + side * 64 + (lane2 & 7) * 8);
        T.foz = (unsigned)((lane2 >> 5) * 512 + 256 + (lane2 & 31) * 8);
    }
#pragma unroll
    for (int n = 0; n < 4; ++n) {
        T.pB[0][n] = lds + kg * 4096 + (8 * g + q) * 128 + (((2 * n + (p >> 1)) ^ ((q & 2) | ((g & 1) << 2))) * 16) + (p & 1) * 8;
#pragma unroll
        for (int k = 1; k < (G2_NS + 3) / 4; ++k) { T.pB[k][n] = T.pB[0][n] + k * 4 * G2_ST; asm volatile("" : "+v"(T.pB[k][n])); }
        asm volatile("" : "+v"(T.pB[0][n]));
    }
    MEMFENCE(); SBAR(); BAR(); MEMFENCE(); SBAR();
    g2_a<0, 0>(T); g2_a<0, 1>(T); g2_a<1, 0>(T); g2_a<1, 1>(T);
    SBAR();
#pragma unroll
    for (int n = 0; n < 8; ++n) { T.B[n][0] = g2_ldb<0>(T, n, 0); T.B[n][1] = g2_ldb<0>(T, n, 1); }
    SBAR();
    STAMP_SEG(3);
    g2_run<KG, 0, 8>(T);
    STAMP_SEG(4);
    g2_run<KG, 8, 24>(T);
    const int orow = 128 * rt2 + 32 * wm + 16 * kg + i16, ocol = 128 * ct2 + 64 * kh + (g & 1) * 16 + (g >> 1) * 8;
    T.xw = lds + G2_XOFF + wid * 4096 + lane2 * 16;
    T.xr = lds + G2_XOFF + (wid ^ 4) * 4096 + lane2 * 16;
    T.snd = ws + WS_XB + (size_t)bid * 32768 + wid * 4096 + lane2 * 16;
    T.rcv = ws + WS_XB + (size_t)(bid ^ 4) * 32768 + wid * 4096 + lane2 * 16;
    T.xp = x + (size_t)orow * D_ + ocol;
    g2_run<KG, 24, 36>(T);
    STAMP_SEG(5);

    MFMA_DRAIN(); SBAR();
#pragma unroll
    for (int j = 0; j < 4; ++j) *(LAS f32x4*)(T.xw + j * 1024) = T.acc[1][j];
    WAIT_LGKM0();
    MEMFENCE(); SBAR(); BAR(); MEMFENCE(); SBAR();
#pragma unroll
    for (int j = 0; j < 4; ++j) T.acc[0][j] += *(LAS const f32x4*)(T.xr + j * 1024);
    asm volatile("s_waitcnt vmcnt(0)" : "+v"(T.xres[0]), "+v"(T.xres[1]), "+v"(T.rc[0]), "+v"(T.rc[1]), "+v"(T.rc[2]), "+v"(T.rc[3]) :: "memory");
    for (unsigned spin = 0;; ++spin) {
        const unsigned mx = umax4((u32x4){umax4(T.rc[0]), umax4(T.rc[1]), umax4(T.rc[2]), umax4(T.rc[3])});
        if (!__any(mx == 0xffffffffu) || spin > SPIN_CAP) break;
        __builtin_amdgcn_s_sleep(4);
        asm volatile("global_load_dwordx4 %0, %4, off sc1\n\tglobal_load_dwordx4 %1, %4, off offset:1024 sc1\n\tglobal_load_dwordx4 %2, %4, off offset:2048 sc1\n\tglobal_load_dwordx4 %3, %4, off offset:3072 sc1\n\ts_waitcnt vmcnt(0)"
                     : "=&v"(T.rc[0]), "=&v"(T.rc[1]), "=&v"(T.rc[2]), "=&v"(T.rc[3]) : "v"(T.rcv) : "memory");
    }
    u16* orp = out + (size_t)orow * D_ + ocol;
#pragma unroll
    for (int j = 0; j < 4; j += 2) {
        const f32x4 s0 = T.acc[0][j] + __builtin_bit_cast(f32x4, T.rc[j]), s1 = T.acc[0][j + 1] + __builtin_bit_cast(f32x4, T.rc[j + 1]);
        float v[8];
#pragma unroll
        for (int e = 0; e < 4; ++e) { auto r = __builtin_amdgcn_permlane16_swap(__float_as_uint(s0[e]), __float_as_uint(s1[e]), false, false); v[e] = __uint_as_float(r[0]); v[4 + e] = __uint_as_float(r[1]); }
        const u32x4 xv = T.xres[j >> 1];
        u32x4 o;
#pragma unroll
        for (int e = 0; e < 4; ++e) o[e] = pk_bf16(v[2 * e] + bf_lo(xv[e]), v[2 * e + 1] + bf_hi(xv[e]));
        *(GAS u32x4*)(size_t)(orp + 16 * j) = o;
    }
    STAMP_SEG(6);
    STAMP_OUT(ws, 0, wid, lane2);
    STAMP_OUT(ws, 1, wid, lane2);
}

__global__ __launch_bounds__(512, 2) void k_main(const char* __restrict__ wqkv, const char* __restrict__ wg, const char* __restrict__ wu, const char* __restrict__ wo, const char* __restrict__ wd, char* __restrict__ ws,
                                                const u16* __restrict__ x, u16* __restrict__ out) {
    extern __shared__ __attribute__((aligned(16))) char smem[];
    TL_ENTRY();
    asm volatile("" : "+s"(wqkv), "+s"(wg), "+s"(wu), "+s"(wo), "+s"(wd), "+s"(ws), "+s"(x), "+s"(out));
    LAS char* lds = (LAS char*)smem;
    const int tid = threadIdx.x, lane = tid & 63;
    const int wid = __builtin_amdgcn_readfirstlane(tid >> 6);
    const int bid = blockIdx.x, xcd = bid & 7, idx = bid >> 3;
    const int ct = 4 * xcd + (idx & 3), rt = idx >> 2;
    const unsigned ldsb = (unsigned)(size_t)lds;
    const int r8 = lane & 7, c8 = lane >> 3, row = 256 * rt + 8 * ct + r8;
    u32x4 xv[2];
    unsigned ep;
    asm volatile("global_load_dwordx4 %0, %3, %4\n\tglobal_load_dwordx4 %1, %3, %4 offset:128\n\ts_load_dword %2, %5, 0x0"
                 : "=&v"(xv[0]), "=&v"(xv[1]), "=&s"(ep) : "v"((unsigned)(row * 2048 + (16 * wid + c8) * 16)), "s"(x), "s"(ws + WS_EP) : "memory");
    G1Ld L;
    {
        unsigned e0, e1;
        asm volatile("s_getpc_b64 %0\n.Lk_pc:\n\ts_mov_b32 %1, .Lk_end0-.Lk_pc\n\ts_mov_b32 %2, .Lk_end1-.Lk_pc" : "=s"(L.pc), "=s"(e0), "=s"(e1));
        const unsigned size = e0 > e1 ? e0 : e1, off = (unsigned)(idx * 16 + (lane & 15)) * 128u;
        L.vc = off < size ? off : size;
        g1_code(L, wid, 7);
    }
    {
        const int c = wid * 64 + lane, rho = c / 14, cc = c - rho * 14;
        const int k = (rho & ~12) | ((rho & 4) << 1) | ((rho & 8) >> 1);
        const int c2 = cc < 12 ? cc : 11;
        L.pa = ws + WS_XN + (size_t)(16 * rt + 2 * wid) * 1024;
        L.stepa = (unsigned)SLAB_B;
        L.va = lane * 16;
        L.la = ldsb + (1 + 2 * wid) * 1024;
        L.px = ws + WS_XN + (size_t)(wid < 2 ? 16 * rt + 14 + wid : rt == 0 ? 0 : 16 * rt - 1) * 1024;
        L.stepx = (unsigned)SLAB_B;
        L.vx = (unsigned)((wid >= 2 && rt == 0 ? (lane & 48) : lane) * 16);
        L.lx = wid < 2 ? ldsb + (15 + wid) * 1024 : ldsb;
        L.p2 = wqkv + ct * 64;
        L.step2 = 32u * 3072u * 2u;
        L.v2 = (unsigned)(k * 6144 + (c2 >> 2) * 2048 + (c2 & 3) * 16);
        L.l2 = ldsb + G1_BOFF + wid * 1024;
        L.pg = wg + ct * 224;
        L.pu = wu + ct * 224;
        L.stepg = 32u * 3584u * 2u;
        L.vg = (unsigned)(k * 7168 + cc * 16);
        L.ma = s_if_lt<7, -1, 0>(wid); L.mb = L.ma; L.mx = s_if_lt<3, -1, 1>(wid); L.mt = s_if_lt<7, 0, (32 - T_S0) / T_J - T_BURST>(wid);
        const int j = lane < 20 * T_J ? lane / 20 : T_J - 1, i = lane < 20 * T_J ? lane - 20 * j : 19, trow = 32 * (T_S0 + j) + idx;
        L.ta = i < 6 ? wqkv + (size_t)trow * 6144 + (i >> 1) * 2048 + xcd * 256 + (i & 1) * 128 : (i < 13 ? wg + (i - 6) * 128 : wu + (i - 13) * 128) + (size_t)trow * 7168 + xcd * 896;
        L.ts = i < 6 ? 32u * T_J * 6144u : 32u * T_J * 7168u;
        L.m7 = s_if_lt<7, 0, 4>(wid);
    }
    L.tb = lane < 8 ? wo + bid * 8192 + lane * 128 : wd + bid * 28672 + (lane < 36 ? lane - 8 : 27) * 128;
    L.tt = lane < 8 ? 1024u : 3584u;
    L.tb -= 4 * L.tt;
    L.pf = ws + WS_XB + (size_t)bid * 32768 + (wid & 3) * 8192;
    const int kh = (bid >> 2) & 1, side = kh ^ 1, rt2 = 2 * rt + ((bid >> 1) & 1), ct2 = 4 * (bid & 1) + (idx & 3);
    const int kg = wid >> 2, wm = wid & 3;
    G2St T;
    T.W.pwo = wo + (size_t)(side * 512) * 2048 + ct2 * 256;
    T.W.pwh = wd + (size_t)(side * 1792 + kg * 80) * 2048 + ct2 * 256;
    T.W.pwd = wd + (size_t)(96 + (wid >> 1) * 96) * 2048 + ct2 * 256;
    T.W.ldsw = ldsb + wid * 1024;
    STAMP_DECL STAMP_START();
    const int m6 = s_if_lt<1, -1, 0>(wid ^ 6), m05 = s_if_lt<6, -1, 0>(wid);
#define W_FIRST() do { g1_p2<0>(L); g1_pg<0>(L); g1_pu<0>(L); \
        g1_piece<G1_BST, 0>(L.p2, L.v2, L.l2, m6); g1_piece<G1_BST + G1_REG, 0>(L.pg, L.vg, L.l2, m6); g1_piece<G1_BST + 2 * G1_REG, 0>(L.pu, L.vg, L.l2, m6); } while (0)
    W_FIRST();
    asm volatile("s_cmp_eq_u32 %3, 6\n\ts_cbranch_scc1 .Lw6_%=\n\ts_cmp_eq_u32 %3, 7\n\ts_cbranch_scc1 .Lw7_%=\n\ts_waitcnt vmcnt(3)\n\ts_branch .Lw_%=\n"
                 ".Lw6_%=:\n\ts_waitcnt vmcnt(6)\n\ts_branch .Lw_%=\n.Lw7_%=:\n\ts_waitcnt vmcnt(%4)\n.Lw_%=:\n\ts_waitcnt lgkmcnt(0)"
                 : "+v"(xv[0]), "+v"(xv[1]), "+s"(ep) : "s"(wid), "i"(1) : "memory", "scc");
#pragma unroll
    for (int i = 0; i < (T_WHEN == 1 ? T_BURST : 0); ++i) g1_touch(L.ta, L.ts, 0, L.m7);
    T.tag = __builtin_amdgcn_readfirstlane(ep * 1664525u + 1013904223u);
    OPAQUE_S(T.tag);
    P0S(2);
    {
        LAS float* red = (LAS float*)(lds + G1_XOFF);
        f32x2 f[8];
#pragma unroll
        for (int j = 0; j < 8; ++j) f[j] = (f32x2){bf_lo(xv[j >> 2][j & 3]), bf_hi(xv[j >> 2][j & 3])};
        {
            const u32x4 ones = {0xffffffffu, 0xffffffffu, 0xffffffffu, 0xffffffffu};
            asm volatile("global_store_dwordx4 %0, %1, %2 sc1\n\tglobal_store_dwordx4 %0, %1, %2 offset:1024 sc1\n\ts_nop 1"
                         :: "v"(L.va), "v"(ones), "s"(ws + WS_HO + (size_t)(64 * (ct >> 4) + (ct & 15)) * SLAB_B + (size_t)(16 * rt + 2 * wid) * 1024) : "memory");
        }
        const f32x2 s2 = ((f[0] + f[1]) + (f[2] + f[3])) + ((f[4] + f[5]) + (f[6] + f[7]));
        const float mw = sum_x8(s2[0] + s2[1]) * (1.0f / 128);
        f32x2 q2 = {0.f, 0.f};
#pragma unroll
        for (int j = 0; j < 8; ++j) { const f32x2 d = f[j] - mw; q2 = __builtin_elementwise_fma(d, d, q2); }
        const float qw = sum_x8(q2[0] + q2[1]);
        if (c8 == 0) *(LAS f32x2*)(red + r8 * 16 + wid * 2) = (f32x2){mw, qw};
        WAIT_LGKM0(); MEMFENCE(); BAR(); MEMFENCE();
#pragma unroll
        for (int i = 0; i < (T_WHEN == 3 ? T_BURST : 0); ++i) g1_touch(L.ta, L.ts, 0, L.m7);
        f32x4 st[4];
#pragma unroll
        for (int k = 0; k < 4; ++k) st[k] = *(LAS const f32x4*)(red + r8 * 16 + k * 4);
        const float mean = (((st[0][0] + st[0][2]) + (st[1][0] + st[1][2])) + ((st[2][0] + st[2][2]) + (st[3][0] + st[3][2]))) * 0.125f;
        float qt = 0.f, dt = 0.f;
#pragma unroll
        for (int k = 0; k < 4; ++k) { const float d0 = st[k][0] - mean, d1 = st[k][2] - mean; qt += st[k][1] + st[k][3]; dt = fmaf(d0, d0, fmaf(d1, d1, dt)); }
        const float mb = rbf(mean);
        const float vb = rbf(fmaf(dt, 128.f, qt) * (1.0f / D_));
        const float den = rbf(__builtin_amdgcn_sqrtf(rbf(vb + 1e-5f)));
        const float rc = __builtin_amdgcn_rcpf(den);
#pragma unroll
        for (int i = 0; i < 2; ++i) {
            u32x4 o;
#pragma unroll
            for (int j = 0; j < 4; ++j) {
                const f32x2 c = f[i * 4 + j] - mb;
                const unsigned d = pk_bf16(c[0], c[1]);
                const f32x2 y = (f32x2){bf_lo(d), bf_hi(d)} * rc;
                o[j] = pk_bf16(y[0], y[1]);
            }
            const int ch = 16 * wid + 8 * i + c8;
            asm volatile("global_store_dwordx4 %0, %1, %2 sc1\n\ts_nop 1" :: "v"((unsigned)((ch >> 2) * (int)SLAB_B + (row >> 4) * 1024 + ((ch & 3) * 16 + (row & 15)) * 16)), "v"(o), "s"(ws + WS_XN) : "memory");
        }
        P0S(3);
        asm volatile("s_waitcnt vmcnt(0)\n\ts_barrier" ::: "memory");
        P0S(4);
        { const int keep = L.mb; L.mb = m05; g1_p2<1>(L); g1_pg<1>(L); g1_pu<1>(L); L.mb = keep; }
#pragma unroll
        for (int i = 0; i < (T_WHEN == 2 ? T_BURST : 0); ++i) g1_touch(L.ta, L.ts, 0, L.m7);
        if (wid == FLAG_WAVE) {
            const bool next = ct >= 30 && rt < 7;
            asm volatile("s_mov_b64 exec, %3\n\tglobal_store_dword %0, %1, %2 sc1\n\ts_mov_b64 exec, -1"
                         :: "v"((unsigned)((rt * 32 + lane) * 256 + (lane < 32 ? ct : ct + 2) * 4)), "v"(T.tag), "s"(ws + WS_LF), "s"(next ? ~0ull : 0xffffffffull) : "memory");
        }
        if (wid == 6) flags_wait<LN_SLEEP>((u32x2){~T.tag, 0u}, ws + WS_LF + (rt * 32 + ct) * 256, (unsigned)(lane < 16 || rt == 0 ? (lane & 15) * 8 : 128), T.tag);
        MEMFENCE(); BAR(); MEMFENCE();
    }
    STAMP_SEG(1);
    if (wid < 4) {
        g1_body<0>(L, T, lds, ws, rt, ct, wm, kh STAMP_PASS); g2_body<0>(T, lds, ws, x, out, rt, ct, wm STAMP_PASS2);
        TL_EXIT(ws, 0); TL_EXIT(ws, 1); TL_EXIT(ws, 2);
        asm volatile(".Lk_end0:");
        __builtin_amdgcn_endpgm();
    }
    g1_body<1>(L, T, lds, ws, rt, ct, wm, kh STAMP_PASS); g2_body<1>(T, lds, ws, x, out, rt, ct, wm STAMP_PASS2);
    TL_EXIT(ws, 0); TL_EXIT(ws, 1); TL_EXIT(ws, 2);
    asm volatile(".Lk_end1:");
    __builtin_amdgcn_endpgm();
}

extern "C" void kernel_launch(void* const* d_in, const int* in_sizes, int n_in,
                              void* d_out, int out_size, void* d_ws, size_t ws_size,
                              hipStream_t stream) {
    const u16* x = (const u16*)d_in[0];
    const char* wqkv = (const char*)d_in[1];
    const char* wo = (const char*)d_in[2];
    const char* wg = (const char*)d_in[4];
    const char* wu = (const char*)d_in[5];
    const char* wd = (const char*)d_in[6];
    char* ws = (char*)d_ws;
    (void)hipFuncSetAttribute((const void*)k_main, hipFuncAttributeMaxDynamicSharedMemorySize, G1_LDS);
    k_main<<<256, 512, G1_LDS, stream>>>(wqkv, wg, wu, wo, wd, ws, x, (u16*)d_out);
}
```

```cpp
#include <hip/hip_runtime.h>
#include <hip/hip_bf16.h>
#include <stdint.h>

typedef __bf16 bf16x8 __attribute__((ext_vector_type(8)));
typedef __bf16 bf16x4 __attribute__((ext_vector_type(4)));
typedef __bf16 bf16x2 __attribute__((ext_vector_type(2)));
typedef float f32x4 __attribute__((ext_vector_type(4)));
typedef float f32x2 __attribute__((ext_vector_type(2)));
typedef unsigned u32x4 __attribute__((ext_vector_type(4)));
typedef unsigned u32x2 __attribute__((ext_vector_type(2)));
typedef unsigned short u16;
#define LAS __attribute__((address_space(3)))
#define GAS __attribute__((address_space(1)))
#define DEVI __device__ __forceinline__

constexpr int S_ = 2048, D_ = 1024;
constexpr int MB_ = S_ / 16;
constexpr size_t SLAB_B = (size_t)MB_ * 1024;
constexpr size_t WS_XN = 0;
constexpr size_t WS_HO = 32 * SLAB_B;
constexpr size_t WS_XB = WS_HO + 144 * SLAB_B;
constexpr size_t WS_FL = WS_XB + 256 * 16384;
constexpr size_t WS_LF = WS_FL + 16384;
constexpr size_t WS_EP = WS_LF + 65536;

DEVI unsigned pk_bf16(float lo, float hi) {
    bf16x2 v = {(__bf16)lo, (__bf16)hi};
    return __builtin_bit_cast(unsigned, v);
}
DEVI float bf_lo(unsigned u) { return __uint_as_float(u << 16); }
DEVI float bf_hi(unsigned u) { return __uint_as_float(u & 0xffff0000u); }
DEVI float rbf(float f) { return (float)(__bf16)f; }
DEVI bf16x8 as_frag(u32x4 v) { return __builtin_bit_cast(bf16x8, v); }
#define MFMA16(a, b, c) __builtin_amdgcn_mfma_f32_16x16x32_bf16((a), (b), (c), 0, 0, 0)
#define SBAR() __builtin_amdgcn_sched_barrier(0)
#define MFMA_ACC(c, a, b) asm volatile("v_mfma_f32_16x16x32_bf16 %0, %1, %2, %0" : "+v"(c) : "v"(a), "v"(b))
#define MFMA_NEW(c, a, b) asm volatile("v_mfma_f32_16x16x32_bf16 %0, %1, %2, 0" : "=&v"(c) : "v"(a), "v"(b))
#define MFMA_DRAIN() asm volatile("s_nop 15" ::: "memory")
#define BAR() __builtin_amdgcn_s_barrier()

DEVI int fresh_lane() { int l; asm volatile("v_mbcnt_lo_u32_b32 %0, -1, 0\n\tv_mbcnt_hi_u32_b32 %0, -1, %0" : "=v"(l)); return l; }

constexpr int G1_AST = 17 * 1024;
constexpr int G1_BST = 21 * 1024;
constexpr int G1_REG = 7168;
constexpr int G1_BOFF = 4 * G1_AST;
constexpr int G1_XOFF = G1_BOFF + 4 * G1_BST;
constexpr int G1_LDS = G1_XOFF + 8192;

constexpr int FILL_IT0 = 3;
constexpr int T_BURST = 1;
#define FRAG_REGS "v191", "v192", "v193", "v194", "v195", "v196", "v197", "v198", "v199", "v200", "v201", "v202", "v203", "v204", "v205", "v206", "v207", "v208", "v209", "v210", "v211", "v212", "v213", "v214", "v215", "v216", "v217", "v218", "v219", "v220", "v221", "v222", "v223", "v224", "v225", "v226", "v227", "v228", "v229", "v230", "v231", "v232", "v233", "v234", "v235", "v236", "v237", "v238", "v239", "v240", "v241", "v242", "v243", "v244", "v245", "v246", "v247", "v248", "v249", "v250", "v251", "v252", "v253", "v254", "v255"
constexpr int FA = 192, FB = 208, FAH = 248, FBH = 252;
struct G1Ld {
    const char *pa, *p2, *pg, *pu, *px;
    unsigned va, v2, vg, vx;
    unsigned la, l2, lx;
    char* pf;
    int ma, mb, mx;
    const char *ta, *tb; unsigned ts, tt; int mt, m7;
};

#define WAIT_LGKM0() __builtin_amdgcn_s_waitcnt(0xC07F)
#define MEMFENCE() asm volatile("" ::: "memory")
#define OPAQUE_S(x) asm volatile("" : "+s"(x))

template <int B, int YES, int NO> DEVI int s_if_lt(int a) { int r; asm("s_cmp_lt_u32 %1, %2\n\ts_cselect_b32 %0, %3, %4" : "=s"(r) : "s"(a), "i"(B), "i"(YES), "i"(NO) : "scc"); return r; }
template <int LOFF, int KIND>
DEVI void g1_piece(const char* base, unsigned voff, unsigned lds, int msk) {
    asm volatile("s_cmp_eq_u32 %3, 0\n\ts_cbranch_scc1 .Lsk_%=\n\ts_add_u32 m0, %2, %4\n\ts_mov_b32 exec_lo, %3\n\ts_ashr_i32 exec_hi, %3, 31\n\t"
                 ".if %5 == 0\n\tglobal_load_lds_dwordx4 %0, %1\n\t.else\n\tglobal_load_lds_dwordx4 %0, %1 sc1\n\t.endif\n\t"
                 ".if %5 == 2\n\tglobal_load_lds_dwordx4 %0, %1 offset:1024 sc1\n\t.endif\n\t"
                 "s_mov_b64 exec, -1\n.Lsk_%=:"
                 :: "v"(voff), "s"(base), "s"(lds), "s"(msk), "i"(LOFF), "i"(KIND) : "memory", "scc");
}
template <int SLOT> DEVI void g1_pa(G1Ld& L) { g1_piece<SLOT * G1_AST, 2>(L.pa, L.va, L.la, L.ma); L.pa += SLAB_B; OPAQUE_S(L.pa); }
template <int SLOT> DEVI void g1_px(G1Ld& L) { g1_piece<SLOT * G1_AST, 1>(L.px, L.vx, L.lx, L.mx); L.px += SLAB_B; OPAQUE_S(L.px); }
template <int SLOT> DEVI void g1_p2(G1Ld& L) { g1_piece<SLOT * G1_BST, 0>(L.p2, L.v2, L.l2, L.mb); L.p2 += 32 * 6144; OPAQUE_S(L.p2); }
template <int SLOT> DEVI void g1_pg(G1Ld& L) { g1_piece<SLOT * G1_BST + G1_REG, 0>(L.pg, L.vg, L.l2, L.mb); L.pg += 32 * 7168; OPAQUE_S(L.pg); }
template <int SLOT> DEVI void g1_pu(G1Ld& L) { g1_piece<SLOT * G1_BST + 2 * G1_REG, 0>(L.pu, L.vg, L.l2, L.mb); L.pu += 32 * 7168; OPAQUE_S(L.pu); }
template <int GRP, int SLOT> DEVI void g1_issue(G1Ld& L) { g1_pa<SLOT>(L); if (GRP == 0) g1_px<SLOT>(L); g1_p2<SLOT>(L); g1_pg<SLOT>(L); g1_pu<SLOT>(L); }
DEVI void g1_touch(const char*& p, unsigned step, int i, int n) {
    asm volatile("s_cmp_ge_u32 %1, %2\n\ts_cbranch_scc1 .Lsk_%=\n\tglobal_load_dword v191, %0, off\n.Lsk_%=:" :: "v"(p), "s"(i), "s"(n) : "memory", "scc", "v191");
    p += step; asm volatile("" : "+v"(p));
}
struct G1Ptr { unsigned pA, pH, pB01, pB23, pBh01, pBh23; };

template <int REG, int OFF> DEVI void ds_tr(unsigned base) { asm volatile("ds_read_b64_tr_b16 v[%c1:%c2], %0 offset:%c3" :: "v"(base), "i"(REG), "i"(REG + 1), "i"(OFF) : "memory"); }
template <int REG, int OFF> DEVI void ds_128(unsigned base) { asm volatile("ds_read_b128 v[%c1:%c2], %0 offset:%c3" :: "v"(base), "i"(REG), "i"(REG + 3), "i"(OFF) : "memory"); }
template <int RA_, int RB_> DEVI void mfma_fr(f32x4& c) { asm volatile("v_mfma_f32_16x16x32_bf16 %0, v[%c1:%c2], v[%c3:%c4], %0" : "+v"(c) : "i"(RA_), "i"(RA_ + 3), "i"(RB_), "i"(RB_ + 3)); }
template <int GRP, int SL, int n, int h> DEVI void g1_ldb(const G1Ptr& P) {
    constexpr int off = (SL & 1) * G1_BST + (GRP == 0 ? (n < 6 ? n * 32 : (n < 8 ? G1_REG + (n - 1) * 32 : 2 * G1_REG + (n - 3) * 32))
                                                      : (n < 5 ? G1_REG + n * 32 : 2 * G1_REG + (n - 5) * 32));
    ds_tr<FB + 4 * n + 2 * h, off + h * 8 * 224>((SL < 2) ? P.pB01 : P.pB23);
}
template <int SL, int m> DEVI void g1_lda(const G1Ptr& P) { ds_128<FA + 4 * m, SL * G1_AST + (1 + m) * 1024>(P.pA); }
template <int SL> DEVI void g1_ldah(const G1Ptr& P) { ds_128<FAH, SL * G1_AST>(P.pH); }
template <int SL, int h> DEVI void g1_ldbh(const G1Ptr& P) { ds_tr<FBH + 2 * h, (SL & 1) * G1_BST + h * 8 * 224>((SL < 2) ? P.pBh01 : P.pBh23); }

constexpr int G2_NS = 7;
constexpr int g2_slot(int j) { return (j & 1) ? G1_BOFF + (j >> 1) * G1_BST : (j >> 1) * G1_AST; }
struct G2W {
    const char *pwo, *pwh, *pwd;
    unsigned vw0, vw1, vw2, ldsw;
};
template <int ST, int I> DEVI void g2_w(const G2W& W) {
    const char* src = ST < 8 ? W.pwo + ST * 64 * 2048 : ST < 32 ? W.pwh + (((ST - 8) & 7) * 224 + ((ST - 8) >> 3) * 32) * 2048 : W.pwd + (ST - 32) * 896 * 2048;
    asm volatile("s_add_u32 m0, %2, %3\n\ts_nop 0\n\tglobal_load_lds_dwordx4 %0, %1" :: "v"(I == 0 ? W.vw0 : ST < 32 ? W.vw1 : W.vw2), "s"(src), "s"(W.ldsw), "i"(g2_slot(ST % G2_NS) + I * 8192) : "memory", "scc");
}

template <int GRP, int ST, bool TAIL>
DEVI void g1_stage(f32x4 (&acc)[4][10], f32x4& acch, G1Ld& L, const G2W& W, int it, const G1Ptr& P) {
    constexpr int NX = (ST + 1) & 3, IS = (ST + 3) & 3;
    constexpr bool OWN0 = !TAIL || ST == 0, LAST = TAIL && ST == 3;
    constexpr int W0 = ST ? 2 * (ST - 1) : 0;
#define MM(m, n) do { if (GRP == 0 && ((n) == 4 || (n) == 5)) mfma_fr<FA + 4 * (m), FB + 4 * (n)>(acc[m][n]); else mfma_fr<FB + 4 * (n), FA + 4 * (m)>(acc[m][n]); } while (0)
#define RB(n, h) do { if (!LAST) g1_ldb<GRP, NX, n, h>(P); } while (0)
#define RC(n, h) g1_ldb<GRP, ST, n, h>(P)
#define RA(m) do { if (!LAST) g1_lda<NX, m>(P); } while (0)
#define G0(x) do { if (GRP == 0) { x; } } while (0)
    asm volatile("s_waitcnt lgkmcnt(9)" ::: "memory");
    MM(0, 0); RC(8, 0);
    asm volatile("s_waitcnt lgkmcnt(9)" ::: "memory");
    MM(1, 0); RC(8, 1);
    MM(1, 1); RC(9, 0); MM(0, 1); RC(9, 1);
    MM(0, 2); if (OWN0) G0(g1_pa<IS>(L)); else G0((g2_w<W0, 0>(W)));
    MM(1, 2); if (OWN0) G0(g1_px<IS>(L));
    MM(1, 3); if (OWN0) G0(g1_p2<IS>(L)); else G0((g2_w<W0, 1>(W)));
    MM(0, 3); if (OWN0) G0(g1_pg<IS>(L)); else if (ST < 3) G0((g2_w<W0 + 1, 0>(W)));
    MM(0, 4); if (OWN0) G0(g1_pu<IS>(L)); else if (ST < 3) G0((g2_w<W0 + 1, 1>(W)));
    MM(1, 4);
    G0((mfma_fr<FBH, FAH>(acch)));
    asm volatile("s_waitcnt vmcnt(%0) lgkmcnt(0)\n\ts_barrier" :: "i"(OWN0 ? (GRP == 0 ? 12 : 10) : ST == 1 ? (GRP == 0 ? 10 : 9) : ST == 2 ? 8 : 6) : "memory", FRAG_REGS);
    if (GRP == 1 && TAIL) {
        if (ST < 3) { g2_w<2 * (ST % 3), 0>(W); g2_w<2 * (ST % 3), 1>(W); }
        if (ST < 2) { g2_w<2 * (ST % 3) + 1, 0>(W); g2_w<2 * (ST % 3) + 1, 1>(W); }
    }
    if (GRP == 1 && !TAIL) {
        g1_issue<1, ST>(L);
        g1_touch(L.ta, L.ts, it * 4 + ST, L.mt);
        if (ST & 1) g1_touch(L.tb, L.tt, it - 2, L.m7);
        if (ST == 2) {
            const u32x4 ones = {0xffffffffu, 0xffffffffu, 0xffffffffu, 0xffffffffu};
            asm volatile("s_cmp_ge_u32 %3, 4\n\ts_cbranch_scc1 .Lsk_%=\n\tglobal_store_dwordx4 %0, %1, %2 sc1\n\ts_nop 1\n.Lsk_%=:" :: "v"(L.va), "v"(ones), "s"(L.pf), "s"(it - FILL_IT0) : "memory", "scc", FRAG_REGS);
            L.pf += 1024; OPAQUE_S(L.pf);
        }
    }
    MM(2, 0); MM(3, 0); RB(0, 0);
    MM(3, 1); RB(0, 1); MM(2, 1); RB(1, 0);
    MM(2, 2); RB(1, 1); MM(3, 2); RB(2, 0);
    MM(3, 3); RB(2, 1); MM(2, 3); RB(3, 0);
    MM(2, 4); RB(3, 1); MM(3, 4); RB(4, 0);
    MM(0, 5); RB(4, 1); MM(1, 5); if (!LAST) G0(g1_ldah<NX>(P));
    MM(1, 6); if (!LAST) G0((g1_ldbh<NX, 0>(P))); MM(0, 6); if (!LAST) G0((g1_ldbh<NX, 1>(P)));
    MM(0, 7); MM(1, 7);
    MM(1, 8); MM(0, 8);
    MM(0, 9); RA(0); MM(1, 9); RA(1);
    MM(2, 5); MM(3, 5); RB(5, 0);
    MM(3, 6); RB(5, 1); MM(2, 6); RB(6, 0);
    MM(2, 7); RB(6, 1); MM(3, 7); RB(7, 0);
    MM(3, 8); RB(7, 1); MM(2, 8);
    MM(2, 9); RA(2); MM(3, 9); RA(3);
#undef MM
#undef RB
#undef RC
#undef RA
#undef G0
}

DEVI void st16(char* dst, u32x4 v) {
    asm volatile("global_store_dwordx4 %0, %1, off sc1\n\ts_nop 1" :: "v"(dst), "v"(v) : "memory");
}

DEVI unsigned glu2(float g0, float g1, float u0, float u1) {
    const f32x2 t = (f32x2){g0, g1} * -1.4426950408889634f;
    const f32x2 d = (f32x2){__builtin_amdgcn_exp2f(t[0]), __builtin_amdgcn_exp2f(t[1])} + 1.0f;
    const f32x2 h = (f32x2){u0, u1} * (f32x2){__builtin_amdgcn_rcpf(d[0]), __builtin_amdgcn_rcpf(d[1])};
    return pk_bf16(h[0], h[1]);
}

DEVI void glu_store(const f32x4& g0, const f32x4& u0, const f32x4& g1, const f32x4& u1, char* dst) {
    unsigned a0 = glu2(g0[0], g0[1], u0[0], u0[1]), a1 = glu2(g0[2], g0[3], u0[2], u0[3]);
    unsigned b0 = glu2(g1[0], g1[1], u1[0], u1[1]), b1 = glu2(g1[2], g1[3], u1[2], u1[3]);
    auto r0 = __builtin_amdgcn_permlane16_swap(a0, b0, false, false);
    auto r1 = __builtin_amdgcn_permlane16_swap(a1, b1, false, false);
    st16(dst, (u32x4){r0[0], r1[0], r0[1], r1[1]});
    SBAR();
}

constexpr int LN_SLEEP = 2;
constexpr int POLL_SLEEP = 8;
constexpr int G2_RCV = 35;
constexpr int RETRY_SLEEP = 4;
constexpr int G2_BE = 2;
constexpr int G2_WL = G2_NS - G2_BE;
constexpr int G2_XOFF = 131072;
constexpr int G2_XST = 31;
constexpr int G2_F = 32;
constexpr int G2_FY = 8, G2_FZ = 12;
constexpr int G2_PA = 1, G2_PY = 9, G2_PZ = 15;
constexpr unsigned SPIN_CAP = 1u << 17;
static_assert(G2_WL == 5 && G2_RCV > G2_F && G2_RCV <= 35 && g2_slot(5) + 16384 <= G2_XOFF, "");
constexpr int g2_grp(int t) { return t < G2_F ? t : G2_F + 2 * (t - G2_F); }
constexpr int g2_afirst(int s) { return s < 2 ? 2 + 2 * s : s + 4 <= 35 ? g2_grp(s + 4) : 40; }
constexpr int g2_alast(int s) { return g2_afirst(s + 1); }
constexpr int g2_tops(int kg, int s) { return (kg == 1 && (s == 0 || s == G2_FY)) || s == G2_FZ ? 1 : 0; }
constexpr int g2_wops(int s) { return s + G2_WL <= 35 ? 2 : 0; }
constexpr int g2_xops(int kg, int s) { return (s < 8 && (s & 1) ? 1 : 0) + (kg == 0 && (s == G2_PA || s == G2_PY || s == G2_PZ) ? 1 : 0) + (s == G2_F ? 2 : 0) + (s == G2_XST ? 4 : 0) + (s == G2_RCV ? 2 : 0); }
constexpr int g2_wait(int kg, int t) {
    const int need = g2_grp(t + 1) - 1;
    int n = 4, last = need < 2 ? 2 * need + 2 : 0;
    for (int s = 0; s < t; ++s) {
        n += g2_tops(kg, s) + g2_wops(s);
        if (t % G2_BE == 0 && s + G2_WL <= t + G2_BE && g2_wops(s) && n > last) last = n;
        for (int g = g2_afirst(s); g < g2_alast(s); ++g) { n += 2; if (g == need && n > last) last = n; }
        n += g2_xops(kg, s);
        if ((kg == 0 && ((t == 4 && s == G2_PA) || (t == 12 && s == G2_PY) || (t == 20 && s == G2_PZ))) || (t == G2_FY && s == 3) || (t == G2_FZ && s == 7)) last = n > last ? n : last;
    }
    return n - last;
}

struct G2St {
    G2W W;
    const char *pa, *pad;
    unsigned va, vo;
    LAS const char* pB[2][4];
    LAS char* xs;
    char* snd; const char *rcv, *xp;
    f32x4 dg[2][4], du[2][4]; char* dd[2]; unsigned hl, hq[4];
    char* flagA; const char* flags; unsigned foa, foz, tag; u32x2 hf; bool poller;
    u32x4 A[40][2];
    bf16x4 B[8][2];
    f32x4 acc[2][8];
    u32x2 xres[4]; u32x4 rc[2];
};
template <int G> DEVI const char* g2_asrc(const G2St& T) { return G < G2_F ? T.pa + (size_t)G * 2 * SLAB_B : T.pad + (size_t)(G - G2_F) * 2 * SLAB_B; }
template <int KG, int G, int I> DEVI void g2_a(G2St& T) {
    asm volatile("global_load_dwordx4 %0, %1, %2 offset:%3 sc1" : "=v"(T.A[G][I]) : "v"(T.va), "s"(g2_asrc<G>(T)), "i"((KG ^ I) * 1024) : "memory");
}
template <int KG, int G, int E> DEVI void g2_again(G2St& T) {
    asm volatile("global_load_dwordx4 %0, %2, %3 offset:%4 sc1 ; spin\n\tglobal_load_dwordx4 %1, %2, %3 offset:%5 sc1 ; spin" : "+v"(T.A[G][0]), "+v"(T.A[G][1]) : "v"(T.va), "s"(g2_asrc<G>(T)), "i"(KG * 1024), "i"((KG ^ 1) * 1024) : "memory");
    if constexpr (G + 1 < E) g2_again<KG, G + 1, E>(T);
}
template <int ST> DEVI bf16x4 g2_ldb(G2St& T, int n, int h) {
    constexpr int SLOT = ST % G2_NS;
    return __builtin_amdgcn_ds_read_tr16_b64_v4bf16((LAS bf16x4*)(T.pB[SLOT & 1][n & 3] + (g2_slot(SLOT) - g2_slot(SLOT & 1)) + (n >> 2) * 8192 + h * 512));
}
DEVI bf16x8 cat(bf16x4 lo, bf16x4 hi) { return __builtin_shufflevector(lo, hi, 0, 1, 2, 3, 4, 5, 6, 7); }
template <int OFF> DEVI void flag_set(char* p, unsigned tag) { const unsigned zero = 0u; asm volatile("s_mov_b64 exec, 1\n\tglobal_store_dword %0, %1, %2 offset:%3 sc1\n\ts_mov_b64 exec, -1" :: "v"(zero), "v"(tag), "s"(p), "i"(OFF) : "memory"); }
template <int SLEEP> DEVI void flags_wait(u32x2 f, const char* base, unsigned voff, unsigned tag) {
#pragma unroll 1
    for (unsigned spin = 0; __any(f[0] != tag || f[1] != tag) && spin < SPIN_CAP; ++spin) {
        if (spin) __builtin_amdgcn_s_sleep(SLEEP);
        asm volatile("global_load_dwordx2 %0, %1, %2 sc1\n\ts_waitcnt vmcnt(0)" : "=&v"(f) : "v"(voff), "s"(base) : "memory");
    }
}
DEVI unsigned umax4(u32x4 v) { const unsigned a = v[0] > v[1] ? v[0] : v[1], b = v[2] > v[3] ? v[2] : v[3]; return a > b ? a : b; }
DEVI bool g2_missing(const u32x4 (&v)[2]) { const unsigned a = umax4(v[0]), b = umax4(v[1]); return __any((a > b ? a : b) == 0xffffffffu); }
DEVI u32x4 never_ones(u32x4 w) {
#pragma unroll
    for (int e = 0; e < 4; ++e) w[e] = w[e] < 0xfffffffeu ? w[e] : 0xfffffffeu;
    return w;
}

template <int KG, int t>
DEVI void g2_stage(G2St& T) {
    constexpr bool TB = t >= G2_F, ISW = g2_wops(t) != 0, RL = t < 35, CHK = KG == 0 && (t == 4 || t == 12 || t == 20);
    constexpr int G0 = g2_grp(t), G1 = TB ? G0 + 1 : G0, NA = g2_alast(t) - g2_afirst(t), GN = NA ? g2_afirst(t) : 0, GN1 = NA == 2 ? GN + 1 : GN;
    u32x4 (&Aa)[2] = T.A[G0]; u32x4 (&Ab)[2] = T.A[G1];
    if (TB) asm volatile("s_waitcnt vmcnt(%4)" : "+v"(Aa[0]), "+v"(Aa[1]), "+v"(Ab[0]), "+v"(Ab[1]) : "i"(g2_wait(KG, t)) : "memory");
    else if (CHK) asm volatile("s_waitcnt vmcnt(%3)" : "+v"(Aa[0]), "+v"(Aa[1]), "+v"(T.hf) : "i"(g2_wait(KG, t)) : "memory");
    else asm volatile("s_waitcnt vmcnt(%2)" : "+v"(Aa[0]), "+v"(Aa[1]) : "i"(g2_wait(KG, t)) : "memory");
    if (KG == 1 && t == 0) flag_set<0>(T.flagA, T.tag);
    if (KG == 1 && t == G2_FY) flag_set<128>(T.flagA, T.tag);
    if (t == G2_FZ) flag_set<256 + KG * 128>(T.flagA, T.tag);
    if (t < 8 && __builtin_expect(g2_missing(Aa), 0)) {
#pragma unroll 1
        for (unsigned spin = 0; spin < SPIN_CAP; ++spin) {
            g2_again<KG, t < 8 ? t : 0, (g2_afirst(t) < 8 ? g2_afirst(t) : 8)>(T);
            asm volatile("s_waitcnt vmcnt(0) ; spin" : "+v"(Aa[0]), "+v"(Aa[1]) :: "memory");
            if (!g2_missing(Aa)) break;
            __builtin_amdgcn_s_sleep(RETRY_SLEEP);
        }
    }
    if (CHK && T.poller) flags_wait<POLL_SLEEP>(T.hf, t == 12 ? T.flags + 128 : T.flags, t == 20 ? T.foz : T.foa, T.tag);
    if (t == G2_F) WAIT_LGKM0();
    if (t % G2_BE == 0) { MEMFENCE(); SBAR(); BAR(); MEMFENCE(); SBAR(); }
    f32x4 ho[4];
    if (t == G2_F) {
#pragma unroll
        for (int j = 0; j < 4; ++j) ho[j] = *(LAS const f32x4*)(T.xs + (KG ^ 1) * 16384 + j * 1024);
        SBAR();
    }
#define RL2(n, h) do { if (RL && (n) >= 0) { T.B[(n) >= 0 ? (n) : 0][h] = g2_ldb<t + 1>(T, (n) >= 0 ? (n) : 0, h); SBAR(); } } while (0)
#define P2(n) do { constexpr int c_ = TB ? ((n) & 3) : (n); u32x4 (&A_)[2] = (n) < 4 ? Aa : Ab; const bf16x8 b_ = cat(T.B[n][0], T.B[n][1]); \
                   if (t == 0) MFMA_NEW(T.acc[(n) & 1][c_], b_, A_[(n) & 1]); else MFMA_ACC(T.acc[(n) & 1][c_], b_, A_[(n) & 1]); SBAR(); RL2((n) - 1, 1); D((n) * 2); \
                   if (t == 0) MFMA_NEW(T.acc[~(n) & 1][c_], b_, A_[~(n) & 1]); else MFMA_ACC(T.acc[~(n) & 1][c_], b_, A_[~(n) & 1]); SBAR(); RL2(n, 0); D((n) * 2 + 1); } while (0)
#define VW(i) do { if (ISW) { g2_w<ISW ? t + G2_WL : 0, i>(T.W); SBAR(); } } while (0)
#define VA(i) do { if (NA == 2) { g2_a<KG, (i) < 2 ? GN : GN1, (i) & 1>(T); SBAR(); } else if (NA && ((i) & 1)) { g2_a<KG, GN, (i) / 2>(T); SBAR(); } } while (0)
    float dg[4], du[4];
    if (t < 8) {
#pragma unroll
        for (int e = 0; e < 4; ++e) { dg[e] = T.dg[(t >> 2) & 1][t & 3][e]; du[e] = T.du[(t >> 2) & 1][t & 3][e]; }
    }
#define D(i) do { if (t < 8) { constexpr int k_ = ((i) & 1) * 2; \
        if ((i) < 2) asm volatile("v_mul_f32 %0, 0xbfb8aa3b, %0\n\tv_mul_f32 %1, 0xbfb8aa3b, %1" : "+v"(dg[k_]), "+v"(dg[k_ + 1])); \
        else if ((i) < 6) asm volatile("v_exp_f32 %0, %0" : "+v"(dg[((i) - 2) & 3])); \
        else if ((i) < 8) asm volatile("v_add_f32 %0, 1.0, %0\n\tv_add_f32 %1, 1.0, %1" : "+v"(dg[k_]), "+v"(dg[k_ + 1])); \
        else if ((i) < 12) asm volatile("v_rcp_f32 %0, %0" : "+v"(dg[(i) & 3])); \
        else if ((i) < 14) asm volatile("v_mul_f32 %0, %2, %0\n\tv_mul_f32 %1, %3, %1" : "+v"(dg[k_]), "+v"(dg[k_ + 1]) : "v"(du[k_]), "v"(du[k_ + 1])); \
        else if ((i) == 14) asm volatile("v_cvt_pk_bf16_f32 %0, %2, %3\n\tv_cvt_pk_bf16_f32 %1, %4, %5" : "=&v"(T.hq[(t & 1) * 2]), "=&v"(T.hq[(t & 1) * 2 + 1]) : "v"(dg[0]), "v"(dg[1]), "v"(dg[2]), "v"(dg[3])); \
        SBAR(); } } while (0)
#define SEND(j) do { if (t == G2_F && ((j) & 1)) { const f32x4 a_ = T.acc[0][4 + (j) - 1] + ho[(j) ? (j) - 1 : 0], b_ = T.acc[0][4 + (j)] + ho[j]; \
        const u32x4 w = never_ones((u32x4){pk_bf16(a_[0], a_[1]), pk_bf16(a_[2], a_[3]), pk_bf16(b_[0], b_[1]), pk_bf16(b_[2], b_[3])}); \
        asm volatile("global_store_dwordx4 %0, %1, %2 offset:%3 sc1\n\ts_nop 1" :: "v"(T.va), "v"(w), "s"(T.snd), "i"((j) / 2 * 1024) : "memory"); SBAR(); } } while (0)
    static_assert(g2_wops(G2_F) == 0 && g2_alast(G2_F) == g2_afirst(G2_F), "the sends are the only memory operations of their stage");
    if (KG == 1) { VW(0); VW(1); VA(0); VA(1); VA(2); VA(3); SEND(0); SEND(1); SEND(2); SEND(3); P2(0); P2(1); P2(2); P2(3); P2(4); P2(5); P2(6); P2(7); }
    else { P2(0); SEND(0); P2(1); SEND(1); P2(2); SEND(2); P2(3); SEND(3); VW(0); P2(4); VW(1); P2(5); VA(0); VA(1); P2(6); VA(2); VA(3); P2(7); }
    RL2(7, 1);
#undef SEND
#undef RL2
#undef VW
#undef VA
#undef D
#undef P2
    if (t < 8 && (t & 1)) {
        auto r0 = __builtin_amdgcn_permlane16_swap(T.hq[0], T.hq[2], false, false);
        auto r1 = __builtin_amdgcn_permlane16_swap(T.hq[1], T.hq[3], false, false);
        const u32x4 w = {r0[0], r1[0], r0[1], r1[1]};
        asm volatile("global_store_dwordx4 %0, %1, %2 offset:%3 sc1\n\ts_nop 1" :: "v"(T.hl), "v"(w), "s"(T.dd[(t >> 2) & 1]), "i"((t & 2) * 1024) : "memory");
        SBAR();
    }
    if (KG == 0 && (t == G2_PA || t == G2_PY || t == G2_PZ)) {
        asm volatile("global_load_dwordx2 %0, %1, %2 offset:%3 sc1" : "=v"(T.hf) : "v"(t == G2_PZ ? T.foz : T.foa), "s"(T.flags), "i"(t == G2_PY ? 128 : 0) : "memory"); SBAR();
    }
    if (t == G2_F - 1) {
        MFMA_DRAIN(); SBAR();
#pragma unroll
        for (int j = 0; j < 4; ++j) *(LAS f32x4*)(T.xs + KG * 16384 + j * 1024) = T.acc[1][4 + j];
        SBAR();
    }
    if (t == G2_XST) {
        asm volatile("global_load_dwordx2 %0, %4, %5\n\tglobal_load_dwordx2 %1, %4, %5 offset:32\n\tglobal_load_dwordx2 %2, %4, %5 offset:64\n\tglobal_load_dwordx2 %3, %4, %5 offset:96"
                     : "=&v"(T.xres[0]), "=&v"(T.xres[1]), "=&v"(T.xres[2]), "=&v"(T.xres[3]) : "v"(T.vo), "s"(T.xp) : "memory");
        SBAR();
    }
    if (t == G2_RCV) {
        asm volatile("global_load_dwordx4 %0, %2, %3 sc1\n\tglobal_load_dwordx4 %1, %2, %3 offset:1024 sc1" : "=&v"(T.rc[0]), "=&v"(T.rc[1]) : "v"(T.va), "s"(T.rcv) : "memory");
        SBAR();
    }
}
template <int KG, int t, int E> DEVI void g2_run(G2St& T) { g2_stage<KG, t>(T); if constexpr (t + 1 < E) g2_run<KG, t + 1, E>(T); }

DEVI unsigned bsel(unsigned m, unsigned a, unsigned b) { return (m & a) | (~m & b); }
typedef short s16x4 __attribute__((ext_vector_type(4)));
#define MFMA16K16(a, b, c) __builtin_amdgcn_mfma_f32_16x16x16bf16_1k(__builtin_bit_cast(s16x4, a), __builtin_bit_cast(s16x4, b), (c), 0, 0, 0)
template <bool MAX> DEVI float rows_reduce(const float (&v)[4]) {
    auto a = __builtin_amdgcn_permlane16_swap(__float_as_uint(v[0]), __float_as_uint(v[1]), false, false);
    auto b = __builtin_amdgcn_permlane16_swap(__float_as_uint(v[2]), __float_as_uint(v[3]), false, false);
    const float t = MAX ? fmaxf(__uint_as_float(a[0]), __uint_as_float(a[1])) : __uint_as_float(a[0]) + __uint_as_float(a[1]);
    const float u = MAX ? fmaxf(__uint_as_float(b[0]), __uint_as_float(b[1])) : __uint_as_float(b[0]) + __uint_as_float(b[1]);
    auto c = __builtin_amdgcn_permlane32_swap(__float_as_uint(t), __float_as_uint(u), false, false);
    return MAX ? fmaxf(__uint_as_float(c[0]), __uint_as_float(c[1])) : __uint_as_float(c[0]) + __uint_as_float(c[1]);
}
DEVI void rows_spread(float w, float (&v)[4]) {
    auto c = __builtin_amdgcn_permlane32_swap(__float_as_uint(w), __float_as_uint(w), false, false);
    auto a = __builtin_amdgcn_permlane16_swap(c[0], c[0], false, false);
    auto b = __builtin_amdgcn_permlane16_swap(c[1], c[1], false, false);
    v[0] = __uint_as_float(a[0]); v[1] = __uint_as_float(a[1]); v[2] = __uint_as_float(b[0]); v[3] = __uint_as_float(b[1]);
}

template <int GRP>
DEVI void g1_body(G1Ld& L, G2St& T, LAS char* lds, char* ws, int rt, int ct, int wm, int kh) {
    G1Ptr P;
    {
        const int lane = fresh_lane(), g = lane >> 4, q = (lane & 15) >> 2, p = lane & 3;
        const unsigned ldsb = (unsigned)(size_t)lds;
        P.pA = ldsb + wm * 4096 + lane * 16;
        P.pH = ldsb + lane * 16;
        const int brow = (16 * (g >> 1) + 4 * (g & 1) + q) * 224 + p * 8;
        P.pB01 = ldsb + G1_BOFF + brow;
        P.pB23 = P.pB01 + 2 * G1_BST;
        P.pBh01 = P.pB01 + (2 + wm) * 32;
        P.pBh23 = P.pB23 + (2 + wm) * 32;
    }
    f32x4 acc[4][10], acch = {0.f, 0.f, 0.f, 0.f};
#pragma unroll
    for (int m = 0; m < 4; ++m)
#pragma unroll
        for (int n = 0; n < 10; ++n) acc[m][n] = (f32x4){0.f, 0.f, 0.f, 0.f};

#pragma unroll
    for (int m = 0; m < 4; ++m)
        asm volatile("" : "+v"(acc[m][0]), "+v"(acc[m][1]), "+v"(acc[m][2]), "+v"(acc[m][3]), "+v"(acc[m][4]), "+v"(acc[m][5]), "+v"(acc[m][6]), "+v"(acc[m][7]), "+v"(acc[m][8]), "+v"(acc[m][9]), "+v"(acch));
    if (GRP == 1 && wm == 3)
        flags_wait<LN_SLEEP>((u32x2){~T.tag, 0u}, ws + WS_LF + (rt * 32 + ct) * 256, L.va < 256u || rt == 0 ? (L.va >> 1) & 127u : 128u, T.tag);
    asm volatile("s_waitcnt vmcnt(3)\n\ts_barrier" ::: "memory", FRAG_REGS);
    g1_pa<0>(L); if (GRP == 0) g1_px<0>(L);
    g1_pa<1>(L); if (GRP == 0) g1_px<1>(L);
    g1_ldb<GRP, 0, 0, 0>(P); g1_ldb<GRP, 0, 0, 1>(P); g1_ldb<GRP, 0, 1, 0>(P); g1_ldb<GRP, 0, 1, 1>(P); g1_ldb<GRP, 0, 2, 0>(P); g1_ldb<GRP, 0, 2, 1>(P);
    g1_ldb<GRP, 0, 3, 0>(P); g1_ldb<GRP, 0, 3, 1>(P); g1_ldb<GRP, 0, 4, 0>(P); g1_ldb<GRP, 0, 4, 1>(P);
    if (GRP == 0) { g1_ldbh<0, 0>(P); g1_ldbh<0, 1>(P); }
    g1_ldb<GRP, 0, 5, 0>(P); g1_ldb<GRP, 0, 5, 1>(P); g1_ldb<GRP, 0, 6, 0>(P); g1_ldb<GRP, 0, 6, 1>(P); g1_ldb<GRP, 0, 7, 0>(P); g1_ldb<GRP, 0, 7, 1>(P);
    g1_issue<GRP, 2>(L);
    asm volatile("s_cmp_eq_u32 %1, 0\n\ts_cbranch_scc1 .Lsk_%=\n\ts_waitcnt vmcnt(%0)\n.Lsk_%=:\n\ts_barrier" :: "i"(GRP == 0 ? 9 : 7), "s"(L.ma) : "memory", "scc", FRAG_REGS);
    g1_lda<0, 0>(P); g1_lda<0, 1>(P); g1_lda<0, 2>(P); g1_lda<0, 3>(P);
    if (GRP == 0) g1_ldah<0>(P);
    if (GRP == 1) g1_issue<1, 3>(L);
    asm volatile("s_waitcnt lgkmcnt(0)" ::: "memory");
    G2W& W = T.W;
#pragma unroll 1
    for (int it = 0; it < 7; ++it) {
        g1_stage<GRP, 0, false>(acc, acch, L, W, it, P);
        g1_stage<GRP, 1, false>(acc, acch, L, W, it, P);
        g1_stage<GRP, 2, false>(acc, acch, L, W, it, P);
        g1_stage<GRP, 3, false>(acc, acch, L, W, it, P);
    }
    {
        const unsigned wid = GRP * 4 + wm, r = L.va >> 7, c = ((L.va >> 4) & 7) ^ ((r & 2) | ((wid & 1) << 2));
        W.vw0 = (8 * wid + r) * 2048 + c * 16 + kh * 128;
        W.vw1 = W.vw0 ^ 128;
        asm volatile("" : "+v"(W.vw0), "+v"(W.vw1) :: FRAG_REGS);
    }
    g1_stage<GRP, 0, true>(acc, acch, L, W, 7, P);
    g1_stage<GRP, 1, true>(acc, acch, L, W, 7, P);
    g1_stage<GRP, 2, true>(acc, acch, L, W, 7, P);
    g1_stage<GRP, 3, true>(acc, acch, L, W, 7, P);
    asm volatile("s_nop 15" ::: "memory", FRAG_REGS);
    SBAR();
    const int lane = fresh_lane(), g = lane >> 4, i16 = lane & 15;
    char* ho = ws + WS_HO;
    const int mb0 = 16 * rt + 4 * wm;
    const unsigned hl = (unsigned)((g & 1) * 1024 + (g >> 1) * 256 + i16 * 16);
    W.vw2 = W.vw0 + 448 * 2048;
    char* hs = ho + (size_t)(64 * (ct >> 4) + (ct & 15)) * SLAB_B + (size_t)mb0 * 1024;
#define GLU1(n, m) glu_store(acc[m][n], acc[m][5 + (n)], acc[(m) + 1][n], acc[(m) + 1][5 + (n)], hs + (size_t)(16 * (1 + (n) / 2)) * SLAB_B + ((n) & 1) * 512 + (m) * 1024 + hl)
    if (GRP == 1) {
        SBAR(); BAR(); MEMFENCE(); SBAR();
        GLU1(0, 0); GLU1(0, 2); GLU1(1, 0); GLU1(1, 2); GLU1(2, 0); GLU1(2, 2);
#pragma unroll
        for (int m = 0; m < 4; ++m) { T.dg[0][m] = acc[m][3]; T.dg[1][m] = acc[m][4]; T.du[0][m] = acc[m][8]; T.du[1][m] = acc[m][9]; }
        T.dd[0] = hs + (size_t)32 * SLAB_B + 512; T.dd[1] = hs + (size_t)48 * SLAB_B;
    } else {
        u32x4 Qf[4], Kf[4]; u32x2 Vh[4][2];
#pragma unroll
        for (int m = 0; m < 4; ++m) {
            Qf[m] = (u32x4){pk_bf16(acc[m][0][0], acc[m][0][1]), pk_bf16(acc[m][0][2], acc[m][0][3]), pk_bf16(acc[m][1][0], acc[m][1][1]), pk_bf16(acc[m][1][2], acc[m][1][3])};
            Kf[m] = (u32x4){pk_bf16(acc[m][2][0], acc[m][2][1]), pk_bf16(acc[m][2][2], acc[m][2][3]), pk_bf16(acc[m][3][0], acc[m][3][1]), pk_bf16(acc[m][3][2], acc[m][3][3])};
#pragma unroll
            for (int b = 0; b < 2; ++b) Vh[m][b] = (u32x2){pk_bf16(acc[m][4 + b][0], acc[m][4 + b][1]), pk_bf16(acc[m][4 + b][2], acc[m][4 + b][3])};
        }
        LAS char* xs = lds + G1_XOFF;
        if (wm < 3) {
            *(LAS u32x4*)(xs + (wm + 1) * 2048 + lane * 16) = Kf[3];
            *(LAS u32x4*)(xs + (wm + 1) * 2048 + 1024 + lane * 16) = (u32x4){Vh[3][0][0], Vh[3][0][1], Vh[3][1][0], Vh[3][1][1]};
        }
        {
            const unsigned h0 = pk_bf16(acch[0], acch[1]), h1 = pk_bf16(acch[2], acch[3]);
            if (wm < 2) {
                *(LAS u32x2*)(xs + lane * 16 + wm * 8) = (u32x2){h0, h1};
            } else {
                LAS char* t = xs + 1024 + ((i16 >> 2) * 16 + 4 * g) * 16 + (wm - 2) * 8 + (i16 & 3) * 2;
                *(LAS u16*)(t) = (u16)h0; *(LAS u16*)(t + 16) = (u16)(h0 >> 16); *(LAS u16*)(t + 32) = (u16)h1; *(LAS u16*)(t + 48) = (u16)(h1 >> 16);
            }
        }
        asm volatile("s_waitcnt lgkmcnt(0)" ::: "memory");
        SBAR(); BAR(); MEMFENCE(); SBAR();
        const int renew = blockIdx.x * 4 + wm;
        const unsigned efo = (unsigned)((lane >> 3) * 8192 + (lane & 7) * 16);
        u32x4 ef = {efo, efo, efo, efo};
        asm volatile("s_cmp_lg_u32 %3, 1\n\ts_cbranch_scc1 .Lsk_%=\n\tglobal_load_dwordx4 %0, %1, %2 sc1\n.Lsk_%=:" : "+v"(ef) : "v"(efo), "s"(ws + WS_LF), "s"(renew) : "memory", "scc");

        const u32x4 Kp = *(LAS const u32x4*)(xs + wm * 2048 + lane * 16);
        const u32x4 Vp = *(LAS const u32x4*)(xs + wm * 2048 + 1024 + lane * 16);
        const int dc = i16 - 4 * g - (i16 & 1);
        unsigned modd = (i16 & 1) ? ~0u : 0u, mc0 = dc >= 0 ? ~0u : 0u, mc1 = dc >= 2 ? ~0u : 0u;
        asm volatile("" : "+v"(modd), "+v"(mc0), "+v"(mc1));
        const unsigned half = (modd & 0xffff0000u) | (~modd & 0x0000ffffu);
        const unsigned pp0 = half & ~mc0, pp1 = half & ~mc1, pc0 = half & mc0, pc1 = half & mc1;
        const float sc = 1.4426950408889634f;
        const f32x4 z = {0.f, 0.f, 0.f, 0.f};
        float e0[4], e1[4], red[4];
#pragma unroll
        for (int m = 0; m < 4; ++m) {
            u32x4 s0 = __builtin_bit_cast(u32x4, MFMA16(as_frag(m ? Kf[m ? m - 1 : 0] : Kp), as_frag(Qf[m]), z));
            const u32x4 s1 = __builtin_bit_cast(u32x4, MFMA16(as_frag(Kf[m]), as_frag(Qf[m]), z));
            if (m == 0 && rt == 0 && wm == 0) { const unsigned c = __float_as_uint(-1e9f / 0.17677669529663687f); s0 = (u32x4){c, c, c, c}; }
            const unsigned d2 = pk_bf16(__uint_as_float(bsel(modd, bsel(mc0, s1[1], s0[1]), bsel(mc0, s1[0], s0[0]))), __uint_as_float(bsel(modd, bsel(mc1, s1[3], s0[3]), bsel(mc1, s1[2], s0[2]))));
            const unsigned l2 = pk_bf16(bf_lo(d2) * 0.17677669529663687f, bf_hi(d2) * 0.17677669529663687f);
            e0[m] = bf_lo(l2); e1[m] = bf_hi(l2);
            red[m] = fmaxf(e0[m], e1[m]);
        }
        {
            rows_spread(rows_reduce<true>(red), red);
        }
#pragma unroll
        for (int m = 0; m < 4; ++m) {
            e0[m] = __builtin_amdgcn_exp2f((e0[m] - red[m]) * sc); e1[m] = __builtin_amdgcn_exp2f((e1[m] - red[m]) * sc);
            red[m] = e0[m] + e1[m];
        }
        {
            float w = __builtin_amdgcn_rcpf(rows_reduce<false>(red));
            rows_spread(w, red);
        }
        char* od = hs + (((g & 1) << 1) | (g >> 1)) * 256 + i16 * 16;
#pragma unroll
        for (int m = 0; m < 4; ++m) {
            const float p0 = e0[m] * red[m], p1 = e1[m] * red[m];
            const unsigned w0 = pk_bf16(p0, p0), w1 = pk_bf16(p1, p1);
            const u32x2 Pp = {w0 & pp0, w1 & pp1}, Pc = {w0 & pc0, w1 & pc1};
            const u32x2 Vp0 = m ? Vh[m ? m - 1 : 0][0] : (u32x2){Vp[0], Vp[1]}, Vp1 = m ? Vh[m ? m - 1 : 0][1] : (u32x2){Vp[2], Vp[3]};
            const f32x4 o0 = MFMA16K16(Vh[m][0], Pc, MFMA16K16(Vp0, Pp, z));
            const f32x4 o1 = MFMA16K16(Vh[m][1], Pc, MFMA16K16(Vp1, Pp, z));
            unsigned x0 = pk_bf16(o0[0], o0[1]), x1 = pk_bf16(o0[2], o0[3]), y0 = pk_bf16(o1[0], o1[1]), y1 = pk_bf16(o1[2], o1[3]);
            auto r0 = __builtin_amdgcn_permlane16_swap(x0, y0, false, false);
            auto r1 = __builtin_amdgcn_permlane16_swap(x1, y1, false, false);
            st16(od + m * 1024, never_ones((u32x4){r0[0], r1[0], r0[1], r1[1]}));
        }
        SBAR();

        asm volatile("s_waitcnt vmcnt(4)" : "+v"(ef) :: "memory");
#pragma unroll
        for (int m = 0; m < 4; ++m) { T.dg[0][m] = acc[m][6]; T.dg[1][m] = acc[m][7]; T.du[0][m] = acc[m][8]; T.du[1][m] = acc[m][9]; }
        T.dd[0] = hs + (size_t)48 * SLAB_B + 512; T.dd[1] = ho + (size_t)(128 + (ct >> 1)) * SLAB_B + (ct & 1) * 512 + (size_t)mb0 * 1024;
        if (renew == 1) {
#pragma unroll 1
            for (unsigned spin = 0; __any(ef[0] != T.tag || ef[1] != T.tag || ef[2] != T.tag || ef[3] != T.tag) && spin < SPIN_CAP; ++spin) {
                __builtin_amdgcn_s_sleep(POLL_SLEEP);
                asm volatile("global_load_dwordx4 %0, %1, %2 sc1\n\ts_waitcnt vmcnt(0)" : "=&v"(ef) : "v"(efo), "s"(ws + WS_LF) : "memory");
            }
            flag_set<0>(ws + WS_EP, T.tag);
        }
    }
#undef GLU1
}

DEVI float sum_x8(float v) {
    v += __uint_as_float(__builtin_amdgcn_update_dpp(0u, __float_as_uint(v), 0x128  , 0xf, 0xf, false));
    { auto t = __builtin_amdgcn_permlane16_swap(__float_as_uint(v), __float_as_uint(v), false, false); v = __uint_as_float(t[0]) + __uint_as_float(t[1]); }
    { auto t = __builtin_amdgcn_permlane32_swap(__float_as_uint(v), __float_as_uint(v), false, false); v = __uint_as_float(t[0]) + __uint_as_float(t[1]); }
    return v;
}

template <int KG>
DEVI void g2_body(G2St& T, LAS char* lds, char* ws, const u16* x, u16* out, int rt, int ct, int wm) {
    constexpr int kg = KG;
    const int bid = blockIdx.x, idx = bid >> 3, wid = KG * 4 + wm;
    const int kh = (bid >> 2) & 1, side = kh ^ 1, rt2 = 2 * rt + ((bid >> 1) & 1), ct2 = 4 * (bid & 1) + (idx & 3);
    const char* flq = ws + WS_FL + (rt * 4 + (bid & 2)) * 512;
    const int lane2 = fresh_lane(), g = lane2 >> 4, i16 = lane2 & 15, q = i16 >> 2, p = lane2 & 3;
    {
        const char* a0 = ws + WS_HO + (size_t)kg * SLAB_B + (size_t)(8 * rt2 + 2 * wm) * 1024;
        T.pa = a0 + (size_t)(64 * side) * SLAB_B;
        T.pad = a0 + (size_t)128 * SLAB_B;
        T.va = lane2 * 16;
    }
    if (KG == 1) { g2_a<KG, 0, 0>(T); g2_a<KG, 0, 1>(T); g2_a<KG, 1, 0>(T); g2_a<KG, 1, 1>(T); SBAR(); }
    T.flagA = ws + WS_FL + (rt * 4 + wm) * 512 + ct * 4;
    T.hl = (unsigned)((g & 1) * 1024 + (g >> 1) * 256 + i16 * 16);
    T.poller = wid == 0;
    T.flags = T.poller ? flq : ws + WS_XB + (size_t)bid * 16384;
    T.foa = (unsigned)((lane2 >> 5) * 512 + side * 64 + (lane2 & 7) * 8);
    T.foz = (unsigned)((lane2 >> 5) * 512 + 256 + (lane2 & 31) * 8);
#pragma unroll
    for (int n = 0; n < 4; ++n) {
        T.pB[0][n] = lds + kg * 4096 + (8 * g + q) * 128 + (((2 * n + (p >> 1)) ^ ((q & 2) | ((g & 1) << 2))) * 16) + (p & 1) * 8;
        T.pB[1][n] = T.pB[0][n] + G1_BOFF;
        asm volatile("" : "+v"(T.pB[0][n]), "+v"(T.pB[1][n]));
    }
#pragma unroll
    for (int n = 0; n < 8; ++n) { T.B[n][0] = g2_ldb<0>(T, n, 0); T.B[n][1] = g2_ldb<0>(T, n, 1); }
    SBAR();
    MEMFENCE(); SBAR(); BAR(); MEMFENCE(); SBAR();
    if (KG == 0) { g2_a<KG, 0, 0>(T); g2_a<KG, 0, 1>(T); g2_a<KG, 1, 0>(T); g2_a<KG, 1, 1>(T); SBAR(); }
    const size_t otile = (size_t)(128 * rt2 + 32 * wm + 16 * kg) * 2048 + (128 * ct2 + 64 * kh) * 2;
    T.vo = (unsigned)(i16 * 2048 + g * 8);
    T.xs = lds + G2_XOFF + wm * 4096 + lane2 * 16;
    T.snd = ws + WS_XB + (size_t)bid * 16384 + wid * 2048;
    T.rcv = ws + WS_XB + (size_t)(bid ^ 4) * 16384 + wid * 2048;
    T.xp = (const char*)x + otile;
    char* op = (char*)out + otile;
    asm volatile("" : "+v"(T.vo), "+v"(T.xs), "+s"(T.snd), "+s"(T.rcv), "+s"(T.xp), "+s"(op));
    SBAR();
    g2_run<KG, 0, 8>(T);
    g2_run<KG, 8, 24>(T);
    g2_run<KG, 24, 36>(T);
    MFMA_DRAIN(); SBAR();
#pragma unroll
    for (int j = 0; j < 4; ++j) *(LAS f32x4*)(T.xs + KG * 16384 + j * 1024) = T.acc[1][j];
    WAIT_LGKM0();
    MEMFENCE(); SBAR(); BAR(); MEMFENCE(); SBAR();
    f32x4 other[4];
#pragma unroll
    for (int j = 0; j < 4; ++j) other[j] = *(LAS const f32x4*)(T.xs + (KG ^ 1) * 16384 + j * 1024);
    asm volatile("s_waitcnt vmcnt(0)" : "+v"(T.xres[0]), "+v"(T.xres[1]), "+v"(T.xres[2]), "+v"(T.xres[3]), "+v"(T.rc[0]), "+v"(T.rc[1]) :: "memory");
#pragma unroll 1
    for (unsigned spin = 0; spin < SPIN_CAP; ++spin) {
        if (!g2_missing(T.rc)) break;
        __builtin_amdgcn_s_sleep(4);
        asm volatile("global_load_dwordx4 %0, %2, %3 sc1\n\tglobal_load_dwordx4 %1, %2, %3 offset:1024 sc1\n\ts_waitcnt vmcnt(0)" : "=&v"(T.rc[0]), "=&v"(T.rc[1]) : "v"(T.va), "s"(T.rcv) : "memory");
    }
    unsigned pk[4][2];
#pragma unroll
    for (int j = 0; j < 4; ++j) {
        const f32x4 xv = {bf_lo(T.xres[j][0]), bf_hi(T.xres[j][0]), bf_lo(T.xres[j][1]), bf_hi(T.xres[j][1])};
        const f32x4 s4 = ((T.acc[0][j] + other[j]) + (f32x4){bf_lo(T.rc[j >> 1][(j & 1) * 2]), bf_hi(T.rc[j >> 1][(j & 1) * 2]), bf_lo(T.rc[j >> 1][(j & 1) * 2 + 1]), bf_hi(T.rc[j >> 1][(j & 1) * 2 + 1])}) + xv;
        pk[j][0] = pk_bf16(s4[0], s4[1]); pk[j][1] = pk_bf16(s4[2], s4[3]);
    }
    const unsigned vs = (T.vo & ~31u) + ((T.vo & 8u) << 2) + (T.vo & 16u);
#pragma unroll
    for (int j = 0; j < 4; j += 2) {
        auto r0 = __builtin_amdgcn_permlane16_swap(pk[j][0], pk[j + 1][0], false, false);
        auto r1 = __builtin_amdgcn_permlane16_swap(pk[j][1], pk[j + 1][1], false, false);
        const u32x4 o = {r0[0], r1[0], r0[1], r1[1]};
        asm volatile("global_store_dwordx4 %0, %1, %2 offset:%3\n\ts_nop 1" :: "v"(vs), "v"(o), "s"(op), "i"(j * 32) : "memory");
    }
}

__global__ __launch_bounds__(512, 2) void k_main(const char* __restrict__ wqkv, const char* __restrict__ wg, const char* __restrict__ wu, const char* __restrict__ wo, const char* __restrict__ wd, char* __restrict__ ws,
                                                const u16* __restrict__ x, u16* __restrict__ out) {
    extern __shared__ __attribute__((aligned(16))) char smem[];
    LAS char* lds = (LAS char*)smem;
    G1Ld L;
    G2St T;
    const int lane0 = threadIdx.x & 63, wid0 = __builtin_amdgcn_readfirstlane(threadIdx.x >> 6), bid0 = blockIdx.x;
    {
        const char* pc; unsigned e0, e1;
        asm volatile("s_getpc_b64 %0\n.Lk_pc:\n\ts_mov_b32 %1, .Lk_end0-.Lk_pc\n\ts_mov_b32 %2, .Lk_end1-.Lk_pc" : "=s"(pc), "=s"(e0), "=s"(e1));
        const unsigned size = e0 > e1 ? e0 : e1, off = (unsigned)((bid0 >> 3) * 16 + (lane0 & 15)) * 128u;
        asm volatile("s_cmp_lg_u32 %2, 0\n\ts_cbranch_scc1 .Lsk_%=\n\tglobal_load_dword v191, %0, %1\n.Lsk_%=:" :: "v"(off < size ? off : size), "s"(pc), "s"(wid0) : "memory", "scc", "v191");
    }
    asm volatile("" : "+s"(wqkv), "+s"(wg), "+s"(wu), "+s"(wo), "+s"(wd), "+s"(ws), "+s"(x), "+s"(out));
    {
        const int xcd0 = bid0 & 7, idx0 = bid0 >> 3, ct0 = 4 * xcd0 + (idx0 & 3), rt0 = idx0 >> 2;
        const int r8 = lane0 & 7, c8 = lane0 >> 3, row = 256 * rt0 + 8 * ct0 + r8;
        u32x4 xv[2];
        unsigned ep;
        asm volatile("global_load_dwordx4 %0, %3, %4\n\tglobal_load_dwordx4 %1, %3, %4 offset:128\n\ts_load_dword %2, %5, 0x0"
                     : "=&v"(xv[0]), "=&v"(xv[1]), "=&s"(ep) : "v"((unsigned)(row * 2048 + (16 * wid0 + c8) * 16)), "s"(x), "s"(ws + WS_EP) : "memory");
        {
            const int j = lane0 < 60 ? lane0 / 20 : 2, i = lane0 < 60 ? lane0 - 20 * j : 19;
            const char *bq = wqkv + (size_t)idx0 * 6144 + xcd0 * 256, *bg = wg + (size_t)idx0 * 7168 + xcd0 * 896 - 6 * 128, *bu = wu + (size_t)idx0 * 7168 + xcd0 * 896 - 13 * 128;
            OPAQUE_S(bq); OPAQUE_S(bg); OPAQUE_S(bu);
            L.ts = i < 6 ? 96u * 6144u : 96u * 7168u;
            L.ta = (i < 6 ? bq : i < 13 ? bg : bu) + (i < 6 ? j * 32 * 6144 + (i >> 1) * 2048 + (i & 1) * 128 : j * 32 * 7168 + i * 128);
            L.m7 = s_if_lt<7, 0, 4>(wid0);
        }
        {
            const u32x4 ones = {0xffffffffu, 0xffffffffu, 0xffffffffu, 0xffffffffu};
            asm volatile("global_store_dwordx4 %0, %1, %2 sc1\n\tglobal_store_dwordx4 %0, %1, %2 offset:1024 sc1\n\ts_nop 1"
                         :: "v"((unsigned)lane0 * 16u), "v"(ones), "s"(ws + WS_HO + (size_t)(64 * (ct0 >> 4) + (ct0 & 15)) * SLAB_B + (size_t)(16 * rt0 + 2 * wid0) * 1024) : "memory");
        }
        asm volatile("s_waitcnt vmcnt(2) lgkmcnt(0)" : "+v"(xv[0]), "+v"(xv[1]), "+s"(ep) :: "memory");
        g1_touch(L.ta, L.ts / 3 * 2, 0, L.m7);
#pragma unroll
        for (int i = 0; i < T_BURST; ++i) g1_touch(L.ta, L.ts, 0, L.m7);
        T.tag = __builtin_amdgcn_readfirstlane(ep * 1664525u + 1013904223u);
        OPAQUE_S(T.tag);
        LAS float* red = (LAS float*)(lds + G1_XOFF);
        f32x2 f[8];
#pragma unroll
        for (int j = 0; j < 8; ++j) f[j] = (f32x2){bf_lo(xv[j >> 2][j & 3]), bf_hi(xv[j >> 2][j & 3])};
        const f32x2 s2 = ((f[0] + f[1]) + (f[2] + f[3])) + ((f[4] + f[5]) + (f[6] + f[7]));
        const float mw = sum_x8(s2[0] + s2[1]) * (1.0f / 128);
        f32x2 q2 = {0.f, 0.f};
#pragma unroll
        for (int j = 0; j < 8; ++j) { const f32x2 d = f[j] - mw; q2 = __builtin_elementwise_fma(d, d, q2); }
        const float qw = sum_x8(q2[0] + q2[1]);
        if (c8 < 2) red[r8 * 16 + c8 * 8 + wid0] = c8 ? qw : mw;
        WAIT_LGKM0(); MEMFENCE(); BAR(); MEMFENCE();
        const f32x4 m0 = *(LAS const f32x4*)(red + r8 * 16), m1 = *(LAS const f32x4*)(red + r8 * 16 + 4), q0 = *(LAS const f32x4*)(red + r8 * 16 + 8), q1 = *(LAS const f32x4*)(red + r8 * 16 + 12);
        const f32x4 ms = m0 + m1;
        const float mean = ((ms[0] + ms[1]) + (ms[2] + ms[3])) * 0.125f;
        const f32x4 e0 = m0 - mean, e1 = m1 - mean, tt = (e0 * e0 + e1 * e1) * 128.f + (q0 + q1);
        const float qt = (tt[0] + tt[1]) + (tt[2] + tt[3]);
        const float mb = rbf(mean);
        const float vb = rbf(qt * (1.0f / D_));
        const float den = rbf(__builtin_amdgcn_sqrtf(rbf(vb + 1e-5f)));
        const float rc = __builtin_amdgcn_rcpf(den);
#pragma unroll
        for (int i = 0; i < 2; ++i) {
            u32x4 o;
#pragma unroll
            for (int j = 0; j < 4; ++j) {
                const f32x2 c = f[i * 4 + j] - mb;
                unsigned d = pk_bf16(c[0], c[1]);
                asm("" : "+v"(d));
                const f32x2 y = (f32x2){bf_lo(d), bf_hi(d)} * rc;
                o[j] = pk_bf16(y[0], y[1]);
            }
            const int ch = 16 * wid0 + 8 * i + c8;
            asm volatile("global_store_dwordx4 %0, %1, %2 sc1\n\ts_nop 1" :: "v"((unsigned)((ch >> 2) * (int)SLAB_B + (row >> 4) * 1024 + ((ch & 3) * 16 + (row & 15)) * 16)), "v"(o), "s"(ws + WS_XN) : "memory");
        }
    }
    int wid = wid0, bid = bid0;
    OPAQUE_S(wid); OPAQUE_S(bid);
    const int lane = fresh_lane();
    const int xcd = bid & 7, idx = bid >> 3, ct = 4 * xcd + (idx & 3), rt = idx >> 2;
    const unsigned ldsb = (unsigned)(size_t)lds;
    {
        const int c = wid * 64 + lane, rho = c / 14, cc = c - rho * 14;
        const int k = (rho & ~12) | ((rho & 4) << 1) | ((rho & 8) >> 1);
        const int c2 = cc < 12 ? cc : 11;
        L.pa = ws + WS_XN + (size_t)(16 * rt + 2 * wid) * 1024;
        L.va = lane * 16;
        L.la = ldsb + (1 + 2 * wid) * 1024;
        L.px = ws + WS_XN + (size_t)(wid < 2 ? 16 * rt + 14 + wid : rt == 0 ? 0 : 16 * rt - 1) * 1024;
        L.vx = (unsigned)((wid >= 2 && rt == 0 ? (lane & 48) : lane) * 16);
        L.lx = wid < 2 ? ldsb + (15 + wid) * 1024 : ldsb;
        L.p2 = wqkv + ct * 64;
        L.v2 = (unsigned)(k * 6144 + (c2 >> 2) * 2048 + (c2 & 3) * 16);
        L.l2 = ldsb + G1_BOFF + wid * 1024;
        L.pg = wg + ct * 224;
        L.pu = wu + ct * 224;
        L.vg = (unsigned)(k * 7168 + cc * 16);
        L.ma = s_if_lt<7, -1, 0>(wid); L.mb = L.ma; L.mx = s_if_lt<3, -1, 1>(wid); L.mt = s_if_lt<7, 0, 10 - T_BURST>(wid);
        const char *bo = wo + bid * 8192 - 4 * 1024, *bd = wd + bid * 28672 - 8 * 128 - 4 * 3584;
        OPAQUE_S(bo); OPAQUE_S(bd);
        L.tb = (lane < 8 ? bo : bd) + (lane < 36 ? lane : 35) * 128;
        L.tt = lane < 8 ? 1024u : 3584u;
        L.pf = ws + WS_XB + (size_t)bid * 16384 + (wid & 3) * 4096 - FILL_IT0 * 1024;
    }
    const int kh = (bid >> 2) & 1, side = kh ^ 1, ct2 = 4 * (bid & 1) + (idx & 3);
    const int kg = wid >> 2, wm = wid & 3;
    T.W.pwo = wo + (size_t)(side * 512) * 2048 + ct2 * 256;
    T.W.pwh = wd + (size_t)(side * 1792 + kg * 80) * 2048 + ct2 * 256;
    T.W.pwd = wd + (size_t)(96 + (wid >> 1) * 96) * 2048 + ct2 * 256;
    T.W.ldsw = ldsb + wid * 1024;
    const unsigned fo = (unsigned)((rt * 32 + lane) * 256 + (lane < 32 ? ct : ct + 2) * 4);
    const int fm = s_if_lt<7, -1, 0>(rt) & s_if_lt<30, 0, -1>(ct);
    asm volatile("" :: "v"(L.v2), "v"(L.vg), "v"(L.vx), "v"(L.tb), "v"(L.tt), "v"(fo), "s"(L.pa), "s"(L.px), "s"(L.pf), "s"(T.W.pwo), "s"(T.W.pwh), "s"(T.W.pwd));
    asm volatile("s_waitcnt vmcnt(0)\n\ts_barrier" ::: "memory");
    asm volatile("s_cmp_lg_u32 %4, 7\n\ts_cbranch_scc1 .Lsk_%=\n\ts_mov_b32 exec_hi, %3\n\tglobal_store_dword %0, %1, %2 sc1\n\ts_mov_b64 exec, -1\n.Lsk_%=:"
                 :: "v"(fo), "v"(T.tag), "s"(ws + WS_LF), "s"(fm), "s"(wid) : "memory", "scc");
    g1_p2<0>(L); g1_pg<0>(L); g1_pu<0>(L); g1_p2<1>(L); g1_pg<1>(L); g1_pu<1>(L);
    if (wid < 4) {
        g1_body<0>(L, T, lds, ws, rt, ct, wm, kh); g2_body<0>(T, lds, ws, x, out, rt, ct, wm);
        asm volatile(".Lk_end0:");
        __builtin_amdgcn_endpgm();
    }
    g1_body<1>(L, T, lds, ws, rt, ct, wm, kh); g2_body<1>(T, lds, ws, x, out, rt, ct, wm);
    asm volatile(".Lk_end1:");
    __builtin_amdgcn_endpgm();
}

extern "C" void kernel_launch(void* const* d_in, const int* in_sizes, int n_in,
                              void* d_out, int out_size, void* d_ws, size_t ws_size,
                              hipStream_t stream) {
    const u16* x = (const u16*)d_in[0];
    const char* wqkv = (const char*)d_in[1];
    const char* wo = (const char*)d_in[2];
    const char* wg = (const char*)d_in[4];
    const char* wu = (const char*)d_in[5];
    const char* wd = (const char*)d_in[6];
    char* ws = (char*)d_ws;
    (void)hipFuncSetAttribute((const void*)k_main, hipFuncAttributeMaxDynamicSharedMemorySize, G1_LDS);
    k_main<<<256, 512, G1_LDS, stream>>>(wqkv, wg, wu, wo, wd, ws, x, (u16*)d_out);
}
```
